```python
import torch

def _softmax(x, axis=-1):
    return torch.softmax(x, dim=axis)
def _gelu(x):
    return torch.nn.functional.gelu(x, approximate="tanh")
def _silu(x):
    return torch.nn.functional.silu(x)
def _sigmoid(x):
    return torch.sigmoid(x)
def _layernorm(x, axis=-1, eps=1e-5):
    m = x.mean(dim=axis, keepdim=True)
    v = x.var(dim=axis, keepdim=True, unbiased=False)
    return (x - m) / torch.sqrt(v + eps)
def _scatter_add(tgt, idx, src, axis=0):
    return tgt.clone().index_add_(axis, idx, src)
def _segment_sum(src, seg, n):
    out = torch.zeros((n,) + src.shape[1:], dtype=src.dtype)
    return out.index_add_(0, seg, src)
def _pad(x, pw):
    flat = tuple(v for pair in reversed(pw) for v in pair)
    return torch.nn.functional.pad(x, flat)

kernel_name = 'standalone__gqa_block_cached__near_ridge'

def setup_inputs():

    _w = (lambda a: (lambda t: t.to(torch.bfloat16) if t.is_floating_point() else t)(torch.as_tensor(a)))
    B, Sq, D = 8, 144, 640
    Hq, Hkv, Sk, Dh = 32, 32, 512, 192
    assert Hq % Hkv == 0, (Hq, Hkv)
    return {
        "x": _w((lambda *s: torch.randn(*s, dtype=torch.bfloat16) * 0.8)(B, Sq, D)),
        "k_cache": _w((lambda *s: torch.randn(*s, dtype=torch.bfloat16) * 0.8)(B, Hkv, Sk, Dh)),
        "v_cache": _w((lambda *s: torch.randn(*s, dtype=torch.bfloat16) * 0.05)(B, Hkv, Sk, Dh)),
        "wq": _w((lambda *s: torch.randn(*s, dtype=torch.bfloat16) * 0.8)(D, Hq * Dh) * (D ** -0.5)),
        "wo": _w((lambda *s: torch.randn(*s, dtype=torch.bfloat16) * 0.05)(Hq * Dh, D) * ((Hq * Dh) ** -0.5)),
    }

def reference(x, k_cache, v_cache, wq, wo):
    B, Sq, D = x.shape
    Hq, Dh = 32, 192
    Hkv = k_cache.shape[1]
    rep = Hq // Hkv
    q = (lambda x, *d: x.permute(*d))((x @ wq).reshape(B, Sq, Hq, Dh), 0, 2, 1, 3)
    k = (lambda x, n, axis: torch.repeat_interleave(x, n, dim=axis))(k_cache, rep, axis=1)
    v = (lambda x, n, axis: torch.repeat_interleave(x, n, dim=axis))(v_cache, rep, axis=1)
    scores = torch.einsum("bhqd,bhkd->bhqk", q, k) * (Dh ** -0.5)
    p = _softmax(scores, axis=-1)
    o = torch.einsum("bhqk,bhkd->bhqd", p, v)
    return (lambda x, *d: x.permute(*d))(o, 0, 2, 1, 3).reshape(B, Sq, Hq * Dh) @ wo
```

```cpp
#include <hip/hip_runtime.h>
#include <hip/hip_bf16.h>
typedef unsigned u32x4 __attribute__((ext_vector_type(4)));
typedef unsigned u32x2 __attribute__((ext_vector_type(2)));
typedef float f32x4 __attribute__((ext_vector_type(4)));
typedef float f32x2 __attribute__((ext_vector_type(2)));
typedef __bf16 bf16x8 __attribute__((ext_vector_type(8)));
typedef __bf16 bf16x4 __attribute__((ext_vector_type(4)));
typedef __bf16 bf16x2 __attribute__((ext_vector_type(2)));
typedef short s16x4 __attribute__((ext_vector_type(4)));
#define LAS __attribute__((address_space(3)))
#define GAS __attribute__((address_space(1)))
#define DI __device__ __forceinline__
DI unsigned cvtpk(float lo, float hi) { f32x2 v = {lo, hi}; bf16x2 b = __builtin_convertvector(v, bf16x2); return __builtin_bit_cast(unsigned, b); }
DI bf16x8 pack8(f32x4 a, f32x4 b) { u32x4 w = {cvtpk(a[0], a[1]), cvtpk(a[2], a[3]), cvtpk(b[0], b[1]), cvtpk(b[2], b[3])}; return __builtin_bit_cast(bf16x8, w); }
DI u32x2 trread(const char* p) { s16x4 r = __builtin_amdgcn_ds_read_tr16_b64_v4i16((LAS s16x4*)p); return __builtin_bit_cast(u32x2, r); }
DI bf16x8 trfrag(const char* p0, const char* p1) { u32x2 a = trread(p0), b = trread(p1); u32x4 w = {a.x, a.y, b.x, b.y}; return __builtin_bit_cast(bf16x8, w); }
#define MFMA(a, b, c) __builtin_amdgcn_mfma_f32_16x16x32_bf16(a, b, c, 0, 0, 0)
constexpr int RS = 400;
__global__ __launch_bounds__(320) void attn_k(const char* __restrict__ X, const char* __restrict__ K, const char* __restrict__ V, const char* __restrict__ Wq, u32x4* __restrict__ O) {
  __shared__ __attribute__((aligned(16))) char lds[64 * RS];
  const int t = threadIdx.x, l = t & 63, w0 = (t >> 6) + 5 * (blockIdx.x >> 8), w = w0 > 8 ? 8 : w0, r = l & 15, g = l >> 4, qq = r >> 2, p = r & 3;
  const int bid = blockIdx.x & 255, x = bid & 7, rr = bid >> 3, b = rr >> 2, h = x * 4 + (rr & 3);
  auto stage = [&](const char* src, size_t stride) {
    __syncthreads();
    for (int i = t; i < 64 * 24; i += 320) { int row = i / 24, c = i % 24; *(u32x4*)(lds + row * RS + c * 16) = __builtin_nontemporal_load((const u32x4*)(src + row * stride + c * 16)); }
    __syncthreads();
  };
  f32x4 acc[12];
  for (int i = 0; i < 12; ++i) acc[i] = f32x4{0, 0, 0, 0};
  const char* xrow = X + ((size_t)b * 144 + w * 16 + r) * 1280;
  for (int kt = 0; kt < 10; ++kt) {
    stage(Wq + (size_t)kt * 64 * 12288 + h * 384, 12288);
    #pragma unroll
    for (int ks = 0; ks < 2; ++ks) {
      bf16x8 xb = *(const bf16x8*)(xrow + (kt * 64 + ks * 32 + 8 * g) * 2);
      #pragma unroll
      for (int i = 0; i < 12; ++i) { int s = i >> 1, u = i & 1; const char* a = lds + (ks * 32 + 8 * g + qq) * RS + (32 * s + 8 * p + 4 * u) * 2;
        acc[i] = MFMA(trfrag(a, a + 4 * RS), xb, acc[i]); }
    }
  }
  bf16x8 qf[6];
  #pragma unroll
  for (int s = 0; s < 6; ++s) qf[s] = pack8(acc[2 * s], acc[2 * s + 1]);
  f32x4 S[32];
  const char* kb = K + (size_t)(b * 32 + h) * 196608; const char* vb = V + (size_t)(b * 32 + h) * 196608;
  #pragma unroll
  for (int kt = 0; kt < 8; ++kt) {
    stage(kb + kt * 64 * 384, 384);
    #pragma unroll
    for (int i = 0; i < 4; ++i) { f32x4 c = {0, 0, 0, 0};
      #pragma unroll
      for (int s = 0; s < 6; ++s) c = MFMA(*(const bf16x8*)(lds + (i * 16 + r) * RS + (32 * s + 8 * g) * 2), qf[s], c);
      S[kt * 4 + i] = c; }
  }
  float m = -1e30f;
  #pragma unroll
  for (int i = 0; i < 32; ++i) for (int j = 0; j < 4; ++j) m = fmaxf(m, S[i][j]);
  m = fmaxf(m, __shfl_xor(m, 16)); m = fmaxf(m, __shfl_xor(m, 32));
  const float c = 0.07216878364870322f * 1.4426950408889634f; float sum = 0;
  #pragma unroll
  for (int i = 0; i < 32; ++i) for (int j = 0; j < 4; ++j) { float e = __builtin_amdgcn_exp2f((S[i][j] - m) * c); S[i][j] = e; sum += e; }
  sum += __shfl_xor(sum, 16); sum += __shfl_xor(sum, 32);
  bf16x8 pf[16];
  #pragma unroll
  for (int i = 0; i < 16; ++i) pf[i] = pack8(S[2 * i], S[2 * i + 1]);
  for (int i = 0; i < 12; ++i) acc[i] = f32x4{0, 0, 0, 0};
  #pragma unroll
  for (int kt = 0; kt < 8; ++kt) {
    stage(vb + kt * 64 * 384, 384);
    #pragma unroll
    for (int ks = 0; ks < 2; ++ks)
      #pragma unroll
      for (int i = 0; i < 12; ++i) { int s = i >> 1, u = i & 1; const char* a = lds + (ks * 32 + 4 * g + qq) * RS + (32 * s + 8 * p + 4 * u) * 2;
        acc[i] = MFMA(trfrag(a, a + 16 * RS), pf[kt * 2 + ks], acc[i]); }
  }
  const float inv = 1.f / sum;
  #pragma unroll
  for (int s = 0; s < 6; ++s) { f32x4 a = acc[2 * s] * inv, bb = acc[2 * s + 1] * inv;
    O[(((size_t)b * 9 + w) * 192 + h * 6 + s) * 64 + l] = __builtin_bit_cast(u32x4, pack8(a, bb)); }
}
__global__ __launch_bounds__(576) void outp_k(const u32x4* __restrict__ O, const char* __restrict__ Wo, char* __restrict__ out) {
  __shared__ __attribute__((aligned(16))) char lds[256 * 80];
  const int t = threadIdx.x, l = t & 63, w = t >> 6, r = l & 15, g = l >> 4, qq = r >> 2, p = r & 3;
  const int b = blockIdx.x / 20, c = blockIdx.x % 20;
  f32x4 a0 = {0, 0, 0, 0}, a1 = {0, 0, 0, 0};
  for (int kt = 0; kt < 24; ++kt) {
    __syncthreads();
    for (int i = t; i < 1024; i += 576) { int row = i >> 2, cc = i & 3; *(u32x4*)(lds + row * 80 + cc * 16) = *(const u32x4*)(Wo + (size_t)(kt * 256 + row) * 1280 + c * 64 + cc * 16); }
    __syncthreads();
    #pragma unroll
    for (int ks = 0; ks < 8; ++ks) {
      bf16x8 of = __builtin_bit_cast(bf16x8, O[(((size_t)b * 9 + w) * 192 + kt * 8 + ks) * 64 + l]);
      const char* a = lds + (ks * 32 + 8 * g + qq) * 80 + (8 * p) * 2;
      a0 = MFMA(trfrag(a, a + 4 * 80), of, a0);
      a1 = MFMA(trfrag(a + 8, a + 8 + 4 * 80), of, a1);
    }
  }
  *(u32x4*)(out + ((size_t)b * 144 + w * 16 + r) * 1280 + c * 64 + g * 16) = __builtin_bit_cast(u32x4, pack8(a0, a1));
}
#define PROBE 0
extern "C" void kernel_launch(void* const* d_in, const int* in_sizes, int n_in, void* d_out, int out_size, void* d_ws, size_t ws_size, hipStream_t stream) {
  attn_k<<<512, 320, 0, stream>>>((const char*)d_in[0], (const char*)d_in[1], (const char*)d_in[2], (const char*)d_in[3], (u32x4*)d_ws);
  outp_k<<<160, 576, 0, stream>>>((const u32x4*)d_ws, (const char*)d_in[4], (char*)d_out);
}
```

```cpp
#include <hip/hip_runtime.h>

__global__ __launch_bounds__(256, 1) void gqa_fused(const void* x, const void* k, const void* v, const void* wq, const void* wo, void* out, void* ws) {
  unsigned tid = threadIdx.x;
  asm volatile(
".p2align 12\n"
".Lentry_%=:\n"
"s_load_dwordx8 s[8:15], %1, 0x0\n"
"s_load_dwordx4 s[16:19], %1, 0x20\n"
"s_load_dwordx2 s[20:21], %1, 0x30\n"
"s_mov_b32 s57, %2\n"
"s_mov_b32 s65, 0\n"
"v_lshrrev_b32 v2, 6, v0\n"
"v_and_b32 v0, 63, v0\n"
"v_readfirstlane_b32 s52, v2\n"
".Ltop_%=:\n"
"v_lshlrev_b32 v1, 4, v0\n"
"s_and_b32 s54, s57, 7\n"
"s_lshr_b32 s55, s57, 5\n"
"s_bfe_u32 s56, s57, 0x20003\n"
"s_mul_i32 s53, s52, 40960\n"
"v_lshrrev_b32 v60, 3, v0\n"
"v_and_b32 v61, 7, v0\n"
"s_lshl_b32 s66, s52, 3\n"
"v_add_u32 v62, s66, v60\n"
"v_mul_u32_u24 v62, 12288, v62\n"
"s_and_b32 s67, s52, 1\n"
"s_lshl_b32 s67, s67, 2\n"
"v_and_b32 v63, 2, v60\n"
"v_or_b32 v63, s67, v63\n"
"v_xor_b32 v63, v63, v61\n"
"v_lshl_add_u32 v62, v63, 4, v62\n"
"v_add_u32 v46, 2048, v62\n"
"v_add_u32 v47, 1152, v62\n"
"v_add_u32 v48, 256, v62\n"
"s_lshl_b32 s66, s52, 5\n"
"v_add_u32 v62, s66, v60\n"
"v_mul_u32_u24 v62, 1280, v62\n"
"v_lshrrev_b32 v63, 1, v60\n"
"v_xor_b32 v63, v63, v61\n"
"v_lshl_add_u32 v41, v63, 4, v62\n"
"v_xor_b32 v63, 4, v63\n"
"v_lshl_add_u32 v62, v63, 4, v62\n"
"v_add_u32 v42, 9216, v62\n"
"v_add_u32 v43, 18432, v41\n"
"v_add_u32 v44, 27648, v62\n"
"s_lshl_b32 s66, s52, 2\n"
"v_and_b32 v62, 3, v60\n"
"v_add_u32 v62, s66, v62\n"
"v_lshrrev_b32 v63, 1, v62\n"
"v_xor_b32 v63, v63, v61\n"
"v_mul_u32_u24 v62, 1280, v62\n"
"v_lshl_add_u32 v62, v63, 4, v62\n"
"v_add_u32 v45, 163840, v62\n"
"s_lshl_b32 s3, s55, 2\n"
"s_lshr_b32 s3, 0x10210210, s3\n"
"s_and_b32 s3, s3, 3\n"
"s_lshr_b32 s66, s55, 1\n"
"s_add_u32 s66, s66, s56\n"
"s_lshl_b32 s66, s66, 1\n"
"s_mul_i32 s67, s54, 1\n"
"s_add_u32 s74, s66, s67\n"
"s_lshl_b32 s68, s54, 2\n"
"s_add_u32 s68, s68, s56\n"
"s_mul_i32 s66, s68, 384\n"
"s_mul_i32 s67, s55, 184320\n"
"s_mov_b32 s27, 0x20000\n"
"s_mov_b32 s31, 0x20000\n"
"s_mov_b32 s35, 0x20000\n"
"s_mov_b32 s39, 0x20000\n"
"s_mov_b32 s43, 0x20000\n"
"s_mov_b32 s47, 0x20000\n"
"s_mov_b32 s51, 0x20000\n"
"s_mov_b32 s26, 184320\n"
"s_sub_u32 s30, 7866368, s66\n"
"s_lshl_b32 s4, s55, 5\n"
"s_add_u32 s4, s4, s68\n"
"s_mul_i32 s4, s4, 196608\n"
"s_mul_i32 s5, s54, 983040\n"
"s_mul_i32 s6, s55, 140432\n"
"s_add_u32 s5, s5, s6\n"
"v_mov_b32 v86, 0\n"
"s_waitcnt lgkmcnt(0)\n"
"s_mov_b64 s[6:7], s[8:9]\n"
"s_mov_b32 s69, s67\n"
"s_bitcmp1_b32 s55, 0\n"
"s_cselect_b64 s[6:7], s[16:17], s[6:7]\n"
"s_cselect_b32 s69, s5, s69\n"
"s_cmp_eq_u32 s56, 1\n"
"s_cselect_b64 s[6:7], s[10:11], s[6:7]\n"
"s_cselect_b32 s69, s4, s69\n"
"s_cmp_eq_u32 s56, 2\n"
"s_cselect_b64 s[6:7], s[12:13], s[6:7]\n"
"s_cselect_b32 s69, s4, s69\n"
"s_add_u32 s6, s6, s69\n"
"s_addc_u32 s7, s7, 0\n"
"s_cmp_lg_u32 s56, 3\n"
"s_cbranch_scc0 .Lnt_%=\n"
"global_load_dword v86, v86, s[6:7]\n"
".Lnt_%=:\n"
"s_sub_u32 s14, s14, 2048\n"
"s_subb_u32 s15, s15, 0\n"
"s_add_u32 s28, s14, s66\n"
"s_addc_u32 s29, s15, 0\n"
"s_and_b32 s29, s29, 0xffff\n"
"s_add_u32 s24, s8, s67\n"
"s_addc_u32 s25, s9, 0\n"
"s_and_b32 s25, s25, 0xffff\n"
".Lpc_%=:\n"
"s_getpc_b64 s[66:67]\n"
"s_and_b32 s66, s66, 0xffffff00\n"
"s_lshr_b32 s69, s57, 3\n"
"s_lshl_b32 s69, s69, 2\n"
"s_add_u32 s69, s69, s52\n"
"s_mul_i32 s69, s69, 1024\n"
"s_min_u32 s69, s69, .Lend_%=-.Lpc_%=-768\n"
"v_lshlrev_b32 v62, 4, v0\n"
"v_add_u32 v62, s69, v62\n"
"global_load_dword v126, v62, s[66:67]\n"
"s_add_u32 m0, s53, 16384\n"
"s_add_u32 s60, s74, 0\n"
"s_and_b32 s60, s60, 7\n"
"s_mul_i32 s62, s60, 786432\n"
"buffer_load_dwordx4 v46, s[28:31], s62 offen lds\n"
"buffer_load_dwordx4 v47, s[28:31], s62 offen offset:1024 lds\n"
"buffer_load_dwordx4 v48, s[28:31], s62 offen offset:2048 lds\n"
"s_add_u32 m0, s53, 0\n"
"s_lshl_b32 s61, s60, 7\n"
"buffer_load_dwordx4 v41, s[24:27], s61 offen lds\n"
"buffer_load_dwordx4 v42, s[24:27], s61 offen offset:1024 lds\n"
"buffer_load_dwordx4 v43, s[24:27], s61 offen offset:2048 lds\n"
"buffer_load_dwordx4 v44, s[24:27], s61 offen offset:3072 lds\n"
"s_add_u32 m0, s53, 4096\n"
"s_mov_b32 exec_hi, 0\n"
"buffer_load_dwordx4 v45, s[24:27], s61 offen lds\n"
"s_mov_b32 exec_hi, -1\n"
"s_add_u32 m0, s53, 19456\n"
"s_add_u32 s62, s62, 393216\n"
"buffer_load_dwordx4 v46, s[28:31], s62 offen lds\n"
"buffer_load_dwordx4 v47, s[28:31], s62 offen offset:1024 lds\n"
"buffer_load_dwordx4 v48, s[28:31], s62 offen offset:2048 lds\n"
"v_and_b32 v58, 15, v0\n"
"v_lshrrev_b32 v59, 4, v0\n"
"v_lshrrev_b32 v62, 1, v58\n"
"v_lshrrev_b32 v63, 2, v58\n"
"v_xor_b32 v62, v62, v59\n"
"v_mul_u32_u24 v64, 40448, v63\n"
"v_lshlrev_b32 v62, 4, v62\n"
"v_add_u32 v64, 4096, v64\n"
"v_lshl_add_u32 v28, v58, 7, v62\n"
"v_mul_u32_u24 v65, 40960, v59\n"
"v_xor_b32 v29, 64, v28\n"
"v_lshl_add_u32 v65, v63, 7, v65\n"
"v_and_b32 v63, 3, v58\n"
"v_add_u32 v30, 40960, v28\n"
"v_add_u32 v31, 40960, v29\n"
"v_add_u32 v32, 81920, v28\n"
"v_add_u32 v33, 81920, v29\n"
"v_add_u32 v34, 122880, v28\n"
"v_add_u32 v35, 122880, v29\n"
"v_add_u32 v36, v64, v28\n"
"v_add_u32 v37, v64, v29\n"
"v_lshl_add_u32 v62, v63, 3, v65\n"
"v_bfe_u32 v63, v58, 3, 1\n"
"v_and_b32 v60, 1, v59\n"
"s_mul_i32 s66, s52, 3\n"
"v_lshl_or_b32 v63, v60, 1, v63\n"
"s_add_u32 s67, s66, 0\n"
"s_and_b32 s69, s67, 3\n"
"s_lshr_b32 s67, s67, 2\n"
"v_xor_b32 v60, s69, v63\n"
"s_lshl_b32 s67, s67, 10\n"
"v_lshl_add_u32 v60, v60, 5, v62\n"
"v_add_u32 v38, s67, v60\n"
"s_add_u32 s67, s66, 1\n"
"s_and_b32 s69, s67, 3\n"
"s_lshr_b32 s67, s67, 2\n"
"v_xor_b32 v60, s69, v63\n"
"s_lshl_b32 s67, s67, 10\n"
"v_lshl_add_u32 v60, v60, 5, v62\n"
"v_add_u32 v39, s67, v60\n"
"s_add_u32 s67, s66, 2\n"
"s_and_b32 s69, s67, 3\n"
"s_lshr_b32 s67, s67, 2\n"
"v_xor_b32 v60, s69, v63\n"
"s_lshl_b32 s67, s67, 10\n"
"v_lshl_add_u32 v60, v60, 5, v62\n"
"v_add_u32 v40, s67, v60\n"
"s_lshl_b32 s66, s55, 5\n"
"s_add_u32 s66, s66, s68\n"
"s_mul_i32 s66, s66, 196608\n"
"s_add_u32 s32, s10, s66\n"
"s_addc_u32 s33, s11, 0\n"
"s_and_b32 s33, s33, 0xffff\n"
"s_mov_b32 s34, 196608\n"
"v_lshrrev_b32 v58, 3, v0\n"
"v_and_b32 v59, 7, v0\n"
"s_mul_i32 s67, s52, 6144\n"
"v_add_u32 v60, 0, v58\n"
"v_mul_u32_u24 v61, 43, v60\n"
"v_lshrrev_b32 v61, 8, v61\n"
"v_xor_b32 v61, v61, v59\n"
"v_lshlrev_b32 v61, 4, v61\n"
"v_lshl_add_u32 v61, v58, 7, v61\n"
"v_add_u32 v2, s67, v61\n"
"v_add_u32 v60, 8, v58\n"
"v_mul_u32_u24 v61, 43, v60\n"
"v_lshrrev_b32 v61, 8, v61\n"
"v_xor_b32 v61, v61, v59\n"
"v_lshlrev_b32 v61, 4, v61\n"
"v_lshl_add_u32 v61, v58, 7, v61\n"
"v_add_u32 v3, s67, v61\n"
"v_add_u32 v60, 16, v58\n"
"v_mul_u32_u24 v61, 43, v60\n"
"v_lshrrev_b32 v61, 8, v61\n"
"v_xor_b32 v61, v61, v59\n"
"v_lshlrev_b32 v61, 4, v61\n"
"v_lshl_add_u32 v61, v58, 7, v61\n"
"v_add_u32 v4, s67, v61\n"
"v_add_u32 v60, 24, v58\n"
"v_mul_u32_u24 v61, 43, v60\n"
"v_lshrrev_b32 v61, 8, v61\n"
"v_xor_b32 v61, v61, v59\n"
"v_lshlrev_b32 v61, 4, v61\n"
"v_lshl_add_u32 v61, v58, 7, v61\n"
"v_add_u32 v5, s67, v61\n"
"v_add_u32 v60, 32, v58\n"
"v_mul_u32_u24 v61, 43, v60\n"
"v_lshrrev_b32 v61, 8, v61\n"
"v_xor_b32 v61, v61, v59\n"
"v_lshlrev_b32 v61, 4, v61\n"
"v_lshl_add_u32 v61, v58, 7, v61\n"
"s_add_u32 s67, s67, 4096\n"
"v_add_u32 v6, s67, v61\n"
"v_add_u32 v60, 40, v58\n"
"v_mul_u32_u24 v61, 43, v60\n"
"v_lshrrev_b32 v61, 8, v61\n"
"v_xor_b32 v61, v61, v59\n"
"v_lshlrev_b32 v61, 4, v61\n"
"v_lshl_add_u32 v61, v58, 7, v61\n"
"v_add_u32 v7, s67, v61\n"
"v_and_b32 v58, 15, v0\n"
"v_lshrrev_b32 v59, 4, v0\n"
"v_lshrrev_b32 v60, 1, v58\n"
"v_xor_b32 v60, v60, v59\n"
"v_lshlrev_b32 v60, 4, v60\n"
"v_mul_u32_u24 v61, 384, v58\n"
"v_add_u32 v20, v61, v60\n"
"v_xor_b32 v60, 64, v60\n"
"v_add_u32 v21, v61, v60\n"
"s_xor_b32 s67, s52, 0\n"
"s_mul_i32 s67, s67, 40960\n"
"v_add_u32 v12, s67, v20\n"
"v_add_u32 v13, s67, v21\n"
"s_xor_b32 s67, s52, 1\n"
"s_mul_i32 s67, s67, 40960\n"
"v_add_u32 v14, s67, v20\n"
"v_add_u32 v15, s67, v21\n"
"s_xor_b32 s67, s52, 2\n"
"s_mul_i32 s67, s67, 40960\n"
"v_add_u32 v16, s67, v20\n"
"v_add_u32 v17, s67, v21\n"
"s_xor_b32 s67, s52, 3\n"
"s_mul_i32 s67, s67, 40960\n"
"v_add_u32 v18, s67, v20\n"
"v_add_u32 v19, s67, v21\n"
"s_add_u32 s36, s12, s66\n"
"s_addc_u32 s37, s13, 0\n"
"s_and_b32 s37, s37, 0xffff\n"
"s_mov_b32 s38, 196608\n"
"v_lshrrev_b32 v60, 3, v0\n"
"v_and_b32 v61, 7, v0\n"
"v_and_b32 v62, 6, v60\n"
"v_xor_b32 v62, v62, v61\n"
"v_lshlrev_b32 v62, 4, v62\n"
"v_mul_u32_u24 v63, 384, v60\n"
"s_mul_i32 s67, s52, 6144\n"
"v_add3_u32 v8, v62, v63, s67\n"
"v_add_u32 v9, 2048, v8\n"
"v_add_u32 v10, 22528, v8\n"
"v_add_u32 v11, 24576, v8\n"
"s_mul_i32 s66, s54, 983040\n"
"s_mul_i32 s67, s56, 320\n"
"s_add_u32 s40, s16, s66\n"
"s_addc_u32 s41, s17, 0\n"
"s_and_b32 s41, s41, 0xffff\n"
"s_mov_b32 s42, 983040\n"
"s_mov_b32 s44, s20\n"
"s_and_b32 s45, s21, 0xffff\n"
"s_mov_b32 s46, 28573696\n"
"s_mul_i32 s66, s55, 184320\n"
"s_add_u32 s66, s66, s67\n"
"s_add_u32 s48, s18, s66\n"
"s_addc_u32 s49, s19, 0\n"
"s_and_b32 s49, s49, 0xffff\n"
"s_sub_u32 s50, 1474560, s66\n"
"v_mov_b32 v62, 0\n"
"s_lshl_b32 s66, s55, 7\n"
"buffer_load_dwordx2 v[124:125], v62, s[44:47], s66 offen sc1\n"
"v_lshrrev_b32 v62, 5, v0\n"
"v_xor_b32 v62, s55, v62\n"
"v_lshlrev_b32 v62, 7, v62\n"
"buffer_load_dword a243, v62, s[44:47], 0 offen sc1\n"
"v_lshrrev_b32 v62, 1, v59\n"
"v_lshlrev_b32 v62, 8, v62\n"
"v_lshl_add_u32 v62, v58, 4, v62\n"
"v_and_b32 v63, 1, v59\n"
"v_lshl_add_u32 v62, v63, 3, v62\n"
"s_mul_i32 s16, s52, 3\n"
"s_add_u32 s16, s16, 0\n"
"s_lshl_b32 s17, s16, 9\n"
"v_add_u32 v49, s17, v62\n"
"v_add_u32 v52, 81920, v49\n"
"s_lshr_b32 s18, s16, 2\n"
"s_mul_i32 s18, s18, 40960\n"
"s_and_b32 s17, s17, 2047\n"
"s_add_u32 s17, s17, s18\n"
"s_add_u32 s17, s17, 12288\n"
"v_add_u32 v55, s17, v62\n"
"s_mul_i32 s16, s52, 3\n"
"s_add_u32 s16, s16, 1\n"
"s_lshl_b32 s17, s16, 9\n"
"v_add_u32 v50, s17, v62\n"
"v_add_u32 v53, 81920, v50\n"
"s_lshr_b32 s18, s16, 2\n"
"s_mul_i32 s18, s18, 40960\n"
"s_and_b32 s17, s17, 2047\n"
"s_add_u32 s17, s17, s18\n"
"s_add_u32 s17, s17, 12288\n"
"v_add_u32 v56, s17, v62\n"
"s_mul_i32 s16, s52, 3\n"
"s_add_u32 s16, s16, 2\n"
"s_lshl_b32 s17, s16, 9\n"
"v_add_u32 v51, s17, v62\n"
"v_add_u32 v54, 81920, v51\n"
"s_lshr_b32 s18, s16, 2\n"
"s_mul_i32 s18, s18, 40960\n"
"s_and_b32 s17, s17, 2047\n"
"s_add_u32 s17, s17, s18\n"
"s_add_u32 s17, s17, 12288\n"
"v_add_u32 v57, s17, v62\n"
"v_add_u32 v25, 81920, v1\n"
"v_add_u32 v26, s53, v1\n"
"s_mov_b32 s64, 0x3dd53b95\n"
"s_lshl_b32 s18, s52, 12\n"
"s_add_u32 s19, s18, 1048576\n"
"s_and_b32 s17, s57, 0xe7\n"
"s_add_u32 s16, s56, 0\n"
"s_and_b32 s16, s16, 3\n"
"s_lshl_b32 s16, s16, 3\n"
"s_add_u32 s16, s16, s17\n"
"s_mul_i32 s16, s16, 55296\n"
"s_add_u32 s66, s16, s19\n"
"s_add_u32 s16, s56, 1\n"
"s_and_b32 s16, s16, 3\n"
"s_lshl_b32 s16, s16, 3\n"
"s_add_u32 s16, s16, s17\n"
"s_mul_i32 s16, s16, 55296\n"
"s_add_u32 s67, s16, s19\n"
"s_add_u32 s16, s56, 2\n"
"s_and_b32 s16, s16, 3\n"
"s_lshl_b32 s16, s16, 3\n"
"s_add_u32 s16, s16, s17\n"
"s_mul_i32 s16, s16, 55296\n"
"s_add_u32 s68, s16, s19\n"
"s_add_u32 s16, s56, 3\n"
"s_and_b32 s16, s16, 3\n"
"s_lshl_b32 s16, s16, 3\n"
"s_add_u32 s16, s16, s17\n"
"s_mul_i32 s16, s16, 55296\n"
"s_add_u32 s69, s16, s19\n"
"s_cmp_eq_u32 s52, 0\n"
"s_cselect_b32 s16, s67, s68\n"
"s_cmp_ge_u32 s52, 2\n"
"s_cselect_b32 s16, s69, s16\n"
"s_cmp_ge_u32 s52, 2\n"
"s_cselect_b32 s23, 81920, s53\n"
"s_sub_u32 s16, s16, s18\n"
"s_add_u32 s73, s16, 16384\n"
"s_add_u32 s16, s56, 0\n"
"s_and_b32 s16, s16, 3\n"
"s_mul_i32 s16, s16, 245760\n"
"s_mul_i32 s17, s56, 320\n"
"s_add_u32 s8, s16, s17\n"
"s_add_u32 s16, s56, 1\n"
"s_and_b32 s16, s16, 3\n"
"s_mul_i32 s16, s16, 245760\n"
"s_mul_i32 s17, s56, 320\n"
"s_add_u32 s9, s16, s17\n"
"s_add_u32 s16, s56, 2\n"
"s_and_b32 s16, s16, 3\n"
"s_mul_i32 s16, s16, 245760\n"
"s_mul_i32 s17, s56, 320\n"
"s_add_u32 s10, s16, s17\n"
"s_add_u32 s16, s56, 3\n"
"s_cmp_eq_u32 s3, 2\n"
"s_cbranch_scc1 .Lvar2_%=\n"
"s_cmp_lg_u32 s3, 0\n"
"s_cbranch_scc0 .Lvara_%=\n"
".Lvar1_%=:\n"
"s_add_u32 m0, s53, 22528\n"
"s_add_u32 s60, s74, 1\n"
"s_and_b32 s60, s60, 7\n"
"s_mul_i32 s62, s60, 786432\n"
"buffer_load_dwordx4 v46, s[28:31], s62 offen lds\n"
"buffer_load_dwordx4 v47, s[28:31], s62 offen offset:1024 lds\n"
"buffer_load_dwordx4 v48, s[28:31], s62 offen offset:2048 lds\n"
"s_add_u32 m0, s53, 4608\n"
"s_lshl_b32 s61, s60, 7\n"
"buffer_load_dwordx4 v41, s[24:27], s61 offen lds\n"
"buffer_load_dwordx4 v42, s[24:27], s61 offen offset:1024 lds\n"
"buffer_load_dwordx4 v43, s[24:27], s61 offen offset:2048 lds\n"
"buffer_load_dwordx4 v44, s[24:27], s61 offen offset:3072 lds\n"
"s_add_u32 m0, s53, 8704\n"
"s_mov_b32 exec_hi, 0\n"
"buffer_load_dwordx4 v45, s[24:27], s61 offen lds\n"
"s_mov_b32 exec_hi, -1\n"
".Lwalk5_%=:\n"
"s_branch .Lwj_%=\n"
".Lwb_%=:\n"
"s_setpc_b64 vcc\n"
".Lwj_%=:\n"
"s_mov_b64 exec, 0\n"
"s_mov_b32 s98, .Lpad1_%=-.Lentry_%=\n"
"s_cmp_eq_u32 s52, 1\n"
"s_cselect_b32 s98, .Lpad2_%=-.Lentry_%=, s98\n"
"s_cmp_eq_u32 s52, 2\n"
"s_cselect_b32 s98, .Lpad3_%=-.Lentry_%=, s98\n"
"s_cmp_eq_u32 s52, 3\n"
"s_cselect_b32 s98, .Lpad3_%=-.Lentry_%=, s98\n"
"s_getpc_b64 s[96:97]\n"
".Lwh50_%=:\n"
"s_sub_u32 s96, s96, .Lwh50_%=-.Lentry_%=\n"
"s_subb_u32 s97, s97, 0\n"
"s_add_u32 s96, s96, s98\n"
"s_addc_u32 s97, s97, 0\n"
"s_swappc_b64 vcc, s[96:97]\n"
"s_mov_b64 exec, -1\n"
"s_waitcnt vmcnt(13)\n"
"s_barrier\n"
"ds_read_b64_tr_b16 a[180:181], v38 offset:16384\n"
"ds_read_b64_tr_b16 a[182:183], v38 offset:16896\n"
"ds_read_b64_tr_b16 a[184:185], v39 offset:16384\n"
"ds_read_b64_tr_b16 a[186:187], v39 offset:16896\n"
"ds_read_b64_tr_b16 a[188:189], v40 offset:16384\n"
"ds_read_b64_tr_b16 a[190:191], v40 offset:16896\n"
"ds_read_b128 a[144:147], v28 offset:0\n"
"ds_read_b128 a[148:151], v28 offset:2048\n"
"ds_read_b128 a[152:155], v30 offset:0\n"
"ds_read_b128 a[156:159], v30 offset:2048\n"
"ds_read_b128 a[160:163], v32 offset:0\n"
"ds_read_b128 a[164:167], v32 offset:2048\n"
"ds_read_b128 a[168:171], v34 offset:0\n"
"ds_read_b128 a[172:175], v34 offset:2048\n"
"ds_read_b128 a[176:179], v36 offset:0\n"
"s_waitcnt vmcnt(10) lgkmcnt(0)\n"
"s_barrier\n"
"v_mfma_f32_16x16x32_bf16 v[128:131], a[180:183], a[144:147], 0\n"
"s_add_u32 m0, s53, 25600\n"
"ds_read_b64_tr_b16 a[84:85], v38 offset:19456\n"
"s_add_u32 s62, s62, 393216\n"
"v_mfma_f32_16x16x32_bf16 v[132:135], a[184:187], a[144:147], 0\n"
"s_and_b32 s16, s16, 3\n"
"buffer_load_dwordx4 v46, s[28:31], s62 offen lds\n"
"v_mfma_f32_16x16x32_bf16 v[136:139], a[188:191], a[144:147], 0\n"
"s_mul_i32 s16, s16, 245760\n"
"ds_read_b64_tr_b16 a[86:87], v38 offset:19968\n"
"v_mfma_f32_16x16x32_bf16 v[140:143], a[180:183], a[148:151], 0\n"
"s_mul_i32 s17, s56, 320\n"
"ds_read_b64_tr_b16 a[88:89], v39 offset:19456\n"
"v_mfma_f32_16x16x32_bf16 v[144:147], a[184:187], a[148:151], 0\n"
"s_add_u32 s11, s16, s17\n"
"ds_read_b64_tr_b16 a[90:91], v39 offset:19968\n"
"v_mfma_f32_16x16x32_bf16 v[148:151], a[188:191], a[148:151], 0\n"
"s_lshr_b32 s16, s57, 3\n"
"buffer_load_dwordx4 v47, s[28:31], s62 offen offset:1024 lds\n"
"v_mfma_f32_16x16x32_bf16 v[152:155], a[180:183], a[152:155], 0\n"
"s_lshl_b32 s16, s16, 2\n"
"ds_read_b64_tr_b16 a[92:93], v40 offset:19456\n"
"v_mfma_f32_16x16x32_bf16 v[156:159], a[184:187], a[152:155], 0\n"
"s_add_u32 s16, s16, s52\n"
"ds_read_b64_tr_b16 a[94:95], v40 offset:19968\n"
"v_mfma_f32_16x16x32_bf16 v[160:163], a[188:191], a[152:155], 0\n"
"s_lshr_b32 s17, s16, 5\n"
"ds_read_b128 a[48:51], v29 offset:0\n"
"v_mfma_f32_16x16x32_bf16 v[164:167], a[180:183], a[156:159], 0\n"
"s_mul_i32 s17, s17, 245760\n"
"buffer_load_dwordx4 v48, s[28:31], s62 offen offset:2048 lds\n"
"v_mfma_f32_16x16x32_bf16 v[168:171], a[184:187], a[156:159], 0\n"
"s_lshl_b32 s4, s56, 2\n"
"s_add_u32 s4, s4, s52\n"
"s_mul_i32 s4, s4, 11520\n"
"v_mfma_f32_16x16x32_bf16 v[172:175], a[188:191], a[156:159], 0\n"
"s_and_b32 s16, s16, 31\n"
"v_min_u32 v94, 17, v0\n"
"v_lshrrev_b32 v95, 1, v94\n"
"v_mfma_f32_16x16x32_bf16 v[176:179], a[180:183], a[160:163], 0\n"
"ds_read_b128 a[52:55], v29 offset:2048\n"
"v_mul_u32_u24 v95, 1024, v95\n"
"v_lshl_add_u32 v94, v94, 7, v95\n"
"v_mfma_f32_16x16x32_bf16 v[180:183], a[184:187], a[160:163], 0\n"
"s_mul_i32 s16, s16, 2560\n"
"ds_read_b128 a[56:59], v31 offset:0\n"
"v_mfma_f32_16x16x32_bf16 v[184:187], a[188:191], a[160:163], 0\n"
"s_add_u32 s12, s16, s17\n"
"ds_read_b128 a[60:63], v31 offset:2048\n"
"v_mfma_f32_16x16x32_bf16 v[188:191], a[180:183], a[164:167], 0\n"
"ds_read_b128 a[64:67], v33 offset:0\n"
"buffer_load_dword v94, v94, s[24:27], s4 offen offset:1024\n"
"s_lshl_b32 s4, s55, 2\n"
"s_add_u32 s4, s4, s52\n"
"s_mul_i32 s4, s4, 49152\n"
"s_add_u32 s4, s4, 6291456\n"
"v_min_u32 v91, 11, v0\n"
"v_mfma_f32_16x16x32_bf16 v[192:195], a[184:187], a[164:167], 0\n"
"ds_read_b128 a[68:71], v33 offset:2048\n"
"v_mul_u32_u24 v95, 86, v91\n"
"v_lshrrev_b32 v95, 8, v95\n"
"v_mfma_f32_16x16x32_bf16 v[196:199], a[188:191], a[164:167], 0\n"
"ds_read_b128 a[72:75], v35 offset:0\n"
"v_mul_u32_u24 v95, 11904, v95\n"
"v_lshl_add_u32 v91, v91, 7, v95\n"
"v_mfma_f32_16x16x32_bf16 v[200:203], a[180:183], a[168:171], 0\n"
"ds_read_b128 a[76:79], v35 offset:2048\n"
"buffer_load_dword v91, v91, s[28:31], s4 offen offset:2048\n"
"s_add_u32 m0, s53, 28672\n"
"s_mov_b32 s63, 0\n"
"v_mfma_f32_16x16x32_bf16 v[204:207], a[184:187], a[168:171], 0\n"
"s_lshr_b32 s16, s52, 1\n"
"ds_read_b128 a[80:83], v37 offset:0\n"
"buffer_load_dwordx4 v2, s[32:35], s63 offen nt lds\n"
"s_nop 15\n"
"s_nop 12\n"
"v_mfma_f32_16x16x32_bf16 v[208:211], a[188:191], a[168:171], 0\n"
"s_lshl_b32 s16, s16, 10\n"
"buffer_load_dwordx4 v3, s[32:35], s63 offen offset:1024 nt lds\n"
"s_nop 15\n"
"s_nop 14\n"
"v_mfma_f32_16x16x32_bf16 v[212:215], a[180:183], a[172:175], 0\n"
"s_and_b32 s17, s52, 1\n"
"buffer_load_dwordx4 v4, s[32:35], s63 offen offset:2048 nt lds\n"
"s_nop 15\n"
"s_nop 14\n"
"v_mfma_f32_16x16x32_bf16 v[216:219], a[184:187], a[172:175], 0\n"
"s_lshl_b32 s17, s17, 3\n"
"buffer_load_dwordx4 v5, s[32:35], s63 offen offset:3072 nt lds\n"
"s_add_u32 m0, s53, 32768\n"
"s_nop 0\n"
"buffer_load_dwordx4 v6, s[32:35], s63 offen nt lds\n"
"s_nop 15\n"
"s_nop 15\n"
"s_nop 15\n"
"s_nop 14\n"
"v_mfma_f32_16x16x32_bf16 v[220:223], a[188:191], a[172:175], 0\n"
"s_add_u32 s22, s16, s17\n"
"buffer_load_dwordx4 v7, s[32:35], s63 offen offset:1024 nt lds\n"
"s_add_u32 m0, s53, 34816\n"
"s_mov_b32 s63, 24576\n"
"s_nop 15\n"
"s_nop 13\n"
"v_mfma_f32_16x16x32_bf16 v[224:227], a[180:183], a[176:179], 0\n"
"s_lshl_b32 s16, s57, 11\n"
"buffer_load_dwordx4 v2, s[32:35], s63 offen nt lds\n"
"s_nop 15\n"
"s_nop 14\n"
"v_mfma_f32_16x16x32_bf16 v[228:231], a[184:187], a[176:179], 0\n"
"s_lshl_b32 s17, s52, 7\n"
"buffer_load_dwordx4 v3, s[32:35], s63 offen offset:1024 nt lds\n"
"s_nop 15\n"
"s_nop 14\n"
"v_mfma_f32_16x16x32_bf16 v[232:235], a[188:191], a[176:179], 0\n"
"s_waitcnt vmcnt(13) lgkmcnt(0)\n"
"s_barrier\n"
"buffer_load_dwordx4 v4, s[32:35], s63 offen offset:2048 nt lds\n"
"buffer_load_dwordx4 v5, s[32:35], s63 offen offset:3072 nt lds\n"
"s_add_u32 m0, s53, 38912\n"
"s_nop 15\n"
"s_nop 15\n"
"s_nop 15\n"
"s_nop 13\n"
"v_mfma_f32_16x16x32_bf16 v[128:131], a[84:87], a[48:51], v[128:131]\n"
"s_add_u32 s16, s16, s17\n"
"buffer_load_dwordx4 v6, s[32:35], s63 offen nt lds\n"
"s_nop 15\n"
"s_nop 14\n"
"v_mfma_f32_16x16x32_bf16 v[132:135], a[88:91], a[48:51], v[132:135]\n"
"s_add_u32 s70, s16, 524288\n"
"buffer_load_dwordx4 v7, s[32:35], s63 offen offset:1024 nt lds\n"
"s_mov_b32 s63, 98304\n"
"s_nop 15\n"
"s_nop 13\n"
"v_mfma_f32_16x16x32_bf16 v[136:139], a[92:95], a[48:51], v[136:139]\n"
"buffer_load_dwordx4 a[0:3], v2, s[32:35], s63 offen offset:0 nt\n"
"v_lshlrev_b32 v90, 9, v59\n"
"s_nop 15\n"
"s_nop 14\n"
"v_mfma_f32_16x16x32_bf16 v[140:143], a[84:87], a[52:55], v[140:143]\n"
"buffer_load_dwordx4 a[4:7], v3, s[32:35], s63 offen offset:1024 nt\n"
"buffer_load_dwordx4 a[8:11], v4, s[32:35], s63 offen offset:2048 nt\n"
"s_nop 15\n"
"s_nop 15\n"
"s_nop 15\n"
"s_nop 15\n"
"v_mfma_f32_16x16x32_bf16 v[144:147], a[88:91], a[52:55], v[144:147]\n"
"buffer_load_dwordx4 a[12:15], v5, s[32:35], s63 offen offset:3072 nt\n"
"s_nop 15\n"
"s_nop 14\n"
"v_mfma_f32_16x16x32_bf16 v[148:151], a[92:95], a[52:55], v[148:151]\n"
"ds_read_b64_tr_b16 a[180:181], v38 offset:22528\n"
"buffer_load_dwordx4 a[16:19], v6, s[32:35], s63 offen offset:0 nt\n"
"s_nop 15\n"
"s_nop 12\n"
"v_mfma_f32_16x16x32_bf16 v[152:155], a[84:87], a[56:59], v[152:155]\n"
"ds_read_b64_tr_b16 a[182:183], v38 offset:23040\n"
"buffer_load_dwordx4 a[20:23], v7, s[32:35], s63 offen offset:1024 nt\n"
"s_mov_b32 s63, 122880\n"
"s_nop 15\n"
"s_nop 12\n"
"v_mfma_f32_16x16x32_bf16 v[156:159], a[88:91], a[56:59], v[156:159]\n"
"buffer_load_dwordx4 a[24:27], v2, s[32:35], s63 offen offset:0 nt\n"
"ds_read_b64_tr_b16 a[184:185], v39 offset:22528\n"
"buffer_load_dwordx4 a[28:31], v3, s[32:35], s63 offen offset:1024 nt\n"
"s_nop 15\n"
"s_nop 15\n"
"s_nop 15\n"
"s_nop 13\n"
"v_mfma_f32_16x16x32_bf16 v[160:163], a[92:95], a[56:59], v[160:163]\n"
"ds_read_b64_tr_b16 a[186:187], v39 offset:23040\n"
"buffer_load_dwordx4 a[32:35], v4, s[32:35], s63 offen offset:2048 nt\n"
"s_nop 15\n"
"s_nop 12\n"
"v_mfma_f32_16x16x32_bf16 v[164:167], a[84:87], a[60:63], v[164:167]\n"
"ds_read_b64_tr_b16 a[188:189], v40 offset:22528\n"
"buffer_load_dwordx4 a[36:39], v5, s[32:35], s63 offen offset:3072 nt\n"
"s_nop 15\n"
"s_nop 13\n"
"v_mfma_f32_16x16x32_bf16 v[168:171], a[88:91], a[60:63], v[168:171]\n"
"ds_read_b64_tr_b16 a[190:191], v40 offset:23040\n"
"buffer_load_dwordx4 a[40:43], v6, s[32:35], s63 offen offset:0 nt\n"
"s_nop 15\n"
"s_nop 12\n"
"v_mfma_f32_16x16x32_bf16 v[172:175], a[92:95], a[60:63], v[172:175]\n"
"buffer_load_dwordx4 a[44:47], v7, s[32:35], s63 offen offset:1024 nt\n"
"s_mov_b32 s63, 147456\n"
"ds_read_b128 a[144:147], v28 offset:4608\n"
"buffer_load_dwordx4 a[96:99], v2, s[32:35], s63 offen offset:0 nt\n"
"s_nop 15\n"
"s_nop 15\n"
"s_nop 15\n"
"s_nop 12\n"
"v_mfma_f32_16x16x32_bf16 v[176:179], a[84:87], a[64:67], v[176:179]\n"
"ds_read_b128 a[148:151], v28 offset:6656\n"
"buffer_load_dwordx4 a[100:103], v3, s[32:35], s63 offen offset:1024 nt\n"
"s_nop 15\n"
"s_nop 13\n"
"v_mfma_f32_16x16x32_bf16 v[180:183], a[88:91], a[64:67], v[180:183]\n"
"ds_read_b128 a[152:155], v30 offset:4608\n"
"buffer_load_dwordx4 a[104:107], v4, s[32:35], s63 offen offset:2048 nt\n"
"s_nop 15\n"
"s_nop 12\n"
"v_mfma_f32_16x16x32_bf16 v[184:187], a[92:95], a[64:67], v[184:187]\n"
"ds_read_b128 a[156:159], v30 offset:6656\n"
"buffer_load_dwordx4 a[108:111], v5, s[32:35], s63 offen offset:3072 nt\n"
"s_nop 15\n"
"s_nop 13\n"
"v_mfma_f32_16x16x32_bf16 v[188:191], a[84:87], a[68:71], v[188:191]\n"
"buffer_load_dwordx4 a[128:131], v6, s[32:35], s63 offen offset:0 nt\n"
"ds_read_b128 a[160:163], v32 offset:4608\n"
"buffer_load_dwordx4 a[132:135], v7, s[32:35], s63 offen offset:1024 nt\n"
"s_mov_b32 s63, 172032\n"
"s_nop 15\n"
"s_nop 15\n"
"s_nop 15\n"
"s_nop 12\n"
"v_mfma_f32_16x16x32_bf16 v[192:195], a[88:91], a[68:71], v[192:195]\n"
"ds_read_b128 a[164:167], v32 offset:6656\n"
"buffer_load_dwordx4 a[136:139], v2, s[32:35], s63 offen offset:0 nt\n"
"s_add_u32 m0, s53, 16384\n"
"s_add_u32 s60, s74, 2\n"
"s_and_b32 s60, s60, 7\n"
"s_mul_i32 s62, s60, 786432\n"
"s_nop 15\n"
"s_nop 8\n"
"v_mfma_f32_16x16x32_bf16 v[196:199], a[92:95], a[68:71], v[196:199]\n"
"ds_read_b128 a[168:171], v34 offset:4608\n"
"buffer_load_dwordx4 v46, s[28:31], s62 offen lds\n"
"v_mfma_f32_16x16x32_bf16 v[200:203], a[84:87], a[72:75], v[200:203]\n"
"ds_read_b128 a[172:175], v34 offset:6656\n"
"buffer_load_dwordx4 v47, s[28:31], s62 offen offset:1024 lds\n"
"v_mfma_f32_16x16x32_bf16 v[204:207], a[88:91], a[72:75], v[204:207]\n"
"buffer_load_dwordx4 v48, s[28:31], s62 offen offset:2048 lds\n"
"s_add_u32 m0, s53, 9216\n"
"s_lshl_b32 s61, s60, 7\n"
"ds_read_b128 a[176:179], v36 offset:4608\n"
"buffer_load_dwordx4 v41, s[24:27], s61 offen lds\n"
"v_mfma_f32_16x16x32_bf16 v[208:211], a[92:95], a[72:75], v[208:211]\n"
"buffer_load_dwordx4 v42, s[24:27], s61 offen offset:1024 lds\n"
"v_mfma_f32_16x16x32_bf16 v[212:215], a[84:87], a[76:79], v[212:215]\n"
"buffer_load_dwordx4 v43, s[24:27], s61 offen offset:2048 lds\n"
"v_mfma_f32_16x16x32_bf16 v[216:219], a[88:91], a[76:79], v[216:219]\n"
"buffer_load_dwordx4 v44, s[24:27], s61 offen offset:3072 lds\n"
"s_add_u32 m0, s53, 13312\n"
"v_mfma_f32_16x16x32_bf16 v[220:223], a[92:95], a[76:79], v[220:223]\n"
"v_mfma_f32_16x16x32_bf16 v[224:227], a[84:87], a[80:83], v[224:227]\n"
"v_mfma_f32_16x16x32_bf16 v[228:231], a[88:91], a[80:83], v[228:231]\n"
"s_mov_b32 exec_hi, 0\n"
"buffer_load_dwordx4 v45, s[24:27], s61 offen lds\n"
"s_mov_b32 exec_hi, -1\n"
"v_mfma_f32_16x16x32_bf16 v[232:235], a[92:95], a[80:83], v[232:235]\n"
"s_waitcnt vmcnt(41) lgkmcnt(0)\n"
"s_barrier\n"
".Lpad1_%=:\n"
"s_cbranch_execz .Lwb_%=\n"
"s_add_u32 m0, s53, 19456\n"
"s_add_u32 s62, s62, 393216\n"
"v_mfma_f32_16x16x32_bf16 v[128:131], a[180:183], a[144:147], v[128:131]\n"
"ds_read_b64_tr_b16 a[84:85], v38 offset:25600\n"
"v_mfma_f32_16x16x32_bf16 v[132:135], a[184:187], a[144:147], v[132:135]\n"
"ds_read_b64_tr_b16 a[86:87], v38 offset:26112\n"
"v_mfma_f32_16x16x32_bf16 v[136:139], a[188:191], a[144:147], v[136:139]\n"
"buffer_load_dwordx4 v46, s[28:31], s62 offen lds\n"
"v_mfma_f32_16x16x32_bf16 v[140:143], a[180:183], a[148:151], v[140:143]\n"
"ds_read_b64_tr_b16 a[88:89], v39 offset:25600\n"
"v_mfma_f32_16x16x32_bf16 v[144:147], a[184:187], a[148:151], v[144:147]\n"
"ds_read_b64_tr_b16 a[90:91], v39 offset:26112\n"
"v_mfma_f32_16x16x32_bf16 v[148:151], a[188:191], a[148:151], v[148:151]\n"
"buffer_load_dword v90, v90, s[44:47], s70 offen\n"
"v_mfma_f32_16x16x32_bf16 v[152:155], a[180:183], a[152:155], v[152:155]\n"
"s_lshl_b32 s16, s57, 10\n"
"buffer_load_dwordx4 v47, s[28:31], s62 offen offset:1024 lds\n"
".Lwalk4_%=:\n"
"s_mov_b64 exec, 0\n"
"s_mov_b32 s98, .Lpad10_%=-.Lentry_%=\n"
"s_cmp_eq_u32 s52, 1\n"
"s_cselect_b32 s98, .Lpad11_%=-.Lentry_%=, s98\n"
"s_cmp_eq_u32 s52, 2\n"
"s_cselect_b32 s98, .Lpad12_%=-.Lentry_%=, s98\n"
"s_cmp_eq_u32 s52, 3\n"
"s_cselect_b32 s98, .Lpad13_%=-.Lentry_%=, s98\n"
"s_getpc_b64 s[96:97]\n"
".Lwh40_%=:\n"
"s_sub_u32 s96, s96, .Lwh40_%=-.Lentry_%=\n"
"s_subb_u32 s97, s97, 0\n"
"s_add_u32 s96, s96, s98\n"
"s_addc_u32 s97, s97, 0\n"
"s_swappc_b64 vcc, s[96:97]\n"
"s_mov_b32 s98, .Lpad14_%=-.Lentry_%=\n"
"s_cmp_eq_u32 s52, 1\n"
"s_cselect_b32 s98, .Lpad15_%=-.Lentry_%=, s98\n"
"s_cmp_eq_u32 s52, 2\n"
"s_cselect_b32 s98, .Lpad16_%=-.Lentry_%=, s98\n"
"s_cmp_eq_u32 s52, 3\n"
"s_cselect_b32 s98, .Lpad16_%=-.Lentry_%=, s98\n"
"s_getpc_b64 s[96:97]\n"
".Lwh41_%=:\n"
"s_sub_u32 s96, s96, .Lwh41_%=-.Lentry_%=\n"
"s_subb_u32 s97, s97, 0\n"
"s_add_u32 s96, s96, s98\n"
"s_addc_u32 s97, s97, 0\n"
"s_swappc_b64 vcc, s[96:97]\n"
"s_mov_b64 exec, -1\n"
"v_mfma_f32_16x16x32_bf16 v[156:159], a[184:187], a[152:155], v[156:159]\n"
"s_lshl_b32 s17, s52, 8\n"
"ds_read_b64_tr_b16 a[92:93], v40 offset:25600\n"
"v_mfma_f32_16x16x32_bf16 v[160:163], a[188:191], a[152:155], v[160:163]\n"
"s_add_u32 s16, s16, s17\n"
"ds_read_b64_tr_b16 a[94:95], v40 offset:26112\n"
"v_mfma_f32_16x16x32_bf16 v[164:167], a[180:183], a[156:159], v[164:167]\n"
"s_add_u32 s16, s16, 15728640\n"
"ds_read_b128 a[48:51], v29 offset:4608\n"
"v_mfma_f32_16x16x32_bf16 v[168:171], a[184:187], a[156:159], v[168:171]\n"
"buffer_load_dwordx4 v48, s[28:31], s62 offen offset:2048 lds\n"
"v_mfma_f32_16x16x32_bf16 v[172:175], a[188:191], a[156:159], v[172:175]\n"
"s_add_u32 m0, s53, 22528\n"
"ds_read_b128 a[52:55], v29 offset:6656\n"
"s_add_u32 s60, s74, 3\n"
"v_mfma_f32_16x16x32_bf16 v[176:179], a[180:183], a[160:163], v[176:179]\n"
"s_and_b32 s60, s60, 7\n"
"ds_read_b128 a[56:59], v31 offset:4608\n"
"s_mul_i32 s62, s60, 786432\n"
"v_mfma_f32_16x16x32_bf16 v[180:183], a[184:187], a[160:163], v[180:183]\n"
"ds_read_b128 a[60:63], v31 offset:6656\n"
"v_lshlrev_b32 v89, 2, v0\n"
"v_mfma_f32_16x16x32_bf16 v[184:187], a[188:191], a[160:163], v[184:187]\n"
"ds_read_b128 a[64:67], v33 offset:4608\n"
"v_mfma_f32_16x16x32_bf16 v[188:191], a[180:183], a[164:167], v[188:191]\n"
"buffer_load_dwordx4 v46, s[28:31], s62 offen lds\n"
"v_mfma_f32_16x16x32_bf16 v[192:195], a[184:187], a[164:167], v[192:195]\n"
"ds_read_b128 a[68:71], v33 offset:6656\n"
"v_mfma_f32_16x16x32_bf16 v[196:199], a[188:191], a[164:167], v[196:199]\n"
"ds_read_b128 a[72:75], v35 offset:4608\n"
"v_mfma_f32_16x16x32_bf16 v[200:203], a[180:183], a[168:171], v[200:203]\n"
"ds_read_b128 a[76:79], v35 offset:6656\n"
"v_mfma_f32_16x16x32_bf16 v[204:207], a[184:187], a[168:171], v[204:207]\n"
"ds_read_b128 a[80:83], v37 offset:4608\n"
"buffer_load_dwordx4 v47, s[28:31], s62 offen offset:1024 lds\n"
"buffer_load_dword v89, v89, s[44:47], s16 offen\n"
"v_mfma_f32_16x16x32_bf16 v[208:211], a[188:191], a[168:171], v[208:211]\n"
"s_mul_i32 s16, s57, 46080\n"
"v_mfma_f32_16x16x32_bf16 v[212:215], a[180:183], a[172:175], v[212:215]\n"
"s_add_u32 s71, s16, 16777216\n"
"v_mfma_f32_16x16x32_bf16 v[216:219], a[184:187], a[172:175], v[216:219]\n"
"v_lshrrev_b32 v60, 2, v0\n"
"v_mfma_f32_16x16x32_bf16 v[220:223], a[188:191], a[172:175], v[220:223]\n"
"buffer_load_dwordx4 v48, s[28:31], s62 offen offset:2048 lds\n"
"v_mfma_f32_16x16x32_bf16 v[224:227], a[180:183], a[176:179], v[224:227]\n"
"s_add_u32 m0, s53, 0\n"
"s_lshl_b32 s61, s60, 7\n"
"v_lshlrev_b32 v61, 7, v60\n"
"v_mfma_f32_16x16x32_bf16 v[228:231], a[184:187], a[176:179], v[228:231]\n"
"v_and_b32 v62, 3, v0\n"
"v_mfma_f32_16x16x32_bf16 v[232:235], a[188:191], a[176:179], v[232:235]\n"
"s_waitcnt vmcnt(8) lgkmcnt(0)\n"
"s_barrier\n"
"ds_read_b64_tr_b16 a[180:181], v38 offset:16384\n"
"v_mfma_f32_16x16x32_bf16 v[128:131], a[84:87], a[48:51], v[128:131]\n"
"buffer_load_dwordx4 v41, s[24:27], s61 offen lds\n"
"v_mfma_f32_16x16x32_bf16 v[132:135], a[88:91], a[48:51], v[132:135]\n"
"ds_read_b64_tr_b16 a[182:183], v38 offset:16896\n"
"v_lshl_add_u32 v61, v62, 3, v61\n"
"v_mfma_f32_16x16x32_bf16 v[136:139], a[92:95], a[48:51], v[136:139]\n"
"ds_read_b64_tr_b16 a[184:185], v39 offset:16384\n"
"v_bfe_u32 v60, v60, 1, 2\n"
"v_mfma_f32_16x16x32_bf16 v[140:143], a[84:87], a[52:55], v[140:143]\n"
"ds_read_b64_tr_b16 a[186:187], v39 offset:16896\n"
"v_xor_b32 v62, 0, v60\n"
"v_mfma_f32_16x16x32_bf16 v[144:147], a[88:91], a[52:55], v[144:147]\n"
"buffer_load_dwordx4 v42, s[24:27], s61 offen offset:1024 lds\n"
"v_mfma_f32_16x16x32_bf16 v[148:151], a[92:95], a[52:55], v[148:151]\n"
"ds_read_b64_tr_b16 a[188:189], v40 offset:16384\n"
"v_lshl_add_u32 v62, v62, 5, v61\n"
"v_mfma_f32_16x16x32_bf16 v[152:155], a[84:87], a[56:59], v[152:155]\n"
"s_xor_b32 s16, s52, 0\n"
"ds_read_b64_tr_b16 a[190:191], v40 offset:16896\n"
"v_mfma_f32_16x16x32_bf16 v[156:159], a[88:91], a[56:59], v[156:159]\n"
"s_mul_i32 s16, s16, 40960\n"
"ds_read_b128 a[144:147], v28 offset:9216\n"
"v_mfma_f32_16x16x32_bf16 v[160:163], a[92:95], a[56:59], v[160:163]\n"
"buffer_load_dwordx4 v43, s[24:27], s61 offen offset:2048 lds\n"
"v_mfma_f32_16x16x32_bf16 v[164:167], a[84:87], a[60:63], v[164:167]\n"
"ds_read_b128 a[148:151], v28 offset:11264\n"
"v_add_u32 v96, s16, v62\n"
"v_mfma_f32_16x16x32_bf16 v[168:171], a[88:91], a[60:63], v[168:171]\n"
"s_xor_b32 s17, s52, 1\n"
"ds_read_b128 a[152:155], v30 offset:9216\n"
"v_mfma_f32_16x16x32_bf16 v[172:175], a[92:95], a[60:63], v[172:175]\n"
"s_mul_i32 s17, s17, 40960\n"
"ds_read_b128 a[156:159], v30 offset:11264\n"
"v_mfma_f32_16x16x32_bf16 v[176:179], a[84:87], a[64:67], v[176:179]\n"
"buffer_load_dwordx4 v44, s[24:27], s61 offen offset:3072 lds\n"
"v_mfma_f32_16x16x32_bf16 v[180:183], a[88:91], a[64:67], v[180:183]\n"
"s_add_u32 m0, s53, 4096\n"
"ds_read_b128 a[160:163], v32 offset:9216\n"
"v_add_u32 v97, s17, v62\n"
"v_mfma_f32_16x16x32_bf16 v[184:187], a[92:95], a[64:67], v[184:187]\n"
"s_xor_b32 s18, s52, 2\n"
"ds_read_b128 a[164:167], v32 offset:11264\n"
"v_mfma_f32_16x16x32_bf16 v[188:191], a[84:87], a[68:71], v[188:191]\n"
"s_mul_i32 s18, s18, 40960\n"
"ds_read_b128 a[168:171], v34 offset:9216\n"
"v_mfma_f32_16x16x32_bf16 v[192:195], a[88:91], a[68:71], v[192:195]\n"
"s_mov_b32 exec_hi, 0\n"
"buffer_load_dwordx4 v45, s[24:27], s61 offen lds\n"
"s_mov_b32 exec_hi, -1\n"
"v_mfma_f32_16x16x32_bf16 v[196:199], a[92:95], a[68:71], v[196:199]\n"
"s_add_u32 m0, s53, 25600\n"
"ds_read_b128 a[172:175], v34 offset:11264\n"
"s_add_u32 s62, s62, 393216\n"
"v_mfma_f32_16x16x32_bf16 v[200:203], a[84:87], a[72:75], v[200:203]\n"
"ds_read_b128 a[176:179], v36 offset:9216\n"
"v_add_u32 v98, s18, v62\n"
"v_mfma_f32_16x16x32_bf16 v[204:207], a[88:91], a[72:75], v[204:207]\n"
"s_xor_b32 s19, s52, 3\n"
"v_mfma_f32_16x16x32_bf16 v[208:211], a[92:95], a[72:75], v[208:211]\n"
"s_mul_i32 s19, s19, 40960\n"
"buffer_load_dwordx4 v46, s[28:31], s62 offen lds\n"
"v_mfma_f32_16x16x32_bf16 v[212:215], a[84:87], a[76:79], v[212:215]\n"
"v_add_u32 v99, s19, v62\n"
"v_mfma_f32_16x16x32_bf16 v[216:219], a[88:91], a[76:79], v[216:219]\n"
"v_xor_b32 v62, 1, v60\n"
"v_mfma_f32_16x16x32_bf16 v[220:223], a[92:95], a[76:79], v[220:223]\n"
"v_lshl_add_u32 v62, v62, 5, v61\n"
"v_mfma_f32_16x16x32_bf16 v[224:227], a[84:87], a[80:83], v[224:227]\n"
"buffer_load_dwordx4 v47, s[28:31], s62 offen offset:1024 lds\n"
"v_mfma_f32_16x16x32_bf16 v[228:231], a[88:91], a[80:83], v[228:231]\n"
"v_add_u32 v100, s16, v62\n"
"v_mfma_f32_16x16x32_bf16 v[232:235], a[92:95], a[80:83], v[232:235]\n"
"s_waitcnt vmcnt(11) lgkmcnt(0)\n"
"s_barrier\n"
"ds_read_b64_tr_b16 a[84:85], v38 offset:19456\n"
"v_mfma_f32_16x16x32_bf16 v[128:131], a[180:183], a[144:147], v[128:131]\n"
"ds_read_b64_tr_b16 a[86:87], v38 offset:19968\n"
"v_add_u32 v101, s17, v62\n"
"v_mfma_f32_16x16x32_bf16 v[132:135], a[184:187], a[144:147], v[132:135]\n"
"ds_read_b64_tr_b16 a[88:89], v39 offset:19456\n"
"v_add_u32 v102, s18, v62\n"
"v_mfma_f32_16x16x32_bf16 v[136:139], a[188:191], a[144:147], v[136:139]\n"
"buffer_load_dwordx4 v48, s[28:31], s62 offen offset:2048 lds\n"
"v_mfma_f32_16x16x32_bf16 v[140:143], a[180:183], a[148:151], v[140:143]\n"
"s_add_u32 m0, s53, 16384\n"
"s_add_u32 s60, s74, 4\n"
"s_and_b32 s60, s60, 7\n"
"v_mfma_f32_16x16x32_bf16 v[144:147], a[184:187], a[148:151], v[144:147]\n"
"s_mul_i32 s62, s60, 786432\n"
"ds_read_b64_tr_b16 a[90:91], v39 offset:19968\n"
"v_add_u32 v103, s19, v62\n"
"v_mfma_f32_16x16x32_bf16 v[148:151], a[188:191], a[148:151], v[148:151]\n"
"ds_read_b64_tr_b16 a[92:93], v40 offset:19456\n"
"v_xor_b32 v62, 2, v60\n"
"v_mfma_f32_16x16x32_bf16 v[152:155], a[180:183], a[152:155], v[152:155]\n"
"buffer_load_dwordx4 v46, s[28:31], s62 offen lds\n"
"v_mfma_f32_16x16x32_bf16 v[156:159], a[184:187], a[152:155], v[156:159]\n"
"ds_read_b64_tr_b16 a[94:95], v40 offset:19968\n"
"v_lshl_add_u32 v62, v62, 5, v61\n"
"v_mfma_f32_16x16x32_bf16 v[160:163], a[188:191], a[152:155], v[160:163]\n"
"ds_read_b128 a[48:51], v29 offset:9216\n"
"v_add_u32 v104, s16, v62\n"
"v_mfma_f32_16x16x32_bf16 v[164:167], a[180:183], a[156:159], v[164:167]\n"
"ds_read_b128 a[52:55], v29 offset:11264\n"
"v_add_u32 v105, s17, v62\n"
"v_mfma_f32_16x16x32_bf16 v[168:171], a[184:187], a[156:159], v[168:171]\n"
"buffer_load_dwordx4 v47, s[28:31], s62 offen offset:1024 lds\n"
"v_mfma_f32_16x16x32_bf16 v[172:175], a[188:191], a[156:159], v[172:175]\n"
"ds_read_b128 a[56:59], v31 offset:9216\n"
"v_add_u32 v106, s18, v62\n"
"v_mfma_f32_16x16x32_bf16 v[176:179], a[180:183], a[160:163], v[176:179]\n"
"ds_read_b128 a[60:63], v31 offset:11264\n"
"v_add_u32 v107, s19, v62\n"
"v_mfma_f32_16x16x32_bf16 v[180:183], a[184:187], a[160:163], v[180:183]\n"
"ds_read_b128 a[64:67], v33 offset:9216\n"
"v_xor_b32 v62, 3, v60\n"
"v_mfma_f32_16x16x32_bf16 v[184:187], a[188:191], a[160:163], v[184:187]\n"
"ds_read_b128 a[68:71], v33 offset:11264\n"
"v_lshl_add_u32 v62, v62, 5, v61\n"
"v_mfma_f32_16x16x32_bf16 v[188:191], a[180:183], a[164:167], v[188:191]\n"
"ds_read_b128 a[72:75], v35 offset:9216\n"
"v_add_u32 v108, s16, v62\n"
"v_mfma_f32_16x16x32_bf16 v[192:195], a[184:187], a[164:167], v[192:195]\n"
"buffer_load_dwordx4 v48, s[28:31], s62 offen offset:2048 lds\n"
"v_mfma_f32_16x16x32_bf16 v[196:199], a[188:191], a[164:167], v[196:199]\n"
"s_add_u32 m0, s53, 4608\n"
"ds_read_b128 a[76:79], v35 offset:11264\n"
"s_lshl_b32 s61, s60, 7\n"
"v_mfma_f32_16x16x32_bf16 v[200:203], a[180:183], a[168:171], v[200:203]\n"
"ds_read_b128 a[80:83], v37 offset:9216\n"
"v_add_u32 v109, s17, v62\n"
"v_mfma_f32_16x16x32_bf16 v[204:207], a[184:187], a[168:171], v[204:207]\n"
"v_add_u32 v110, s18, v62\n"
"v_mfma_f32_16x16x32_bf16 v[208:211], a[188:191], a[168:171], v[208:211]\n"
"buffer_load_dwordx4 v41, s[24:27], s61 offen lds\n"
"v_mfma_f32_16x16x32_bf16 v[212:215], a[180:183], a[172:175], v[212:215]\n"
"v_add_u32 v111, s19, v62\n"
"v_mfma_f32_16x16x32_bf16 v[216:219], a[184:187], a[172:175], v[216:219]\n"
"v_mov_b32 v22, 0xff800000\n"
"v_mfma_f32_16x16x32_bf16 v[220:223], a[188:191], a[172:175], v[220:223]\n"
"v_mov_b32 v23, 0xff800000\n"
"v_mfma_f32_16x16x32_bf16 v[224:227], a[180:183], a[176:179], v[224:227]\n"
"buffer_load_dwordx4 v42, s[24:27], s61 offen offset:1024 lds\n"
"v_mfma_f32_16x16x32_bf16 v[228:231], a[184:187], a[176:179], v[228:231]\n"
"v_mov_b32 v24, 0xff800000\n"
"v_mfma_f32_16x16x32_bf16 v[232:235], a[188:191], a[176:179], v[232:235]\n"
"s_waitcnt vmcnt(8) lgkmcnt(0)\n"
"s_barrier\n"
"ds_read_b64_tr_b16 a[180:181], v38 offset:22528\n"
"v_mfma_f32_16x16x32_bf16 v[128:131], a[84:87], a[48:51], v[128:131]\n"
"ds_read_b64_tr_b16 a[182:183], v38 offset:23040\n"
"v_mul_u32_u24 v60, 40960, v59\n"
"v_mfma_f32_16x16x32_bf16 v[132:135], a[88:91], a[48:51], v[132:135]\n"
"buffer_load_dwordx4 v43, s[24:27], s61 offen offset:2048 lds\n"
"v_mfma_f32_16x16x32_bf16 v[136:139], a[92:95], a[48:51], v[136:139]\n"
"ds_read_b64_tr_b16 a[184:185], v39 offset:22528\n"
"v_bfe_u32 v61, v0, 2, 2\n"
"v_mfma_f32_16x16x32_bf16 v[140:143], a[84:87], a[52:55], v[140:143]\n"
"ds_read_b64_tr_b16 a[186:187], v39 offset:23040\n"
"v_mul_u32_u24 v61, 320, v61\n"
"v_mfma_f32_16x16x32_bf16 v[144:147], a[88:91], a[52:55], v[144:147]\n"
"ds_read_b64_tr_b16 a[188:189], v40 offset:22528\n"
"v_and_b32 v62, 3, v0\n"
"v_mfma_f32_16x16x32_bf16 v[148:151], a[92:95], a[52:55], v[148:151]\n"
"buffer_load_dwordx4 v44, s[24:27], s61 offen offset:3072 lds\n"
"v_mfma_f32_16x16x32_bf16 v[152:155], a[84:87], a[56:59], v[152:155]\n"
"s_add_u32 m0, s53, 8704\n"
"ds_read_b64_tr_b16 a[190:191], v40 offset:23040\n"
"v_lshl_add_u32 v61, v62, 3, v61\n"
"v_mfma_f32_16x16x32_bf16 v[156:159], a[88:91], a[56:59], v[156:159]\n"
"ds_read_b128 a[144:147], v28 offset:0\n"
"v_add_u32 v60, v60, v61\n"
"v_mfma_f32_16x16x32_bf16 v[160:163], a[92:95], a[56:59], v[160:163]\n"
"ds_read_b128 a[148:151], v28 offset:2048\n"
"v_and_b32 v61, 1, v59\n"
"v_mfma_f32_16x16x32_bf16 v[164:167], a[84:87], a[60:63], v[164:167]\n"
"s_mov_b32 exec_hi, 0\n"
"buffer_load_dwordx4 v45, s[24:27], s61 offen lds\n"
"s_mov_b32 exec_hi, -1\n"
"v_mfma_f32_16x16x32_bf16 v[168:171], a[88:91], a[60:63], v[168:171]\n"
"s_add_u32 m0, s53, 19456\n"
"ds_read_b128 a[152:155], v30 offset:0\n"
"s_add_u32 s62, s62, 393216\n"
"v_mfma_f32_16x16x32_bf16 v[172:175], a[92:95], a[60:63], v[172:175]\n"
"s_mul_i32 s18, s52, 3\n"
"ds_read_b128 a[156:159], v30 offset:2048\n"
"v_mfma_f32_16x16x32_bf16 v[176:179], a[84:87], a[64:67], v[176:179]\n"
"s_add_u32 s16, s18, 3\n"
"ds_read_b128 a[160:163], v32 offset:0\n"
"v_mfma_f32_16x16x32_bf16 v[180:183], a[88:91], a[64:67], v[180:183]\n"
"buffer_load_dwordx4 v46, s[28:31], s62 offen lds\n"
"v_mfma_f32_16x16x32_bf16 v[184:187], a[92:95], a[64:67], v[184:187]\n"
"s_sub_u32 s17, s16, 10\n"
"s_cselect_b32 s16, s16, s17\n"
"ds_read_b128 a[164:167], v32 offset:2048\n"
"v_mfma_f32_16x16x32_bf16 v[188:191], a[84:87], a[68:71], v[188:191]\n"
"ds_read_b128 a[168:171], v34 offset:0\n"
"v_xor_b32 v62, s16, v61\n"
"v_mfma_f32_16x16x32_bf16 v[192:195], a[88:91], a[68:71], v[192:195]\n"
"ds_read_b128 a[172:175], v34 offset:2048\n"
"v_lshl_add_u32 v62, v62, 5, v60\n"
"v_mfma_f32_16x16x32_bf16 v[196:199], a[92:95], a[68:71], v[196:199]\n"
"ds_read_b128 a[176:179], v36 offset:0\n"
"v_accvgpr_write_b32 a112, v62\n"
"v_mfma_f32_16x16x32_bf16 v[200:203], a[84:87], a[72:75], v[200:203]\n"
"s_add_u32 s16, s18, 4\n"
"buffer_load_dwordx4 v47, s[28:31], s62 offen offset:1024 lds\n"
"v_mfma_f32_16x16x32_bf16 v[204:207], a[88:91], a[72:75], v[204:207]\n"
"s_sub_u32 s17, s16, 10\n"
"s_cselect_b32 s16, s16, s17\n"
"v_mfma_f32_16x16x32_bf16 v[208:211], a[92:95], a[72:75], v[208:211]\n"
"v_xor_b32 v62, s16, v61\n"
"v_mfma_f32_16x16x32_bf16 v[212:215], a[84:87], a[76:79], v[212:215]\n"
"v_lshl_add_u32 v62, v62, 5, v60\n"
"v_mfma_f32_16x16x32_bf16 v[216:219], a[88:91], a[76:79], v[216:219]\n"
"buffer_load_dwordx4 v48, s[28:31], s62 offen offset:2048 lds\n"
"v_mfma_f32_16x16x32_bf16 v[220:223], a[92:95], a[76:79], v[220:223]\n"
"v_accvgpr_write_b32 a113, v62\n"
"v_mfma_f32_16x16x32_bf16 v[224:227], a[84:87], a[80:83], v[224:227]\n"
"s_add_u32 m0, s53, 22528\n"
"s_add_u32 s60, s74, 5\n"
"s_and_b32 s60, s60, 7\n"
"v_mfma_f32_16x16x32_bf16 v[228:231], a[88:91], a[80:83], v[228:231]\n"
"s_mul_i32 s62, s60, 786432\n"
"s_add_u32 s16, s18, 5\n"
"v_mfma_f32_16x16x32_bf16 v[232:235], a[92:95], a[80:83], v[232:235]\n"
"s_waitcnt vmcnt(11) lgkmcnt(0)\n"
"s_barrier\n"
"ds_read_b64_tr_b16 a[84:85], v38 offset:25600\n"
"v_mfma_f32_16x16x32_bf16 v[128:131], a[180:183], a[144:147], v[128:131]\n"
"buffer_load_dwordx4 v46, s[28:31], s62 offen lds\n"
"v_mfma_f32_16x16x32_bf16 v[132:135], a[184:187], a[144:147], v[132:135]\n"
"s_sub_u32 s17, s16, 10\n"
"s_cselect_b32 s16, s16, s17\n"
"ds_read_b64_tr_b16 a[86:87], v38 offset:26112\n"
"v_mfma_f32_16x16x32_bf16 v[136:139], a[188:191], a[144:147], v[136:139]\n"
"ds_read_b64_tr_b16 a[88:89], v39 offset:25600\n"
"v_xor_b32 v62, s16, v61\n"
"v_mfma_f32_16x16x32_bf16 v[140:143], a[180:183], a[148:151], v[140:143]\n"
"ds_read_b64_tr_b16 a[90:91], v39 offset:26112\n"
"v_lshl_add_u32 v62, v62, 5, v60\n"
"v_mfma_f32_16x16x32_bf16 v[144:147], a[184:187], a[148:151], v[144:147]\n"
"buffer_load_dwordx4 v47, s[28:31], s62 offen offset:1024 lds\n"
"v_mfma_f32_16x16x32_bf16 v[148:151], a[188:191], a[148:151], v[148:151]\n"
"ds_read_b64_tr_b16 a[92:93], v40 offset:25600\n"
"v_accvgpr_write_b32 a114, v62\n"
"v_mfma_f32_16x16x32_bf16 v[152:155], a[180:183], a[152:155], v[152:155]\n"
"s_add_u32 s16, s18, 0\n"
"ds_read_b64_tr_b16 a[94:95], v40 offset:26112\n"
"v_mfma_f32_16x16x32_bf16 v[156:159], a[184:187], a[152:155], v[156:159]\n"
"s_sub_u32 s17, s16, 10\n"
"s_cselect_b32 s16, s16, s17\n"
"ds_read_b128 a[48:51], v29 offset:0\n"
"v_mfma_f32_16x16x32_bf16 v[160:163], a[188:191], a[152:155], v[160:163]\n"
"buffer_load_dwordx4 v48, s[28:31], s62 offen offset:2048 lds\n"
"v_mfma_f32_16x16x32_bf16 v[164:167], a[180:183], a[156:159], v[164:167]\n"
"s_add_u32 m0, s53, 9216\n"
"ds_read_b128 a[52:55], v29 offset:2048\n"
"s_lshl_b32 s61, s60, 7\n"
"v_mfma_f32_16x16x32_bf16 v[168:171], a[184:187], a[156:159], v[168:171]\n"
"ds_read_b128 a[56:59], v31 offset:0\n"
"v_xor_b32 v62, s16, v61\n"
"v_mfma_f32_16x16x32_bf16 v[172:175], a[188:191], a[156:159], v[172:175]\n"
"ds_read_b128 a[60:63], v31 offset:2048\n"
"v_lshl_add_u32 v62, v62, 5, v60\n"
"v_mfma_f32_16x16x32_bf16 v[176:179], a[180:183], a[160:163], v[176:179]\n"
"buffer_load_dwordx4 v41, s[24:27], s61 offen lds\n"
"v_mfma_f32_16x16x32_bf16 v[180:183], a[184:187], a[160:163], v[180:183]\n"
"ds_read_b128 a[64:67], v33 offset:0\n"
"v_accvgpr_write_b32 a115, v62\n"
"v_mfma_f32_16x16x32_bf16 v[184:187], a[188:191], a[160:163], v[184:187]\n"
"s_add_u32 s16, s18, 6\n"
"ds_read_b128 a[68:71], v33 offset:2048\n"
"v_mfma_f32_16x16x32_bf16 v[188:191], a[180:183], a[164:167], v[188:191]\n"
"s_sub_u32 s17, s16, 10\n"
"s_cselect_b32 s16, s16, s17\n"
"ds_read_b128 a[72:75], v35 offset:0\n"
"v_mfma_f32_16x16x32_bf16 v[192:195], a[184:187], a[164:167], v[192:195]\n"
"ds_read_b128 a[76:79], v35 offset:2048\n"
"v_xor_b32 v62, s16, v61\n"
"v_mfma_f32_16x16x32_bf16 v[196:199], a[188:191], a[164:167], v[196:199]\n"
"ds_read_b128 a[80:83], v37 offset:0\n"
"v_lshl_add_u32 v62, v62, 5, v60\n"
"v_mfma_f32_16x16x32_bf16 v[200:203], a[180:183], a[168:171], v[200:203]\n"
"buffer_load_dwordx4 v42, s[24:27], s61 offen offset:1024 lds\n"
"v_mfma_f32_16x16x32_bf16 v[204:207], a[184:187], a[168:171], v[204:207]\n"
"v_accvgpr_write_b32 a116, v62\n"
"v_mfma_f32_16x16x32_bf16 v[208:211], a[188:191], a[168:171], v[208:211]\n"
"s_add_u32 s16, s18, 7\n"
"v_mfma_f32_16x16x32_bf16 v[212:215], a[180:183], a[172:175], v[212:215]\n"
"s_sub_u32 s17, s16, 10\n"
"s_cselect_b32 s16, s16, s17\n"
"v_mfma_f32_16x16x32_bf16 v[216:219], a[184:187], a[172:175], v[216:219]\n"
"buffer_load_dwordx4 v43, s[24:27], s61 offen offset:2048 lds\n"
"v_mfma_f32_16x16x32_bf16 v[220:223], a[188:191], a[172:175], v[220:223]\n"
"v_xor_b32 v62, s16, v61\n"
"v_mfma_f32_16x16x32_bf16 v[224:227], a[180:183], a[176:179], v[224:227]\n"
"v_lshl_add_u32 v62, v62, 5, v60\n"
"v_mfma_f32_16x16x32_bf16 v[228:231], a[184:187], a[176:179], v[228:231]\n"
"v_accvgpr_write_b32 a117, v62\n"
"v_mfma_f32_16x16x32_bf16 v[232:235], a[188:191], a[176:179], v[232:235]\n"
"s_waitcnt vmcnt(9) lgkmcnt(0)\n"
"s_barrier\n"
"ds_read_b64_tr_b16 a[180:181], v38 offset:16384\n"
"v_mfma_f32_16x16x32_bf16 v[128:131], a[84:87], a[48:51], v[128:131]\n"
"s_add_u32 s16, s18, 1\n"
"buffer_load_dwordx4 v44, s[24:27], s61 offen offset:3072 lds\n"
"v_mfma_f32_16x16x32_bf16 v[132:135], a[88:91], a[48:51], v[132:135]\n"
"s_add_u32 m0, s53, 13312\n"
"ds_read_b64_tr_b16 a[182:183], v38 offset:16896\n"
"v_mfma_f32_16x16x32_bf16 v[136:139], a[92:95], a[48:51], v[136:139]\n"
"s_sub_u32 s17, s16, 10\n"
"s_cselect_b32 s16, s16, s17\n"
"ds_read_b64_tr_b16 a[184:185], v39 offset:16384\n"
"v_mfma_f32_16x16x32_bf16 v[140:143], a[84:87], a[52:55], v[140:143]\n"
"ds_read_b64_tr_b16 a[186:187], v39 offset:16896\n"
"v_xor_b32 v62, s16, v61\n"
"v_mfma_f32_16x16x32_bf16 v[144:147], a[88:91], a[52:55], v[144:147]\n"
"s_mov_b32 exec_hi, 0\n"
"buffer_load_dwordx4 v45, s[24:27], s61 offen lds\n"
"s_mov_b32 exec_hi, -1\n"
"v_mfma_f32_16x16x32_bf16 v[148:151], a[92:95], a[52:55], v[148:151]\n"
"s_add_u32 m0, s53, 25600\n"
"ds_read_b64_tr_b16 a[188:189], v40 offset:16384\n"
"s_add_u32 s62, s62, 393216\n"
"v_mfma_f32_16x16x32_bf16 v[152:155], a[84:87], a[56:59], v[152:155]\n"
"ds_read_b64_tr_b16 a[190:191], v40 offset:16896\n"
"v_lshl_add_u32 v62, v62, 5, v60\n"
"v_mfma_f32_16x16x32_bf16 v[156:159], a[88:91], a[56:59], v[156:159]\n"
"ds_read_b128 a[144:147], v28 offset:4608\n"
"v_accvgpr_write_b32 a118, v62\n"
"v_mfma_f32_16x16x32_bf16 v[160:163], a[92:95], a[56:59], v[160:163]\n"
"s_add_u32 s16, s18, 8\n"
"buffer_load_dwordx4 v46, s[28:31], s62 offen lds\n"
"v_mfma_f32_16x16x32_bf16 v[164:167], a[84:87], a[60:63], v[164:167]\n"
"s_sub_u32 s17, s16, 10\n"
"s_cselect_b32 s16, s16, s17\n"
"ds_read_b128 a[148:151], v28 offset:6656\n"
"v_mfma_f32_16x16x32_bf16 v[168:171], a[88:91], a[60:63], v[168:171]\n"
"ds_read_b128 a[152:155], v30 offset:4608\n"
"v_xor_b32 v62, s16, v61\n"
"v_mfma_f32_16x16x32_bf16 v[172:175], a[92:95], a[60:63], v[172:175]\n"
"ds_read_b128 a[156:159], v30 offset:6656\n"
"v_lshl_add_u32 v62, v62, 5, v60\n"
"v_mfma_f32_16x16x32_bf16 v[176:179], a[84:87], a[64:67], v[176:179]\n"
"buffer_load_dwordx4 v47, s[28:31], s62 offen offset:1024 lds\n"
"v_mfma_f32_16x16x32_bf16 v[180:183], a[88:91], a[64:67], v[180:183]\n"
"ds_read_b128 a[160:163], v32 offset:4608\n"
"v_accvgpr_write_b32 a119, v62\n"
"v_mfma_f32_16x16x32_bf16 v[184:187], a[92:95], a[64:67], v[184:187]\n"
"s_add_u32 s16, s18, 2\n"
"ds_read_b128 a[164:167], v32 offset:6656\n"
"v_mfma_f32_16x16x32_bf16 v[188:191], a[84:87], a[68:71], v[188:191]\n"
"s_sub_u32 s17, s16, 10\n"
"s_cselect_b32 s16, s16, s17\n"
"ds_read_b128 a[168:171], v34 offset:4608\n"
"v_mfma_f32_16x16x32_bf16 v[192:195], a[88:91], a[68:71], v[192:195]\n"
"ds_read_b128 a[172:175], v34 offset:6656\n"
"v_xor_b32 v62, s16, v61\n"
"v_mfma_f32_16x16x32_bf16 v[196:199], a[92:95], a[68:71], v[196:199]\n"
"buffer_load_dwordx4 v48, s[28:31], s62 offen offset:2048 lds\n"
"v_mfma_f32_16x16x32_bf16 v[200:203], a[84:87], a[72:75], v[200:203]\n"
"ds_read_b128 a[176:179], v36 offset:4608\n"
"v_lshl_add_u32 v62, v62, 5, v60\n"
"v_mfma_f32_16x16x32_bf16 v[204:207], a[88:91], a[72:75], v[204:207]\n"
"v_accvgpr_write_b32 a120, v62\n"
"v_mfma_f32_16x16x32_bf16 v[208:211], a[92:95], a[72:75], v[208:211]\n"
"s_add_u32 s16, s18, 9\n"
"v_mfma_f32_16x16x32_bf16 v[212:215], a[84:87], a[76:79], v[212:215]\n"
"s_sub_u32 s17, s16, 10\n"
"s_cselect_b32 s16, s16, s17\n"
"v_mfma_f32_16x16x32_bf16 v[216:219], a[88:91], a[76:79], v[216:219]\n"
"v_xor_b32 v62, s16, v61\n"
"v_mfma_f32_16x16x32_bf16 v[220:223], a[92:95], a[76:79], v[220:223]\n"
"v_lshl_add_u32 v62, v62, 5, v60\n"
"v_mfma_f32_16x16x32_bf16 v[224:227], a[84:87], a[80:83], v[224:227]\n"
"s_add_u32 m0, s53, 16384\n"
"s_add_u32 s60, s74, 6\n"
"s_and_b32 s60, s60, 7\n"
"v_mfma_f32_16x16x32_bf16 v[228:231], a[88:91], a[80:83], v[228:231]\n"
"s_mul_i32 s62, s60, 786432\n"
"v_accvgpr_write_b32 a121, v62\n"
"v_mfma_f32_16x16x32_bf16 v[232:235], a[92:95], a[80:83], v[232:235]\n"
"s_waitcnt vmcnt(11) lgkmcnt(0)\n"
"s_barrier\n"
"ds_read_b64_tr_b16 a[84:85], v38 offset:19456\n"
"v_mfma_f32_16x16x32_bf16 v[128:131], a[180:183], a[144:147], v[128:131]\n"
"s_and_b32 s18, s52, 1\n"
"buffer_load_dwordx4 v46, s[28:31], s62 offen lds\n"
"v_mfma_f32_16x16x32_bf16 v[132:135], a[184:187], a[144:147], v[132:135]\n"
"s_lshl_b32 s18, s18, 1\n"
"ds_read_b64_tr_b16 a[86:87], v38 offset:19968\n"
"v_mfma_f32_16x16x32_bf16 v[136:139], a[188:191], a[144:147], v[136:139]\n"
"s_mul_i32 s19, s52, 5120\n"
"ds_read_b64_tr_b16 a[88:89], v39 offset:19456\n"
"v_mfma_f32_16x16x32_bf16 v[140:143], a[180:183], a[148:151], v[140:143]\n"
"ds_read_b64_tr_b16 a[90:91], v39 offset:19968\n"
"v_add_u32 v60, 0, v0\n"
"v_mfma_f32_16x16x32_bf16 v[144:147], a[184:187], a[148:151], v[144:147]\n"
"buffer_load_dwordx4 v47, s[28:31], s62 offen offset:1024 lds\n"
"v_mfma_f32_16x16x32_bf16 v[148:151], a[188:191], a[148:151], v[148:151]\n"
"ds_read_b64_tr_b16 a[92:93], v40 offset:19456\n"
"v_mul_u32_u24 v61, 205, v60\n"
"v_mfma_f32_16x16x32_bf16 v[152:155], a[180:183], a[152:155], v[152:155]\n"
"ds_read_b64_tr_b16 a[94:95], v40 offset:19968\n"
"v_lshrrev_b32 v61, 12, v61\n"
"v_mfma_f32_16x16x32_bf16 v[156:159], a[184:187], a[152:155], v[156:159]\n"
"ds_read_b128 a[48:51], v29 offset:4608\n"
"v_mul_u32_u24 v62, 20, v61\n"
"v_mfma_f32_16x16x32_bf16 v[160:163], a[188:191], a[152:155], v[160:163]\n"
"buffer_load_dwordx4 v48, s[28:31], s62 offen offset:2048 lds\n"
"v_mfma_f32_16x16x32_bf16 v[164:167], a[180:183], a[156:159], v[164:167]\n"
"s_add_u32 m0, s53, 0\n"
"ds_read_b128 a[52:55], v29 offset:6656\n"
"s_lshl_b32 s61, s60, 7\n"
"v_mfma_f32_16x16x32_bf16 v[168:171], a[184:187], a[156:159], v[168:171]\n"
"ds_read_b128 a[56:59], v31 offset:4608\n"
"v_sub_u32 v60, v60, v62\n"
"v_mfma_f32_16x16x32_bf16 v[172:175], a[188:191], a[156:159], v[172:175]\n"
"ds_read_b128 a[60:63], v31 offset:6656\n"
"v_xor_b32 v60, s18, v60\n"
"v_mfma_f32_16x16x32_bf16 v[176:179], a[180:183], a[160:163], v[176:179]\n"
"buffer_load_dwordx4 v41, s[24:27], s61 offen lds\n"
"v_mfma_f32_16x16x32_bf16 v[180:183], a[184:187], a[160:163], v[180:183]\n"
"ds_read_b128 a[64:67], v33 offset:4608\n"
"v_lshrrev_b32 v62, 2, v61\n"
"v_mfma_f32_16x16x32_bf16 v[184:187], a[188:191], a[160:163], v[184:187]\n"
"ds_read_b128 a[68:71], v33 offset:6656\n"
"v_mad_u32_u24 v61, v62, 12, v61\n"
"v_mfma_f32_16x16x32_bf16 v[188:191], a[180:183], a[164:167], v[188:191]\n"
"ds_read_b128 a[72:75], v35 offset:4608\n"
"v_mul_u32_u24 v61, 1280, v61\n"
"v_mfma_f32_16x16x32_bf16 v[192:195], a[184:187], a[164:167], v[192:195]\n"
"ds_read_b128 a[76:79], v35 offset:6656\n"
"v_lshl_add_u32 v60, v60, 4, v61\n"
"v_mfma_f32_16x16x32_bf16 v[196:199], a[188:191], a[164:167], v[196:199]\n"
"ds_read_b128 a[80:83], v37 offset:4608\n"
"v_add_u32 v60, s19, v60\n"
"v_mfma_f32_16x16x32_bf16 v[200:203], a[180:183], a[168:171], v[200:203]\n"
"buffer_load_dwordx4 v42, s[24:27], s61 offen offset:1024 lds\n"
"v_mfma_f32_16x16x32_bf16 v[204:207], a[184:187], a[168:171], v[204:207]\n"
"v_accvgpr_write_b32 a122, v60\n"
"v_mfma_f32_16x16x32_bf16 v[208:211], a[188:191], a[168:171], v[208:211]\n"
"v_add_u32 v60, 64, v0\n"
"v_mfma_f32_16x16x32_bf16 v[212:215], a[180:183], a[172:175], v[212:215]\n"
"v_mul_u32_u24 v61, 205, v60\n"
"v_mfma_f32_16x16x32_bf16 v[216:219], a[184:187], a[172:175], v[216:219]\n"
"buffer_load_dwordx4 v43, s[24:27], s61 offen offset:2048 lds\n"
"v_mfma_f32_16x16x32_bf16 v[220:223], a[188:191], a[172:175], v[220:223]\n"
"v_lshrrev_b32 v61, 12, v61\n"
"v_mfma_f32_16x16x32_bf16 v[224:227], a[180:183], a[176:179], v[224:227]\n"
"v_mul_u32_u24 v62, 20, v61\n"
"v_mfma_f32_16x16x32_bf16 v[228:231], a[184:187], a[176:179], v[228:231]\n"
"v_sub_u32 v60, v60, v62\n"
"v_mfma_f32_16x16x32_bf16 v[232:235], a[188:191], a[176:179], v[232:235]\n"
"s_waitcnt vmcnt(9) lgkmcnt(0)\n"
"s_barrier\n"
"ds_read_b64_tr_b16 a[180:181], v38 offset:22528\n"
"v_mfma_f32_16x16x32_bf16 v[128:131], a[84:87], a[48:51], v[128:131]\n"
"buffer_load_dwordx4 v44, s[24:27], s61 offen offset:3072 lds\n"
"v_mfma_f32_16x16x32_bf16 v[132:135], a[88:91], a[48:51], v[132:135]\n"
"s_add_u32 m0, s53, 4096\n"
"ds_read_b64_tr_b16 a[182:183], v38 offset:23040\n"
"v_xor_b32 v60, s18, v60\n"
"v_mfma_f32_16x16x32_bf16 v[136:139], a[92:95], a[48:51], v[136:139]\n"
"ds_read_b64_tr_b16 a[184:185], v39 offset:22528\n"
"v_lshrrev_b32 v62, 2, v61\n"
"v_mfma_f32_16x16x32_bf16 v[140:143], a[84:87], a[52:55], v[140:143]\n"
"ds_read_b64_tr_b16 a[186:187], v39 offset:23040\n"
"v_mad_u32_u24 v61, v62, 12, v61\n"
"v_mfma_f32_16x16x32_bf16 v[144:147], a[88:91], a[52:55], v[144:147]\n"
"s_mov_b32 exec_hi, 0\n"
"buffer_load_dwordx4 v45, s[24:27], s61 offen lds\n"
"s_mov_b32 exec_hi, -1\n"
"v_mfma_f32_16x16x32_bf16 v[148:151], a[92:95], a[52:55], v[148:151]\n"
"s_add_u32 m0, s53, 19456\n"
"ds_read_b64_tr_b16 a[188:189], v40 offset:22528\n"
"s_add_u32 s62, s62, 393216\n"
"v_mfma_f32_16x16x32_bf16 v[152:155], a[84:87], a[56:59], v[152:155]\n"
"ds_read_b64_tr_b16 a[190:191], v40 offset:23040\n"
"v_mul_u32_u24 v61, 1280, v61\n"
"v_mfma_f32_16x16x32_bf16 v[156:159], a[88:91], a[56:59], v[156:159]\n"
"ds_read_b128 a[144:147], v28 offset:9216\n"
"v_lshl_add_u32 v60, v60, 4, v61\n"
"s_nop 0\n"
".Lpad2_%=:\n"
"s_cbranch_execz .Lwb_%=\n"
"v_mfma_f32_16x16x32_bf16 v[160:163], a[92:95], a[56:59], v[160:163]\n"
"buffer_load_dwordx4 v46, s[28:31], s62 offen lds\n"
"v_mfma_f32_16x16x32_bf16 v[164:167], a[84:87], a[60:63], v[164:167]\n"
"ds_read_b128 a[148:151], v28 offset:11264\n"
"v_add_u32 v60, s19, v60\n"
"v_mfma_f32_16x16x32_bf16 v[168:171], a[88:91], a[60:63], v[168:171]\n"
"ds_read_b128 a[152:155], v30 offset:9216\n"
"v_subrev_u32 v60, 1024, v60\n"
"v_mfma_f32_16x16x32_bf16 v[172:175], a[92:95], a[60:63], v[172:175]\n"
"ds_read_b128 a[156:159], v30 offset:11264\n"
"v_accvgpr_write_b32 a123, v60\n"
"v_mfma_f32_16x16x32_bf16 v[176:179], a[84:87], a[64:67], v[176:179]\n"
"buffer_load_dwordx4 v47, s[28:31], s62 offen offset:1024 lds\n"
"v_mfma_f32_16x16x32_bf16 v[180:183], a[88:91], a[64:67], v[180:183]\n"
"ds_read_b128 a[160:163], v32 offset:9216\n"
"v_add_u32 v60, 128, v0\n"
"v_mfma_f32_16x16x32_bf16 v[184:187], a[92:95], a[64:67], v[184:187]\n"
"ds_read_b128 a[164:167], v32 offset:11264\n"
"v_mul_u32_u24 v61, 205, v60\n"
"v_mfma_f32_16x16x32_bf16 v[188:191], a[84:87], a[68:71], v[188:191]\n"
"ds_read_b128 a[168:171], v34 offset:9216\n"
"v_lshrrev_b32 v61, 12, v61\n"
"v_mfma_f32_16x16x32_bf16 v[192:195], a[88:91], a[68:71], v[192:195]\n"
"ds_read_b128 a[172:175], v34 offset:11264\n"
"v_mul_u32_u24 v62, 20, v61\n"
"v_mfma_f32_16x16x32_bf16 v[196:199], a[92:95], a[68:71], v[196:199]\n"
"buffer_load_dwordx4 v48, s[28:31], s62 offen offset:2048 lds\n"
"v_mfma_f32_16x16x32_bf16 v[200:203], a[84:87], a[72:75], v[200:203]\n"
"ds_read_b128 a[176:179], v36 offset:9216\n"
"v_sub_u32 v60, v60, v62\n"
"v_mfma_f32_16x16x32_bf16 v[204:207], a[88:91], a[72:75], v[204:207]\n"
"v_xor_b32 v60, s18, v60\n"
"v_mfma_f32_16x16x32_bf16 v[208:211], a[92:95], a[72:75], v[208:211]\n"
"v_lshrrev_b32 v62, 2, v61\n"
"v_mfma_f32_16x16x32_bf16 v[212:215], a[84:87], a[76:79], v[212:215]\n"
"v_mad_u32_u24 v61, v62, 12, v61\n"
"v_mfma_f32_16x16x32_bf16 v[216:219], a[88:91], a[76:79], v[216:219]\n"
"v_mul_u32_u24 v61, 1280, v61\n"
"v_mfma_f32_16x16x32_bf16 v[220:223], a[92:95], a[76:79], v[220:223]\n"
"v_lshl_add_u32 v60, v60, 4, v61\n"
"v_mfma_f32_16x16x32_bf16 v[224:227], a[84:87], a[80:83], v[224:227]\n"
"s_add_u32 m0, s53, 22528\n"
"s_add_u32 s60, s74, 7\n"
"s_and_b32 s60, s60, 7\n"
"v_mfma_f32_16x16x32_bf16 v[228:231], a[88:91], a[80:83], v[228:231]\n"
"s_mul_i32 s62, s60, 786432\n"
"v_add_u32 v60, s19, v60\n"
"v_mfma_f32_16x16x32_bf16 v[232:235], a[92:95], a[80:83], v[232:235]\n"
"s_waitcnt vmcnt(11) lgkmcnt(0)\n"
"s_barrier\n"
"ds_read_b64_tr_b16 a[84:85], v38 offset:25600\n"
"v_mfma_f32_16x16x32_bf16 v[128:131], a[180:183], a[144:147], v[128:131]\n"
"buffer_load_dwordx4 v46, s[28:31], s62 offen lds\n"
"v_mfma_f32_16x16x32_bf16 v[132:135], a[184:187], a[144:147], v[132:135]\n"
"ds_read_b64_tr_b16 a[86:87], v38 offset:26112\n"
"v_subrev_u32 v60, 2048, v60\n"
"v_mfma_f32_16x16x32_bf16 v[136:139], a[188:191], a[144:147], v[136:139]\n"
"ds_read_b64_tr_b16 a[88:89], v39 offset:25600\n"
"v_accvgpr_write_b32 a124, v60\n"
"v_mfma_f32_16x16x32_bf16 v[140:143], a[180:183], a[148:151], v[140:143]\n"
"ds_read_b64_tr_b16 a[90:91], v39 offset:26112\n"
"v_add_u32 v60, 192, v0\n"
"v_mfma_f32_16x16x32_bf16 v[144:147], a[184:187], a[148:151], v[144:147]\n"
"buffer_load_dwordx4 v47, s[28:31], s62 offen offset:1024 lds\n"
"v_mfma_f32_16x16x32_bf16 v[148:151], a[188:191], a[148:151], v[148:151]\n"
"ds_read_b64_tr_b16 a[92:93], v40 offset:25600\n"
"v_mul_u32_u24 v61, 205, v60\n"
"v_mfma_f32_16x16x32_bf16 v[152:155], a[180:183], a[152:155], v[152:155]\n"
"ds_read_b64_tr_b16 a[94:95], v40 offset:26112\n"
"v_lshrrev_b32 v61, 12, v61\n"
"v_mfma_f32_16x16x32_bf16 v[156:159], a[184:187], a[152:155], v[156:159]\n"
"ds_read_b128 a[48:51], v29 offset:9216\n"
"v_mul_u32_u24 v62, 20, v61\n"
"v_mfma_f32_16x16x32_bf16 v[160:163], a[188:191], a[152:155], v[160:163]\n"
"buffer_load_dwordx4 v48, s[28:31], s62 offen offset:2048 lds\n"
"v_mfma_f32_16x16x32_bf16 v[164:167], a[180:183], a[156:159], v[164:167]\n"
"s_add_u32 m0, s53, 4608\n"
"ds_read_b128 a[52:55], v29 offset:11264\n"
"s_lshl_b32 s61, s60, 7\n"
"v_mfma_f32_16x16x32_bf16 v[168:171], a[184:187], a[156:159], v[168:171]\n"
"ds_read_b128 a[56:59], v31 offset:9216\n"
"v_sub_u32 v60, v60, v62\n"
"v_mfma_f32_16x16x32_bf16 v[172:175], a[188:191], a[156:159], v[172:175]\n"
"ds_read_b128 a[60:63], v31 offset:11264\n"
"v_xor_b32 v60, s18, v60\n"
"v_mfma_f32_16x16x32_bf16 v[176:179], a[180:183], a[160:163], v[176:179]\n"
"buffer_load_dwordx4 v41, s[24:27], s61 offen lds\n"
"v_mfma_f32_16x16x32_bf16 v[180:183], a[184:187], a[160:163], v[180:183]\n"
"ds_read_b128 a[64:67], v33 offset:9216\n"
"v_lshrrev_b32 v62, 2, v61\n"
"v_mfma_f32_16x16x32_bf16 v[184:187], a[188:191], a[160:163], v[184:187]\n"
"ds_read_b128 a[68:71], v33 offset:11264\n"
"v_mad_u32_u24 v61, v62, 12, v61\n"
"v_mfma_f32_16x16x32_bf16 v[188:191], a[180:183], a[164:167], v[188:191]\n"
"ds_read_b128 a[72:75], v35 offset:9216\n"
"v_mul_u32_u24 v61, 1280, v61\n"
"v_mfma_f32_16x16x32_bf16 v[192:195], a[184:187], a[164:167], v[192:195]\n"
"ds_read_b128 a[76:79], v35 offset:11264\n"
"v_lshl_add_u32 v60, v60, 4, v61\n"
"v_mfma_f32_16x16x32_bf16 v[196:199], a[188:191], a[164:167], v[196:199]\n"
"ds_read_b128 a[80:83], v37 offset:9216\n"
"v_add_u32 v60, s19, v60\n"
"v_mfma_f32_16x16x32_bf16 v[200:203], a[180:183], a[168:171], v[200:203]\n"
"buffer_load_dwordx4 v42, s[24:27], s61 offen offset:1024 lds\n"
"v_mfma_f32_16x16x32_bf16 v[204:207], a[184:187], a[168:171], v[204:207]\n"
"v_subrev_u32 v60, 3072, v60\n"
"v_mfma_f32_16x16x32_bf16 v[208:211], a[188:191], a[168:171], v[208:211]\n"
"v_accvgpr_write_b32 a125, v60\n"
"v_mfma_f32_16x16x32_bf16 v[212:215], a[180:183], a[172:175], v[212:215]\n"
"v_add_u32 v60, 256, v0\n"
"v_mfma_f32_16x16x32_bf16 v[216:219], a[184:187], a[172:175], v[216:219]\n"
"buffer_load_dwordx4 v43, s[24:27], s61 offen offset:2048 lds\n"
"v_mfma_f32_16x16x32_bf16 v[220:223], a[188:191], a[172:175], v[220:223]\n"
"v_mul_u32_u24 v61, 205, v60\n"
"v_mfma_f32_16x16x32_bf16 v[224:227], a[180:183], a[176:179], v[224:227]\n"
"v_lshrrev_b32 v61, 12, v61\n"
"v_mfma_f32_16x16x32_bf16 v[228:231], a[184:187], a[176:179], v[228:231]\n"
"v_mul_u32_u24 v62, 20, v61\n"
"v_mfma_f32_16x16x32_bf16 v[232:235], a[188:191], a[176:179], v[232:235]\n"
"s_waitcnt vmcnt(9) lgkmcnt(0)\n"
"s_barrier\n"
"ds_read_b64_tr_b16 a[180:181], v38 offset:16384\n"
"v_mfma_f32_16x16x32_bf16 v[128:131], a[84:87], a[48:51], v[128:131]\n"
"buffer_load_dwordx4 v44, s[24:27], s61 offen offset:3072 lds\n"
"v_mfma_f32_16x16x32_bf16 v[132:135], a[88:91], a[48:51], v[132:135]\n"
"s_add_u32 m0, s53, 8704\n"
"ds_read_b64_tr_b16 a[182:183], v38 offset:16896\n"
"v_sub_u32 v60, v60, v62\n"
"v_mfma_f32_16x16x32_bf16 v[136:139], a[92:95], a[48:51], v[136:139]\n"
"ds_read_b64_tr_b16 a[184:185], v39 offset:16384\n"
"v_xor_b32 v60, s18, v60\n"
"v_mfma_f32_16x16x32_bf16 v[140:143], a[84:87], a[52:55], v[140:143]\n"
"ds_read_b64_tr_b16 a[186:187], v39 offset:16896\n"
"v_lshrrev_b32 v62, 2, v61\n"
"v_mfma_f32_16x16x32_bf16 v[144:147], a[88:91], a[52:55], v[144:147]\n"
"s_mov_b32 exec_hi, 0\n"
"buffer_load_dwordx4 v45, s[24:27], s61 offen lds\n"
"s_mov_b32 exec_hi, -1\n"
"v_mfma_f32_16x16x32_bf16 v[148:151], a[92:95], a[52:55], v[148:151]\n"
"s_add_u32 m0, s53, 25600\n"
"ds_read_b64_tr_b16 a[188:189], v40 offset:16384\n"
"s_add_u32 s62, s62, 393216\n"
"v_mfma_f32_16x16x32_bf16 v[152:155], a[84:87], a[56:59], v[152:155]\n"
"ds_read_b64_tr_b16 a[190:191], v40 offset:16896\n"
"v_mad_u32_u24 v61, v62, 12, v61\n"
"v_mfma_f32_16x16x32_bf16 v[156:159], a[88:91], a[56:59], v[156:159]\n"
"ds_read_b128 a[144:147], v28 offset:0\n"
"v_mul_u32_u24 v61, 1280, v61\n"
"v_mfma_f32_16x16x32_bf16 v[160:163], a[92:95], a[56:59], v[160:163]\n"
"buffer_load_dwordx4 v46, s[28:31], s62 offen lds\n"
"v_mfma_f32_16x16x32_bf16 v[164:167], a[84:87], a[60:63], v[164:167]\n"
"ds_read_b128 a[148:151], v28 offset:2048\n"
"v_lshl_add_u32 v60, v60, 4, v61\n"
"v_mfma_f32_16x16x32_bf16 v[168:171], a[88:91], a[60:63], v[168:171]\n"
"ds_read_b128 a[152:155], v30 offset:0\n"
"v_add_u32 v60, s19, v60\n"
"v_mfma_f32_16x16x32_bf16 v[172:175], a[92:95], a[60:63], v[172:175]\n"
"ds_read_b128 a[156:159], v30 offset:2048\n"
"v_accvgpr_write_b32 a126, v60\n"
"v_mfma_f32_16x16x32_bf16 v[176:179], a[84:87], a[64:67], v[176:179]\n"
"s_and_b32 s18, s70, 0xffff3fff\n"
"buffer_load_dwordx4 v47, s[28:31], s62 offen offset:1024 lds\n"
"v_mfma_f32_16x16x32_bf16 v[180:183], a[88:91], a[64:67], v[180:183]\n"
"ds_read_b128 a[160:163], v32 offset:0\n"
"v_lshlrev_b32 v60, 14, v59\n"
"v_mfma_f32_16x16x32_bf16 v[184:187], a[92:95], a[64:67], v[184:187]\n"
"ds_read_b128 a[164:167], v32 offset:2048\n"
"v_add_u32 v60, s18, v60\n"
"v_mfma_f32_16x16x32_bf16 v[188:191], a[84:87], a[68:71], v[188:191]\n"
"ds_read_b128 a[168:171], v34 offset:0\n"
"v_accvgpr_write_b32 a127, v60\n"
"v_mfma_f32_16x16x32_bf16 v[192:195], a[88:91], a[68:71], v[192:195]\n"
"ds_read_b128 a[172:175], v34 offset:2048\n"
"v_mfma_f32_16x16x32_bf16 v[196:199], a[92:95], a[68:71], v[196:199]\n"
"buffer_load_dwordx4 v48, s[28:31], s62 offen offset:2048 lds\n"
"v_mfma_f32_16x16x32_bf16 v[200:203], a[84:87], a[72:75], v[200:203]\n"
"ds_read_b128 a[176:179], v36 offset:0\n"
"v_mfma_f32_16x16x32_bf16 v[204:207], a[88:91], a[72:75], v[204:207]\n"
"v_mfma_f32_16x16x32_bf16 v[208:211], a[92:95], a[72:75], v[208:211]\n"
"v_mfma_f32_16x16x32_bf16 v[212:215], a[84:87], a[76:79], v[212:215]\n"
"v_mfma_f32_16x16x32_bf16 v[216:219], a[88:91], a[76:79], v[216:219]\n"
"v_mfma_f32_16x16x32_bf16 v[220:223], a[92:95], a[76:79], v[220:223]\n"
"v_mfma_f32_16x16x32_bf16 v[224:227], a[84:87], a[80:83], v[224:227]\n"
"s_add_u32 m0, s53, 16384\n"
"s_mov_b32 s60, 8\n"
"s_mul_i32 s62, s60, 786432\n"
"v_mfma_f32_16x16x32_bf16 v[228:231], a[88:91], a[80:83], v[228:231]\n"
"v_mfma_f32_16x16x32_bf16 v[232:235], a[92:95], a[80:83], v[232:235]\n"
"s_waitcnt vmcnt(11) lgkmcnt(0)\n"
"s_barrier\n"
"ds_read_b64_tr_b16 a[84:85], v38 offset:19456\n"
"v_mfma_f32_16x16x32_bf16 v[128:131], a[180:183], a[144:147], v[128:131]\n"
"buffer_load_dwordx4 v46, s[28:31], s62 offen lds\n"
"v_mfma_f32_16x16x32_bf16 v[132:135], a[184:187], a[144:147], v[132:135]\n"
"ds_read_b64_tr_b16 a[86:87], v38 offset:19968\n"
"v_mfma_f32_16x16x32_bf16 v[136:139], a[188:191], a[144:147], v[136:139]\n"
"ds_read_b64_tr_b16 a[88:89], v39 offset:19456\n"
"v_mfma_f32_16x16x32_bf16 v[140:143], a[180:183], a[148:151], v[140:143]\n"
"ds_read_b64_tr_b16 a[90:91], v39 offset:19968\n"
"v_mfma_f32_16x16x32_bf16 v[144:147], a[184:187], a[148:151], v[144:147]\n"
"buffer_load_dwordx4 v47, s[28:31], s62 offen offset:1024 lds\n"
"v_mfma_f32_16x16x32_bf16 v[148:151], a[188:191], a[148:151], v[148:151]\n"
"ds_read_b64_tr_b16 a[92:93], v40 offset:19456\n"
"v_mfma_f32_16x16x32_bf16 v[152:155], a[180:183], a[152:155], v[152:155]\n"
"ds_read_b64_tr_b16 a[94:95], v40 offset:19968\n"
"v_mfma_f32_16x16x32_bf16 v[156:159], a[184:187], a[152:155], v[156:159]\n"
"ds_read_b128 a[48:51], v29 offset:0\n"
"v_mfma_f32_16x16x32_bf16 v[160:163], a[188:191], a[152:155], v[160:163]\n"
"buffer_load_dwordx4 v48, s[28:31], s62 offen offset:2048 lds\n"
"v_mfma_f32_16x16x32_bf16 v[164:167], a[180:183], a[156:159], v[164:167]\n"
"s_add_u32 m0, s53, 9216\n"
"ds_read_b128 a[52:55], v29 offset:2048\n"
"s_lshl_b32 s61, s60, 7\n"
"v_mfma_f32_16x16x32_bf16 v[168:171], a[184:187], a[156:159], v[168:171]\n"
"ds_read_b128 a[56:59], v31 offset:0\n"
"v_mfma_f32_16x16x32_bf16 v[172:175], a[188:191], a[156:159], v[172:175]\n"
"ds_read_b128 a[60:63], v31 offset:2048\n"
"v_mfma_f32_16x16x32_bf16 v[176:179], a[180:183], a[160:163], v[176:179]\n"
"buffer_load_dwordx4 v41, s[24:27], s61 offen lds\n"
"v_mfma_f32_16x16x32_bf16 v[180:183], a[184:187], a[160:163], v[180:183]\n"
"ds_read_b128 a[64:67], v33 offset:0\n"
"v_mfma_f32_16x16x32_bf16 v[184:187], a[188:191], a[160:163], v[184:187]\n"
"ds_read_b128 a[68:71], v33 offset:2048\n"
"v_mfma_f32_16x16x32_bf16 v[188:191], a[180:183], a[164:167], v[188:191]\n"
"ds_read_b128 a[72:75], v35 offset:0\n"
"v_mfma_f32_16x16x32_bf16 v[192:195], a[184:187], a[164:167], v[192:195]\n"
"ds_read_b128 a[76:79], v35 offset:2048\n"
"v_mfma_f32_16x16x32_bf16 v[196:199], a[188:191], a[164:167], v[196:199]\n"
"ds_read_b128 a[80:83], v37 offset:0\n"
"v_mfma_f32_16x16x32_bf16 v[200:203], a[180:183], a[168:171], v[200:203]\n"
"buffer_load_dwordx4 v42, s[24:27], s61 offen offset:1024 lds\n"
"v_mfma_f32_16x16x32_bf16 v[204:207], a[184:187], a[168:171], v[204:207]\n"
"v_mfma_f32_16x16x32_bf16 v[208:211], a[188:191], a[168:171], v[208:211]\n"
"v_mfma_f32_16x16x32_bf16 v[212:215], a[180:183], a[172:175], v[212:215]\n"
"v_mfma_f32_16x16x32_bf16 v[216:219], a[184:187], a[172:175], v[216:219]\n"
"buffer_load_dwordx4 v43, s[24:27], s61 offen offset:2048 lds\n"
"v_mfma_f32_16x16x32_bf16 v[220:223], a[188:191], a[172:175], v[220:223]\n"
"v_mfma_f32_16x16x32_bf16 v[224:227], a[180:183], a[176:179], v[224:227]\n"
"v_mfma_f32_16x16x32_bf16 v[228:231], a[184:187], a[176:179], v[228:231]\n"
"v_mfma_f32_16x16x32_bf16 v[232:235], a[188:191], a[176:179], v[232:235]\n"
"s_waitcnt vmcnt(9) lgkmcnt(0)\n"
"s_barrier\n"
"ds_read_b64_tr_b16 a[180:181], v38 offset:22528\n"
"v_mfma_f32_16x16x32_bf16 v[128:131], a[84:87], a[48:51], v[128:131]\n"
"buffer_load_dwordx4 v44, s[24:27], s61 offen offset:3072 lds\n"
"v_mfma_f32_16x16x32_bf16 v[132:135], a[88:91], a[48:51], v[132:135]\n"
"s_add_u32 m0, s53, 13312\n"
"ds_read_b64_tr_b16 a[182:183], v38 offset:23040\n"
"v_mfma_f32_16x16x32_bf16 v[136:139], a[92:95], a[48:51], v[136:139]\n"
"ds_read_b64_tr_b16 a[184:185], v39 offset:22528\n"
"v_mfma_f32_16x16x32_bf16 v[140:143], a[84:87], a[52:55], v[140:143]\n"
"ds_read_b64_tr_b16 a[186:187], v39 offset:23040\n"
"v_mfma_f32_16x16x32_bf16 v[144:147], a[88:91], a[52:55], v[144:147]\n"
"s_mov_b32 exec_hi, 0\n"
"buffer_load_dwordx4 v45, s[24:27], s61 offen lds\n"
"s_mov_b32 exec_hi, -1\n"
"v_mfma_f32_16x16x32_bf16 v[148:151], a[92:95], a[52:55], v[148:151]\n"
"s_add_u32 m0, s53, 19456\n"
"ds_read_b64_tr_b16 a[188:189], v40 offset:22528\n"
"s_add_u32 s62, s62, 393216\n"
"v_mfma_f32_16x16x32_bf16 v[152:155], a[84:87], a[56:59], v[152:155]\n"
"ds_read_b64_tr_b16 a[190:191], v40 offset:23040\n"
"v_mfma_f32_16x16x32_bf16 v[156:159], a[88:91], a[56:59], v[156:159]\n"
"ds_read_b128 a[144:147], v28 offset:4608\n"
"v_mfma_f32_16x16x32_bf16 v[160:163], a[92:95], a[56:59], v[160:163]\n"
"buffer_load_dwordx4 v46, s[28:31], s62 offen lds\n"
"v_mfma_f32_16x16x32_bf16 v[164:167], a[84:87], a[60:63], v[164:167]\n"
"ds_read_b128 a[148:151], v28 offset:6656\n"
"v_mfma_f32_16x16x32_bf16 v[168:171], a[88:91], a[60:63], v[168:171]\n"
"ds_read_b128 a[152:155], v30 offset:4608\n"
"v_mfma_f32_16x16x32_bf16 v[172:175], a[92:95], a[60:63], v[172:175]\n"
"ds_read_b128 a[156:159], v30 offset:6656\n"
"v_mfma_f32_16x16x32_bf16 v[176:179], a[84:87], a[64:67], v[176:179]\n"
"buffer_load_dwordx4 v47, s[28:31], s62 offen offset:1024 lds\n"
"v_mfma_f32_16x16x32_bf16 v[180:183], a[88:91], a[64:67], v[180:183]\n"
"ds_read_b128 a[160:163], v32 offset:4608\n"
"v_mfma_f32_16x16x32_bf16 v[184:187], a[92:95], a[64:67], v[184:187]\n"
"ds_read_b128 a[164:167], v32 offset:6656\n"
"v_mfma_f32_16x16x32_bf16 v[188:191], a[84:87], a[68:71], v[188:191]\n"
"ds_read_b128 a[168:171], v34 offset:4608\n"
"v_mfma_f32_16x16x32_bf16 v[192:195], a[88:91], a[68:71], v[192:195]\n"
"ds_read_b128 a[172:175], v34 offset:6656\n"
"v_mfma_f32_16x16x32_bf16 v[196:199], a[92:95], a[68:71], v[196:199]\n"
"buffer_load_dwordx4 v48, s[28:31], s62 offen offset:2048 lds\n"
"v_mfma_f32_16x16x32_bf16 v[200:203], a[84:87], a[72:75], v[200:203]\n"
"ds_read_b128 a[176:179], v36 offset:4608\n"
"v_mfma_f32_16x16x32_bf16 v[204:207], a[88:91], a[72:75], v[204:207]\n"
"v_mfma_f32_16x16x32_bf16 v[208:211], a[92:95], a[72:75], v[208:211]\n"
"v_mfma_f32_16x16x32_bf16 v[212:215], a[84:87], a[76:79], v[212:215]\n"
"v_mfma_f32_16x16x32_bf16 v[216:219], a[88:91], a[76:79], v[216:219]\n"
"v_mfma_f32_16x16x32_bf16 v[220:223], a[92:95], a[76:79], v[220:223]\n"
"v_mfma_f32_16x16x32_bf16 v[224:227], a[84:87], a[80:83], v[224:227]\n"
"s_add_u32 m0, s53, 22528\n"
"s_mov_b32 s60, 9\n"
"s_mul_i32 s62, s60, 786432\n"
"v_mfma_f32_16x16x32_bf16 v[228:231], a[88:91], a[80:83], v[228:231]\n"
"v_mfma_f32_16x16x32_bf16 v[232:235], a[92:95], a[80:83], v[232:235]\n"
"s_waitcnt vmcnt(11) lgkmcnt(0)\n"
"s_barrier\n"
"ds_read_b64_tr_b16 a[84:85], v38 offset:25600\n"
"v_mfma_f32_16x16x32_bf16 v[128:131], a[180:183], a[144:147], v[128:131]\n"
"buffer_load_dwordx4 v46, s[28:31], s62 offen lds\n"
"v_mfma_f32_16x16x32_bf16 v[132:135], a[184:187], a[144:147], v[132:135]\n"
"ds_read_b64_tr_b16 a[86:87], v38 offset:26112\n"
"v_mfma_f32_16x16x32_bf16 v[136:139], a[188:191], a[144:147], v[136:139]\n"
"ds_read_b64_tr_b16 a[88:89], v39 offset:25600\n"
"v_mfma_f32_16x16x32_bf16 v[140:143], a[180:183], a[148:151], v[140:143]\n"
"ds_read_b64_tr_b16 a[90:91], v39 offset:26112\n"
"v_mfma_f32_16x16x32_bf16 v[144:147], a[184:187], a[148:151], v[144:147]\n"
"buffer_load_dwordx4 v47, s[28:31], s62 offen offset:1024 lds\n"
"v_mfma_f32_16x16x32_bf16 v[148:151], a[188:191], a[148:151], v[148:151]\n"
"ds_read_b64_tr_b16 a[92:93], v40 offset:25600\n"
"v_mfma_f32_16x16x32_bf16 v[152:155], a[180:183], a[152:155], v[152:155]\n"
"ds_read_b64_tr_b16 a[94:95], v40 offset:26112\n"
"v_mfma_f32_16x16x32_bf16 v[156:159], a[184:187], a[152:155], v[156:159]\n"
"ds_read_b128 a[48:51], v29 offset:4608\n"
"v_mfma_f32_16x16x32_bf16 v[160:163], a[188:191], a[152:155], v[160:163]\n"
"buffer_load_dwordx4 v48, s[28:31], s62 offen offset:2048 lds\n"
"v_mfma_f32_16x16x32_bf16 v[164:167], a[180:183], a[156:159], v[164:167]\n"
"s_add_u32 m0, s53, 0\n"
"ds_read_b128 a[52:55], v29 offset:6656\n"
"s_lshl_b32 s61, s60, 7\n"
"v_mfma_f32_16x16x32_bf16 v[168:171], a[184:187], a[156:159], v[168:171]\n"
"ds_read_b128 a[56:59], v31 offset:4608\n"
"v_mfma_f32_16x16x32_bf16 v[172:175], a[188:191], a[156:159], v[172:175]\n"
"ds_read_b128 a[60:63], v31 offset:6656\n"
"v_mfma_f32_16x16x32_bf16 v[176:179], a[180:183], a[160:163], v[176:179]\n"
"buffer_load_dwordx4 v41, s[24:27], s61 offen lds\n"
"v_mfma_f32_16x16x32_bf16 v[180:183], a[184:187], a[160:163], v[180:183]\n"
"ds_read_b128 a[64:67], v33 offset:4608\n"
"v_mfma_f32_16x16x32_bf16 v[184:187], a[188:191], a[160:163], v[184:187]\n"
"ds_read_b128 a[68:71], v33 offset:6656\n"
"v_mfma_f32_16x16x32_bf16 v[188:191], a[180:183], a[164:167], v[188:191]\n"
"ds_read_b128 a[72:75], v35 offset:4608\n"
"v_mfma_f32_16x16x32_bf16 v[192:195], a[184:187], a[164:167], v[192:195]\n"
"ds_read_b128 a[76:79], v35 offset:6656\n"
"v_mfma_f32_16x16x32_bf16 v[196:199], a[188:191], a[164:167], v[196:199]\n"
"ds_read_b128 a[80:83], v37 offset:4608\n"
"v_mfma_f32_16x16x32_bf16 v[200:203], a[180:183], a[168:171], v[200:203]\n"
"buffer_load_dwordx4 v42, s[24:27], s61 offen offset:1024 lds\n"
"v_mfma_f32_16x16x32_bf16 v[204:207], a[184:187], a[168:171], v[204:207]\n"
"v_mfma_f32_16x16x32_bf16 v[208:211], a[188:191], a[168:171], v[208:211]\n"
"v_mfma_f32_16x16x32_bf16 v[212:215], a[180:183], a[172:175], v[212:215]\n"
"v_mfma_f32_16x16x32_bf16 v[216:219], a[184:187], a[172:175], v[216:219]\n"
"buffer_load_dwordx4 v43, s[24:27], s61 offen offset:2048 lds\n"
"v_mfma_f32_16x16x32_bf16 v[220:223], a[188:191], a[172:175], v[220:223]\n"
"v_mfma_f32_16x16x32_bf16 v[224:227], a[180:183], a[176:179], v[224:227]\n"
"v_mfma_f32_16x16x32_bf16 v[228:231], a[184:187], a[176:179], v[228:231]\n"
"v_mfma_f32_16x16x32_bf16 v[232:235], a[188:191], a[176:179], v[232:235]\n"
"s_waitcnt vmcnt(9) lgkmcnt(0)\n"
"s_barrier\n"
"ds_read_b64_tr_b16 a[180:181], v38 offset:16384\n"
"v_mfma_f32_16x16x32_bf16 v[128:131], a[84:87], a[48:51], v[128:131]\n"
"buffer_load_dwordx4 v44, s[24:27], s61 offen offset:3072 lds\n"
"v_mfma_f32_16x16x32_bf16 v[132:135], a[88:91], a[48:51], v[132:135]\n"
"s_add_u32 m0, s53, 4096\n"
"ds_read_b64_tr_b16 a[182:183], v38 offset:16896\n"
"v_mfma_f32_16x16x32_bf16 v[136:139], a[92:95], a[48:51], v[136:139]\n"
"ds_read_b64_tr_b16 a[184:185], v39 offset:16384\n"
"v_mfma_f32_16x16x32_bf16 v[140:143], a[84:87], a[52:55], v[140:143]\n"
"ds_read_b64_tr_b16 a[186:187], v39 offset:16896\n"
"v_mfma_f32_16x16x32_bf16 v[144:147], a[88:91], a[52:55], v[144:147]\n"
"s_mov_b32 exec_hi, 0\n"
"buffer_load_dwordx4 v45, s[24:27], s61 offen lds\n"
"s_mov_b32 exec_hi, -1\n"
"v_mfma_f32_16x16x32_bf16 v[148:151], a[92:95], a[52:55], v[148:151]\n"
"s_add_u32 m0, s53, 25600\n"
"ds_read_b64_tr_b16 a[188:189], v40 offset:16384\n"
"s_add_u32 s62, s62, 393216\n"
"v_mfma_f32_16x16x32_bf16 v[152:155], a[84:87], a[56:59], v[152:155]\n"
"ds_read_b64_tr_b16 a[190:191], v40 offset:16896\n"
"v_mfma_f32_16x16x32_bf16 v[156:159], a[88:91], a[56:59], v[156:159]\n"
"ds_read_b128 a[144:147], v28 offset:9216\n"
"v_mfma_f32_16x16x32_bf16 v[160:163], a[92:95], a[56:59], v[160:163]\n"
"buffer_load_dwordx4 v46, s[28:31], s62 offen lds\n"
"v_mfma_f32_16x16x32_bf16 v[164:167], a[84:87], a[60:63], v[164:167]\n"
"ds_read_b128 a[148:151], v28 offset:11264\n"
"v_mfma_f32_16x16x32_bf16 v[168:171], a[88:91], a[60:63], v[168:171]\n"
"ds_read_b128 a[152:155], v30 offset:9216\n"
"v_mfma_f32_16x16x32_bf16 v[172:175], a[92:95], a[60:63], v[172:175]\n"
"ds_read_b128 a[156:159], v30 offset:11264\n"
"v_mfma_f32_16x16x32_bf16 v[176:179], a[84:87], a[64:67], v[176:179]\n"
"buffer_load_dwordx4 v47, s[28:31], s62 offen offset:1024 lds\n"
"v_mfma_f32_16x16x32_bf16 v[180:183], a[88:91], a[64:67], v[180:183]\n"
"ds_read_b128 a[160:163], v32 offset:9216\n"
"v_mfma_f32_16x16x32_bf16 v[184:187], a[92:95], a[64:67], v[184:187]\n"
"ds_read_b128 a[164:167], v32 offset:11264\n"
"v_mfma_f32_16x16x32_bf16 v[188:191], a[84:87], a[68:71], v[188:191]\n"
"ds_read_b128 a[168:171], v34 offset:9216\n"
"v_mfma_f32_16x16x32_bf16 v[192:195], a[88:91], a[68:71], v[192:195]\n"
"ds_read_b128 a[172:175], v34 offset:11264\n"
"v_mfma_f32_16x16x32_bf16 v[196:199], a[92:95], a[68:71], v[196:199]\n"
"buffer_load_dwordx4 v48, s[28:31], s62 offen offset:2048 lds\n"
"v_mfma_f32_16x16x32_bf16 v[200:203], a[84:87], a[72:75], v[200:203]\n"
"ds_read_b128 a[176:179], v36 offset:9216\n"
"v_mfma_f32_16x16x32_bf16 v[204:207], a[88:91], a[72:75], v[204:207]\n"
"v_mfma_f32_16x16x32_bf16 v[208:211], a[92:95], a[72:75], v[208:211]\n"
"buffer_load_dwordx4 a[140:143], v3, s[32:35], s63 offen offset:1024 nt\n"
"v_mfma_f32_16x16x32_bf16 v[212:215], a[84:87], a[76:79], v[212:215]\n"
"buffer_load_dwordx4 a[244:247], v4, s[32:35], s63 offen offset:2048 nt\n"
"v_mfma_f32_16x16x32_bf16 v[216:219], a[88:91], a[76:79], v[216:219]\n"
"buffer_load_dwordx4 a[248:251], v5, s[32:35], s63 offen offset:3072 nt\n"
"v_mfma_f32_16x16x32_bf16 v[220:223], a[92:95], a[76:79], v[220:223]\n"
"buffer_load_dwordx4 a[252:255], v6, s[32:35], s63 offen offset:0 nt\n"
"v_mfma_f32_16x16x32_bf16 v[224:227], a[84:87], a[80:83], v[224:227]\n"
"buffer_load_dwordx4 v[252:255], v7, s[32:35], s63 offen offset:1024 nt\n"
"v_mfma_f32_16x16x32_bf16 v[228:231], a[88:91], a[80:83], v[228:231]\n"
"v_mfma_f32_16x16x32_bf16 v[232:235], a[92:95], a[80:83], v[232:235]\n"
"s_waitcnt vmcnt(16) lgkmcnt(0)\n"
"s_barrier\n"
"ds_read_b64_tr_b16 a[84:85], v38 offset:19456\n"
"v_mfma_f32_16x16x32_bf16 v[128:131], a[180:183], a[144:147], v[128:131]\n"
"ds_read_b64_tr_b16 a[86:87], v38 offset:19968\n"
"v_mfma_f32_16x16x32_bf16 v[132:135], a[184:187], a[144:147], v[132:135]\n"
"ds_read_b64_tr_b16 a[88:89], v39 offset:19456\n"
"v_mfma_f32_16x16x32_bf16 v[136:139], a[188:191], a[144:147], v[136:139]\n"
"ds_read_b64_tr_b16 a[90:91], v39 offset:19968\n"
"v_mfma_f32_16x16x32_bf16 v[140:143], a[180:183], a[148:151], v[140:143]\n"
"ds_read_b64_tr_b16 a[92:93], v40 offset:19456\n"
"v_mfma_f32_16x16x32_bf16 v[144:147], a[184:187], a[148:151], v[144:147]\n"
"ds_read_b64_tr_b16 a[94:95], v40 offset:19968\n"
"v_mfma_f32_16x16x32_bf16 v[148:151], a[188:191], a[148:151], v[148:151]\n"
"ds_read_b128 a[48:51], v29 offset:9216\n"
"v_mfma_f32_16x16x32_bf16 v[152:155], a[180:183], a[152:155], v[152:155]\n"
"ds_read_b128 a[52:55], v29 offset:11264\n"
"v_mfma_f32_16x16x32_bf16 v[156:159], a[184:187], a[152:155], v[156:159]\n"
"ds_read_b128 a[56:59], v31 offset:9216\n"
"v_mfma_f32_16x16x32_bf16 v[160:163], a[188:191], a[152:155], v[160:163]\n"
"ds_read_b128 a[60:63], v31 offset:11264\n"
"v_mfma_f32_16x16x32_bf16 v[164:167], a[180:183], a[156:159], v[164:167]\n"
"ds_read_b128 a[64:67], v33 offset:9216\n"
"v_mfma_f32_16x16x32_bf16 v[168:171], a[184:187], a[156:159], v[168:171]\n"
"ds_read_b128 a[68:71], v33 offset:11264\n"
"v_mfma_f32_16x16x32_bf16 v[172:175], a[188:191], a[156:159], v[172:175]\n"
"ds_read_b128 a[72:75], v35 offset:9216\n"
"v_mfma_f32_16x16x32_bf16 v[176:179], a[180:183], a[160:163], v[176:179]\n"
"ds_read_b128 a[76:79], v35 offset:11264\n"
"v_mfma_f32_16x16x32_bf16 v[180:183], a[184:187], a[160:163], v[180:183]\n"
"ds_read_b128 a[80:83], v37 offset:9216\n"
"v_mfma_f32_16x16x32_bf16 v[184:187], a[188:191], a[160:163], v[184:187]\n"
"v_mfma_f32_16x16x32_bf16 v[188:191], a[180:183], a[164:167], v[188:191]\n"
"v_mfma_f32_16x16x32_bf16 v[192:195], a[184:187], a[164:167], v[192:195]\n"
"v_mfma_f32_16x16x32_bf16 v[196:199], a[188:191], a[164:167], v[196:199]\n"
"v_mfma_f32_16x16x32_bf16 v[200:203], a[180:183], a[168:171], v[200:203]\n"
"v_mfma_f32_16x16x32_bf16 v[204:207], a[184:187], a[168:171], v[204:207]\n"
"v_mfma_f32_16x16x32_bf16 v[208:211], a[188:191], a[168:171], v[208:211]\n"
"v_mfma_f32_16x16x32_bf16 v[212:215], a[180:183], a[172:175], v[212:215]\n"
"v_mfma_f32_16x16x32_bf16 v[216:219], a[184:187], a[172:175], v[216:219]\n"
"v_mfma_f32_16x16x32_bf16 v[220:223], a[188:191], a[172:175], v[220:223]\n"
"v_mfma_f32_16x16x32_bf16 v[224:227], a[180:183], a[176:179], v[224:227]\n"
"s_add_u32 m0, s53, 16384\n"
"s_mov_b32 s63, 49152\n"
"v_mfma_f32_16x16x32_bf16 v[228:231], a[184:187], a[176:179], v[228:231]\n"
"v_mfma_f32_16x16x32_bf16 v[232:235], a[188:191], a[176:179], v[232:235]\n"
"s_waitcnt vmcnt(5) lgkmcnt(0)\n"
"s_barrier\n"
"ds_read_b64_tr_b16 a[180:181], v38 offset:22528\n"
"ds_read_b64_tr_b16 a[182:183], v38 offset:23040\n"
"v_mfma_f32_16x16x32_bf16 v[128:131], a[84:87], a[48:51], v[128:131]\n"
"ds_read_b64_tr_b16 a[184:185], v39 offset:22528\n"
"v_mfma_f32_16x16x32_bf16 v[132:135], a[88:91], a[48:51], v[132:135]\n"
"ds_read_b64_tr_b16 a[186:187], v39 offset:23040\n"
"v_mfma_f32_16x16x32_bf16 v[136:139], a[92:95], a[48:51], v[136:139]\n"
"ds_read_b64_tr_b16 a[188:189], v40 offset:22528\n"
"ds_read_b64_tr_b16 a[190:191], v40 offset:23040\n"
"buffer_load_dwordx4 v2, s[32:35], s63 offen nt lds\n"
"v_mfma_f32_16x16x32_bf16 v[140:143], a[84:87], a[52:55], v[140:143]\n"
"ds_read_b128 a[144:147], v28 offset:0\n"
"v_mfma_f32_16x16x32_bf16 v[144:147], a[88:91], a[52:55], v[144:147]\n"
"ds_read_b128 a[148:151], v28 offset:2048\n"
"v_mfma_f32_16x16x32_bf16 v[148:151], a[92:95], a[52:55], v[148:151]\n"
"ds_read_b128 a[152:155], v30 offset:0\n"
"ds_read_b128 a[156:159], v30 offset:2048\n"
"v_mfma_f32_16x16x32_bf16 v[152:155], a[84:87], a[56:59], v[152:155]\n"
"ds_read_b128 a[160:163], v32 offset:0\n"
"v_mfma_f32_16x16x32_bf16 v[156:159], a[88:91], a[56:59], v[156:159]\n"
"ds_read_b128 a[164:167], v32 offset:2048\n"
"buffer_load_dwordx4 v3, s[32:35], s63 offen offset:1024 nt lds\n"
"v_mfma_f32_16x16x32_bf16 v[160:163], a[92:95], a[56:59], v[160:163]\n"
"ds_read_b128 a[168:171], v34 offset:0\n"
"ds_read_b128 a[172:175], v34 offset:2048\n"
"v_mfma_f32_16x16x32_bf16 v[164:167], a[84:87], a[60:63], v[164:167]\n"
"ds_read_b128 a[176:179], v36 offset:0\n"
"v_mfma_f32_16x16x32_bf16 v[168:171], a[88:91], a[60:63], v[168:171]\n"
"ds_read_b64_tr_b16 a[228:229], v38 offset:25600\n"
"v_mfma_f32_16x16x32_bf16 v[172:175], a[92:95], a[60:63], v[172:175]\n"
"ds_read_b64_tr_b16 a[230:231], v38 offset:26112\n"
"v_mfma_f32_16x16x32_bf16 v[176:179], a[84:87], a[64:67], v[176:179]\n"
"ds_read_b64_tr_b16 a[232:233], v39 offset:25600\n"
"ds_read_b64_tr_b16 a[234:235], v39 offset:26112\n"
"buffer_load_dwordx4 v4, s[32:35], s63 offen offset:2048 nt lds\n"
"v_mfma_f32_16x16x32_bf16 v[180:183], a[88:91], a[64:67], v[180:183]\n"
"ds_read_b64_tr_b16 a[236:237], v40 offset:25600\n"
"v_mfma_f32_16x16x32_bf16 v[184:187], a[92:95], a[64:67], v[184:187]\n"
"ds_read_b64_tr_b16 a[238:239], v40 offset:26112\n"
"v_mfma_f32_16x16x32_bf16 v[188:191], a[84:87], a[68:71], v[188:191]\n"
"ds_read_b128 a[192:195], v29 offset:0\n"
"ds_read_b128 a[196:199], v29 offset:2048\n"
"v_mfma_f32_16x16x32_bf16 v[192:195], a[88:91], a[68:71], v[192:195]\n"
"ds_read_b128 a[200:203], v31 offset:0\n"
"v_mfma_f32_16x16x32_bf16 v[196:199], a[92:95], a[68:71], v[196:199]\n"
"ds_read_b128 a[204:207], v31 offset:2048\n"
"buffer_load_dwordx4 v5, s[32:35], s63 offen offset:3072 nt lds\n"
"v_mfma_f32_16x16x32_bf16 v[200:203], a[84:87], a[72:75], v[200:203]\n"
"ds_read_b128 a[208:211], v33 offset:0\n"
"ds_read_b128 a[212:215], v33 offset:2048\n"
"v_mfma_f32_16x16x32_bf16 v[204:207], a[88:91], a[72:75], v[204:207]\n"
"s_add_u32 m0, s53, 20480\n"
"ds_read_b128 a[216:219], v35 offset:0\n"
"v_mfma_f32_16x16x32_bf16 v[208:211], a[92:95], a[72:75], v[208:211]\n"
"ds_read_b128 a[220:223], v35 offset:2048\n"
"v_mfma_f32_16x16x32_bf16 v[212:215], a[84:87], a[76:79], v[212:215]\n"
"ds_read_b128 a[224:227], v37 offset:0\n"
"v_mfma_f32_16x16x32_bf16 v[216:219], a[88:91], a[76:79], v[216:219]\n"
"buffer_load_dwordx4 v6, s[32:35], s63 offen nt lds\n"
"v_mfma_f32_16x16x32_bf16 v[220:223], a[92:95], a[76:79], v[220:223]\n"
"v_mfma_f32_16x16x32_bf16 v[224:227], a[84:87], a[80:83], v[224:227]\n"
"v_mfma_f32_16x16x32_bf16 v[228:231], a[88:91], a[80:83], v[228:231]\n"
"v_mfma_f32_16x16x32_bf16 v[232:235], a[92:95], a[80:83], v[232:235]\n"
"s_waitcnt lgkmcnt(0)\n"
"s_barrier\n"
"v_mfma_f32_16x16x32_bf16 v[224:227], a[180:183], a[176:179], v[224:227]\n"
"ds_read_b128 a[48:51], v20 offset:28672\n"
"buffer_load_dwordx4 v7, s[32:35], s63 offen offset:1024 nt lds\n"
"s_add_u32 m0, s53, 22528\n"
"v_mfma_f32_16x16x32_bf16 v[224:227], a[228:231], a[224:227], v[224:227]\n"
"s_mov_b32 s63, 73728\n"
"ds_read_b128 a[52:55], v21 offset:28672\n"
"v_readfirstlane_b32 s58, v124\n"
"v_readfirstlane_b32 s59, v125\n"
"v_mfma_f32_16x16x32_bf16 v[228:231], a[184:187], a[176:179], v[228:231]\n"
"ds_read_b128 a[56:59], v20 offset:28800\n"
"v_mfma_f32_16x16x32_bf16 v[228:231], a[232:235], a[224:227], v[228:231]\n"
"ds_read_b128 a[60:63], v21 offset:28800\n"
"v_mfma_f32_16x16x32_bf16 v[232:235], a[188:191], a[176:179], v[232:235]\n"
"ds_read_b128 a[64:67], v20 offset:28928\n"
"v_mul_f32 v224, s64, v224\n"
"v_mul_f32 v225, s64, v225\n"
"v_mul_f32 v226, s64, v226\n"
"v_mul_f32 v227, s64, v227\n"
"v_cvt_pk_bf16_f32 v64, v224, v225\n"
".Lpad3_%=:\n"
"s_cbranch_execz .Lwb_%=\n"
"v_cvt_pk_bf16_f32 v65, v226, v227\n"
"v_mfma_f32_16x16x32_bf16 v[232:235], a[236:239], a[224:227], v[232:235]\n"
"buffer_load_dwordx4 v2, s[32:35], s63 offen nt lds\n"
"v_mfma_f32_16x16x32_bf16 v[128:131], a[180:183], a[144:147], v[128:131]\n"
"ds_read_b128 a[68:71], v21 offset:28928\n"
"ds_write_b64 v55, v[64:65]\n"
"v_mul_f32 v228, s64, v228\n"
"v_mul_f32 v229, s64, v229\n"
"v_mul_f32 v230, s64, v230\n"
"v_mul_f32 v231, s64, v231\n"
"v_cvt_pk_bf16_f32 v66, v228, v229\n"
"v_cvt_pk_bf16_f32 v67, v230, v231\n"
"v_mfma_f32_16x16x32_bf16 v[128:131], a[228:231], a[192:195], v[128:131]\n"
"ds_write_b64 v56, v[66:67]\n"
"v_mfma_f32_16x16x32_bf16 v[132:135], a[184:187], a[144:147], v[132:135]\n"
"ds_read_b128 a[72:75], v12 offset:28672\n"
"v_mul_f32 v232, s64, v232\n"
"v_mul_f32 v233, s64, v233\n"
"v_mul_f32 v234, s64, v234\n"
"v_mul_f32 v235, s64, v235\n"
"v_cvt_pk_bf16_f32 v68, v232, v233\n"
"v_cvt_pk_bf16_f32 v69, v234, v235\n"
"v_mfma_f32_16x16x32_bf16 v[132:135], a[232:235], a[192:195], v[132:135]\n"
"buffer_load_dwordx4 v3, s[32:35], s63 offen offset:1024 nt lds\n"
"v_mfma_f32_16x16x32_bf16 v[136:139], a[188:191], a[144:147], v[136:139]\n"
"ds_read_b128 a[76:79], v13 offset:28672\n"
"ds_write_b64 v57, v[68:69]\n"
"v_mul_f32 v128, s64, v128\n"
"v_mul_f32 v129, s64, v129\n"
"v_mul_f32 v130, s64, v130\n"
"v_mul_f32 v131, s64, v131\n"
"v_cvt_pk_bf16_f32 v70, v128, v129\n"
"v_cvt_pk_bf16_f32 v71, v130, v131\n"
"v_mfma_f32_16x16x32_bf16 v[136:139], a[236:239], a[192:195], v[136:139]\n"
"ds_write_b64 v49, v[70:71] offset:0\n"
"v_mfma_f32_16x16x32_bf16 v[140:143], a[180:183], a[148:151], v[140:143]\n"
"ds_read_b128 a[80:83], v12 offset:28800\n"
"v_mul_f32 v132, s64, v132\n"
"v_mul_f32 v133, s64, v133\n"
"v_mul_f32 v134, s64, v134\n"
"v_mul_f32 v135, s64, v135\n"
"v_cvt_pk_bf16_f32 v72, v132, v133\n"
"v_cvt_pk_bf16_f32 v73, v134, v135\n"
"v_mfma_f32_16x16x32_bf16 v[140:143], a[228:231], a[196:199], v[140:143]\n"
"ds_write_b64 v50, v[72:73] offset:0\n"
"v_mfma_f32_16x16x32_bf16 v[144:147], a[184:187], a[148:151], v[144:147]\n"
"ds_read_b128 a[84:87], v13 offset:28800\n"
"v_mul_f32 v136, s64, v136\n"
"v_mul_f32 v137, s64, v137\n"
"v_mul_f32 v138, s64, v138\n"
"v_mul_f32 v139, s64, v139\n"
"v_cvt_pk_bf16_f32 v74, v136, v137\n"
"v_cvt_pk_bf16_f32 v75, v138, v139\n"
"v_mfma_f32_16x16x32_bf16 v[144:147], a[232:235], a[196:199], v[144:147]\n"
"buffer_load_dwordx4 v4, s[32:35], s63 offen offset:2048 nt lds\n"
"v_mfma_f32_16x16x32_bf16 v[148:151], a[188:191], a[148:151], v[148:151]\n"
"ds_read_b128 a[88:91], v12 offset:28928\n"
"ds_write_b64 v51, v[74:75] offset:0\n"
"v_mul_f32 v140, s64, v140\n"
"v_mul_f32 v141, s64, v141\n"
"v_mul_f32 v142, s64, v142\n"
"v_mul_f32 v143, s64, v143\n"
"v_cvt_pk_bf16_f32 v76, v140, v141\n"
"v_cvt_pk_bf16_f32 v77, v142, v143\n"
"v_mfma_f32_16x16x32_bf16 v[148:151], a[236:239], a[196:199], v[148:151]\n"
"ds_write_b64 v49, v[76:77] offset:6144\n"
"v_mfma_f32_16x16x32_bf16 v[152:155], a[180:183], a[152:155], v[152:155]\n"
"ds_read_b128 a[92:95], v13 offset:28928\n"
"v_mul_f32 v144, s64, v144\n"
"v_mul_f32 v145, s64, v145\n"
"v_mul_f32 v146, s64, v146\n"
"v_mul_f32 v147, s64, v147\n"
"v_cvt_pk_bf16_f32 v78, v144, v145\n"
"v_cvt_pk_bf16_f32 v79, v146, v147\n"
"v_mfma_f32_16x16x32_bf16 v[152:155], a[228:231], a[200:203], v[152:155]\n"
"buffer_load_dwordx4 v5, s[32:35], s63 offen offset:3072 nt lds\n"
"v_mfma_f32_16x16x32_bf16 v[156:159], a[184:187], a[152:155], v[156:159]\n"
"ds_write_b64 v50, v[78:79] offset:6144\n"
"v_mul_f32 v148, s64, v148\n"
"v_mul_f32 v149, s64, v149\n"
"v_mul_f32 v150, s64, v150\n"
"v_mul_f32 v151, s64, v151\n"
"v_cvt_pk_bf16_f32 v64, v148, v149\n"
"v_cvt_pk_bf16_f32 v65, v150, v151\n"
"v_mfma_f32_16x16x32_bf16 v[156:159], a[232:235], a[200:203], v[156:159]\n"
"s_add_u32 m0, s53, 26624\n"
"ds_write_b64 v51, v[64:65] offset:6144\n"
"v_mfma_f32_16x16x32_bf16 v[160:163], a[188:191], a[152:155], v[160:163]\n"
"v_mul_f32 v152, s64, v152\n"
"v_mul_f32 v153, s64, v153\n"
"v_mul_f32 v154, s64, v154\n"
"v_mul_f32 v155, s64, v155\n"
"v_cvt_pk_bf16_f32 v66, v152, v153\n"
"v_cvt_pk_bf16_f32 v67, v154, v155\n"
"v_mfma_f32_16x16x32_bf16 v[160:163], a[236:239], a[200:203], v[160:163]\n"
"ds_write_b64 v49, v[66:67] offset:40960\n"
"v_mfma_f32_16x16x32_bf16 v[164:167], a[180:183], a[156:159], v[164:167]\n"
"v_mul_f32 v156, s64, v156\n"
"v_mul_f32 v157, s64, v157\n"
"v_mul_f32 v158, s64, v158\n"
"v_mul_f32 v159, s64, v159\n"
"v_cvt_pk_bf16_f32 v68, v156, v157\n"
"v_cvt_pk_bf16_f32 v69, v158, v159\n"
"v_mfma_f32_16x16x32_bf16 v[164:167], a[228:231], a[204:207], v[164:167]\n"
"buffer_load_dwordx4 v6, s[32:35], s63 offen nt lds\n"
"v_mfma_f32_16x16x32_bf16 v[168:171], a[184:187], a[156:159], v[168:171]\n"
"ds_write_b64 v50, v[68:69] offset:40960\n"
"v_mul_f32 v160, s64, v160\n"
"v_mul_f32 v161, s64, v161\n"
"v_mul_f32 v162, s64, v162\n"
"v_mul_f32 v163, s64, v163\n"
"v_cvt_pk_bf16_f32 v70, v160, v161\n"
"v_cvt_pk_bf16_f32 v71, v162, v163\n"
"v_mfma_f32_16x16x32_bf16 v[168:171], a[232:235], a[204:207], v[168:171]\n"
"ds_write_b64 v51, v[70:71] offset:40960\n"
"v_mfma_f32_16x16x32_bf16 v[172:175], a[188:191], a[156:159], v[172:175]\n"
"v_mul_f32 v164, s64, v164\n"
"v_mul_f32 v165, s64, v165\n"
"v_mul_f32 v166, s64, v166\n"
"v_mul_f32 v167, s64, v167\n"
"v_cvt_pk_bf16_f32 v72, v164, v165\n"
"v_cvt_pk_bf16_f32 v73, v166, v167\n"
"v_mfma_f32_16x16x32_bf16 v[172:175], a[236:239], a[204:207], v[172:175]\n"
"buffer_load_dwordx4 v7, s[32:35], s63 offen offset:1024 nt lds\n"
"v_mfma_f32_16x16x32_bf16 v[176:179], a[180:183], a[160:163], v[176:179]\n"
"s_waitcnt lgkmcnt(13)\n"
"s_barrier\n"
"ds_write_b64 v49, v[72:73] offset:47104\n"
"v_mul_f32 v168, s64, v168\n"
"v_mul_f32 v169, s64, v169\n"
"v_mul_f32 v170, s64, v170\n"
"v_mul_f32 v171, s64, v171\n"
"v_cvt_pk_bf16_f32 v74, v168, v169\n"
"v_cvt_pk_bf16_f32 v75, v170, v171\n"
"v_mfma_f32_16x16x32_bf16 v[176:179], a[228:231], a[208:211], v[176:179]\n"
"ds_write_b64 v50, v[74:75] offset:47104\n"
"v_mfma_f32_16x16x32_bf16 v[180:183], a[184:187], a[160:163], v[180:183]\n"
"ds_read_b128 a[192:195], v1 offset:12288\n"
"v_mul_f32 v172, s64, v172\n"
"v_mul_f32 v173, s64, v173\n"
"v_mul_f32 v174, s64, v174\n"
"v_mul_f32 v175, s64, v175\n"
"v_cvt_pk_bf16_f32 v76, v172, v173\n"
"v_cvt_pk_bf16_f32 v77, v174, v175\n"
"v_mfma_f32_16x16x32_bf16 v[180:183], a[232:235], a[208:211], v[180:183]\n"
"ds_write_b64 v51, v[76:77] offset:47104\n"
"v_mfma_f32_16x16x32_bf16 v[184:187], a[188:191], a[160:163], v[184:187]\n"
"ds_read_b128 a[196:199], v1 offset:13312\n"
"v_mul_f32 v176, s64, v176\n"
"v_mul_f32 v177, s64, v177\n"
"v_mul_f32 v178, s64, v178\n"
"v_mul_f32 v179, s64, v179\n"
"v_cvt_pk_bf16_f32 v78, v176, v177\n"
"v_cvt_pk_bf16_f32 v79, v178, v179\n"
"v_mfma_f32_16x16x32_bf16 v[184:187], a[236:239], a[208:211], v[184:187]\n"
"ds_write_b64 v52, v[78:79] offset:0\n"
"v_mfma_f32_16x16x32_bf16 v[188:191], a[180:183], a[164:167], v[188:191]\n"
"ds_read_b128 a[200:203], v1 offset:53248\n"
"v_mul_f32 v180, s64, v180\n"
"v_mul_f32 v181, s64, v181\n"
"v_mul_f32 v182, s64, v182\n"
"v_mul_f32 v183, s64, v183\n"
"v_cvt_pk_bf16_f32 v64, v180, v181\n"
"v_cvt_pk_bf16_f32 v65, v182, v183\n"
"v_mfma_f32_16x16x32_bf16 v[188:191], a[228:231], a[212:215], v[188:191]\n"
"ds_write_b64 v53, v[64:65] offset:0\n"
"v_mfma_f32_16x16x32_bf16 v[192:195], a[184:187], a[164:167], v[192:195]\n"
"ds_read_b128 a[204:207], v1 offset:54272\n"
"v_mul_f32 v184, s64, v184\n"
"v_mul_f32 v185, s64, v185\n"
"v_mul_f32 v186, s64, v186\n"
"v_mul_f32 v187, s64, v187\n"
"v_cvt_pk_bf16_f32 v66, v184, v185\n"
"v_cvt_pk_bf16_f32 v67, v186, v187\n"
"v_mfma_f32_16x16x32_bf16 v[192:195], a[232:235], a[212:215], v[192:195]\n"
"ds_write_b64 v54, v[66:67] offset:0\n"
"v_mfma_f32_16x16x32_bf16 v[196:199], a[188:191], a[164:167], v[196:199]\n"
"ds_read_b128 a[208:211], v25 offset:12288\n"
"v_mul_f32 v188, s64, v188\n"
"v_mul_f32 v189, s64, v189\n"
"v_mul_f32 v190, s64, v190\n"
"v_mul_f32 v191, s64, v191\n"
"v_cvt_pk_bf16_f32 v68, v188, v189\n"
"v_cvt_pk_bf16_f32 v69, v190, v191\n"
"v_mfma_f32_16x16x32_bf16 v[196:199], a[236:239], a[212:215], v[196:199]\n"
"ds_write_b64 v52, v[68:69] offset:6144\n"
"v_mfma_f32_16x16x32_bf16 v[200:203], a[180:183], a[168:171], v[200:203]\n"
"ds_read_b128 a[212:215], v25 offset:13312\n"
"v_mul_f32 v192, s64, v192\n"
"v_mul_f32 v193, s64, v193\n"
"v_mul_f32 v194, s64, v194\n"
"v_mul_f32 v195, s64, v195\n"
"v_cvt_pk_bf16_f32 v70, v192, v193\n"
"v_cvt_pk_bf16_f32 v71, v194, v195\n"
"v_mfma_f32_16x16x32_bf16 v[200:203], a[228:231], a[216:219], v[200:203]\n"
"ds_write_b64 v53, v[70:71] offset:6144\n"
"v_mfma_f32_16x16x32_bf16 v[204:207], a[184:187], a[168:171], v[204:207]\n"
"v_mul_f32 v196, s64, v196\n"
"v_mul_f32 v197, s64, v197\n"
"v_mul_f32 v198, s64, v198\n"
"v_mul_f32 v199, s64, v199\n"
"v_cvt_pk_bf16_f32 v72, v196, v197\n"
"v_cvt_pk_bf16_f32 v73, v198, v199\n"
"v_mfma_f32_16x16x32_bf16 v[204:207], a[232:235], a[216:219], v[204:207]\n"
"ds_write_b64 v54, v[72:73] offset:6144\n"
"v_mfma_f32_16x16x32_bf16 v[208:211], a[188:191], a[168:171], v[208:211]\n"
"v_mul_f32 v200, s64, v200\n"
"v_mul_f32 v201, s64, v201\n"
"v_mul_f32 v202, s64, v202\n"
"v_mul_f32 v203, s64, v203\n"
"v_cvt_pk_bf16_f32 v74, v200, v201\n"
"v_cvt_pk_bf16_f32 v75, v202, v203\n"
"v_mfma_f32_16x16x32_bf16 v[208:211], a[236:239], a[216:219], v[208:211]\n"
"ds_write_b64 v52, v[74:75] offset:40960\n"
"v_mfma_f32_16x16x32_bf16 v[212:215], a[180:183], a[172:175], v[212:215]\n"
"v_mul_f32 v204, s64, v204\n"
"v_mul_f32 v205, s64, v205\n"
"v_mul_f32 v206, s64, v206\n"
"v_mul_f32 v207, s64, v207\n"
"v_cvt_pk_bf16_f32 v76, v204, v205\n"
"v_cvt_pk_bf16_f32 v77, v206, v207\n"
"v_mfma_f32_16x16x32_bf16 v[212:215], a[228:231], a[220:223], v[212:215]\n"
"ds_write_b64 v53, v[76:77] offset:40960\n"
"v_mfma_f32_16x16x32_bf16 v[216:219], a[184:187], a[172:175], v[216:219]\n"
"v_mul_f32 v208, s64, v208\n"
"v_mul_f32 v209, s64, v209\n"
"v_mul_f32 v210, s64, v210\n"
"v_mul_f32 v211, s64, v211\n"
"v_cvt_pk_bf16_f32 v78, v208, v209\n"
"v_cvt_pk_bf16_f32 v79, v210, v211\n"
"v_mfma_f32_16x16x32_bf16 v[216:219], a[232:235], a[220:223], v[216:219]\n"
"ds_write_b64 v54, v[78:79] offset:40960\n"
"v_mfma_f32_16x16x32_bf16 v[220:223], a[188:191], a[172:175], v[220:223]\n"
"v_mul_f32 v212, s64, v212\n"
"v_mul_f32 v213, s64, v213\n"
"v_mul_f32 v214, s64, v214\n"
"v_mul_f32 v215, s64, v215\n"
"v_cvt_pk_bf16_f32 v64, v212, v213\n"
"v_cvt_pk_bf16_f32 v65, v214, v215\n"
"v_mfma_f32_16x16x32_bf16 v[220:223], a[236:239], a[220:223], v[220:223]\n"
"s_waitcnt lgkmcnt(3)\n"
"ds_write_b64 v52, v[64:65] offset:47104\n"
"v_mfma_f32_16x16x32_bf16 v[80:83], a[48:51], a[192:195], 0\n"
"v_mul_f32 v216, s64, v216\n"
"v_mul_f32 v217, s64, v217\n"
"v_mul_f32 v218, s64, v218\n"
"v_mul_f32 v219, s64, v219\n"
"v_cvt_pk_bf16_f32 v66, v216, v217\n"
"v_cvt_pk_bf16_f32 v67, v218, v219\n"
"v_mfma_f32_16x16x32_bf16 v[80:83], a[52:55], a[196:199], v[80:83]\n"
"ds_write_b64 v53, v[66:67] offset:47104\n"
"v_mfma_f32_16x16x32_bf16 v[80:83], a[56:59], a[200:203], v[80:83]\n"
"v_mul_f32 v220, s64, v220\n"
"v_mul_f32 v221, s64, v221\n"
"v_mul_f32 v222, s64, v222\n"
"v_mul_f32 v223, s64, v223\n"
"v_cvt_pk_bf16_f32 v68, v220, v221\n"
"v_mfma_f32_16x16x32_bf16 v[80:83], a[60:63], a[204:207], v[80:83]\n"
"v_cvt_pk_bf16_f32 v69, v222, v223\n"
"v_mfma_f32_16x16x32_bf16 v[80:83], a[64:67], a[208:211], v[80:83]\n"
"ds_write_b64 v54, v[68:69] offset:47104\n"
"v_mfma_f32_16x16x32_bf16 v[80:83], a[68:71], a[212:215], v[80:83]\n"
"s_waitcnt vmcnt(12)\n"
"s_waitcnt lgkmcnt(0)\n"
"s_nop 7\n"
"s_branch .Ljoin_%=\n"
".Lve1_%=:\n"
".Lvar2_%=:\n"
"s_add_u32 m0, s53, 22528\n"
"s_add_u32 s60, s74, 1\n"
"s_and_b32 s60, s60, 7\n"
"s_mul_i32 s62, s60, 786432\n"
"buffer_load_dwordx4 v46, s[28:31], s62 offen lds\n"
"buffer_load_dwordx4 v47, s[28:31], s62 offen offset:1024 lds\n"
"buffer_load_dwordx4 v48, s[28:31], s62 offen offset:2048 lds\n"
"s_add_u32 m0, s53, 4608\n"
"s_lshl_b32 s61, s60, 7\n"
"buffer_load_dwordx4 v41, s[24:27], s61 offen lds\n"
"buffer_load_dwordx4 v42, s[24:27], s61 offen offset:1024 lds\n"
"buffer_load_dwordx4 v43, s[24:27], s61 offen offset:2048 lds\n"
"buffer_load_dwordx4 v44, s[24:27], s61 offen offset:3072 lds\n"
"s_add_u32 m0, s53, 8704\n"
"s_mov_b32 exec_hi, 0\n"
"buffer_load_dwordx4 v45, s[24:27], s61 offen lds\n"
"s_mov_b32 exec_hi, -1\n"
".Lwalk7_%=:\n"
"s_mov_b64 exec, 0\n"
"s_mov_b32 s98, .Lpad4_%=-.Lentry_%=\n"
"s_cmp_eq_u32 s52, 1\n"
"s_cselect_b32 s98, .Lpad5_%=-.Lentry_%=, s98\n"
"s_cmp_eq_u32 s52, 2\n"
"s_cselect_b32 s98, .Lpad6_%=-.Lentry_%=, s98\n"
"s_cmp_eq_u32 s52, 3\n"
"s_cselect_b32 s98, .Lpad6_%=-.Lentry_%=, s98\n"
"s_getpc_b64 s[96:97]\n"
".Lwh70_%=:\n"
"s_sub_u32 s96, s96, .Lwh70_%=-.Lentry_%=\n"
"s_subb_u32 s97, s97, 0\n"
"s_add_u32 s96, s96, s98\n"
"s_addc_u32 s97, s97, 0\n"
"s_swappc_b64 vcc, s[96:97]\n"
"s_mov_b64 exec, -1\n"
"s_waitcnt vmcnt(13)\n"
"s_barrier\n"
"ds_read_b64_tr_b16 a[180:181], v38 offset:16384\n"
"ds_read_b64_tr_b16 a[182:183], v38 offset:16896\n"
"ds_read_b64_tr_b16 a[184:185], v39 offset:16384\n"
"ds_read_b64_tr_b16 a[186:187], v39 offset:16896\n"
"ds_read_b64_tr_b16 a[188:189], v40 offset:16384\n"
"ds_read_b64_tr_b16 a[190:191], v40 offset:16896\n"
"ds_read_b128 a[144:147], v28 offset:0\n"
"ds_read_b128 a[148:151], v28 offset:2048\n"
"ds_read_b128 a[152:155], v30 offset:0\n"
"ds_read_b128 a[156:159], v30 offset:2048\n"
"ds_read_b128 a[160:163], v32 offset:0\n"
"ds_read_b128 a[164:167], v32 offset:2048\n"
"ds_read_b128 a[168:171], v34 offset:0\n"
"ds_read_b128 a[172:175], v34 offset:2048\n"
"ds_read_b128 a[176:179], v36 offset:0\n"
"s_waitcnt vmcnt(10) lgkmcnt(0)\n"
"s_barrier\n"
"v_mfma_f32_16x16x32_bf16 v[128:131], a[180:183], a[144:147], 0\n"
"s_add_u32 m0, s53, 25600\n"
"ds_read_b64_tr_b16 a[84:85], v38 offset:19456\n"
"s_add_u32 s62, s62, 393216\n"
"v_mfma_f32_16x16x32_bf16 v[132:135], a[184:187], a[144:147], 0\n"
"s_and_b32 s16, s16, 3\n"
"buffer_load_dwordx4 v46, s[28:31], s62 offen lds\n"
"v_mfma_f32_16x16x32_bf16 v[136:139], a[188:191], a[144:147], 0\n"
"s_mul_i32 s16, s16, 245760\n"
"ds_read_b64_tr_b16 a[86:87], v38 offset:19968\n"
"v_mfma_f32_16x16x32_bf16 v[140:143], a[180:183], a[148:151], 0\n"
"s_mul_i32 s17, s56, 320\n"
"ds_read_b64_tr_b16 a[88:89], v39 offset:19456\n"
"v_mfma_f32_16x16x32_bf16 v[144:147], a[184:187], a[148:151], 0\n"
"s_add_u32 s11, s16, s17\n"
"ds_read_b64_tr_b16 a[90:91], v39 offset:19968\n"
"v_mfma_f32_16x16x32_bf16 v[148:151], a[188:191], a[148:151], 0\n"
"s_lshr_b32 s16, s57, 3\n"
"buffer_load_dwordx4 v47, s[28:31], s62 offen offset:1024 lds\n"
"v_mfma_f32_16x16x32_bf16 v[152:155], a[180:183], a[152:155], 0\n"
"s_lshl_b32 s16, s16, 2\n"
"ds_read_b64_tr_b16 a[92:93], v40 offset:19456\n"
"v_mfma_f32_16x16x32_bf16 v[156:159], a[184:187], a[152:155], 0\n"
"s_add_u32 s16, s16, s52\n"
"ds_read_b64_tr_b16 a[94:95], v40 offset:19968\n"
"v_mfma_f32_16x16x32_bf16 v[160:163], a[188:191], a[152:155], 0\n"
"s_lshr_b32 s17, s16, 5\n"
"ds_read_b128 a[48:51], v29 offset:0\n"
"v_mfma_f32_16x16x32_bf16 v[164:167], a[180:183], a[156:159], 0\n"
"s_mul_i32 s17, s17, 245760\n"
"buffer_load_dwordx4 v48, s[28:31], s62 offen offset:2048 lds\n"
"v_mfma_f32_16x16x32_bf16 v[168:171], a[184:187], a[156:159], 0\n"
"s_lshl_b32 s4, s56, 2\n"
"s_add_u32 s4, s4, s52\n"
"s_mul_i32 s4, s4, 11520\n"
"v_mfma_f32_16x16x32_bf16 v[172:175], a[188:191], a[156:159], 0\n"
"s_and_b32 s16, s16, 31\n"
"v_min_u32 v94, 17, v0\n"
"v_lshrrev_b32 v95, 1, v94\n"
"v_mfma_f32_16x16x32_bf16 v[176:179], a[180:183], a[160:163], 0\n"
"ds_read_b128 a[52:55], v29 offset:2048\n"
"v_mul_u32_u24 v95, 1024, v95\n"
"v_lshl_add_u32 v94, v94, 7, v95\n"
"v_mfma_f32_16x16x32_bf16 v[180:183], a[184:187], a[160:163], 0\n"
"s_mul_i32 s16, s16, 2560\n"
"ds_read_b128 a[56:59], v31 offset:0\n"
"v_mfma_f32_16x16x32_bf16 v[184:187], a[188:191], a[160:163], 0\n"
"s_add_u32 s12, s16, s17\n"
"ds_read_b128 a[60:63], v31 offset:2048\n"
"v_mfma_f32_16x16x32_bf16 v[188:191], a[180:183], a[164:167], 0\n"
"ds_read_b128 a[64:67], v33 offset:0\n"
"buffer_load_dword v94, v94, s[24:27], s4 offen offset:1024\n"
"s_lshl_b32 s4, s55, 2\n"
"s_add_u32 s4, s4, s52\n"
"s_mul_i32 s4, s4, 49152\n"
"s_add_u32 s4, s4, 6291456\n"
"v_min_u32 v91, 11, v0\n"
"v_mfma_f32_16x16x32_bf16 v[192:195], a[184:187], a[164:167], 0\n"
"ds_read_b128 a[68:71], v33 offset:2048\n"
"v_mul_u32_u24 v95, 86, v91\n"
"v_lshrrev_b32 v95, 8, v95\n"
"v_mfma_f32_16x16x32_bf16 v[196:199], a[188:191], a[164:167], 0\n"
"ds_read_b128 a[72:75], v35 offset:0\n"
"v_mul_u32_u24 v95, 11904, v95\n"
"v_lshl_add_u32 v91, v91, 7, v95\n"
"v_mfma_f32_16x16x32_bf16 v[200:203], a[180:183], a[168:171], 0\n"
"s_lshr_b32 s16, s52, 1\n"
"ds_read_b128 a[76:79], v35 offset:2048\n"
"v_mfma_f32_16x16x32_bf16 v[204:207], a[184:187], a[168:171], 0\n"
"s_lshl_b32 s16, s16, 10\n"
"ds_read_b128 a[80:83], v37 offset:0\n"
"v_mfma_f32_16x16x32_bf16 v[208:211], a[188:191], a[168:171], 0\n"
"s_and_b32 s17, s52, 1\n"
"buffer_load_dword v91, v91, s[28:31], s4 offen offset:2048\n"
"v_mfma_f32_16x16x32_bf16 v[212:215], a[180:183], a[172:175], 0\n"
"s_add_u32 m0, s53, 16384\n"
"s_add_u32 s60, s74, 2\n"
"s_and_b32 s60, s60, 7\n"
"s_mul_i32 s62, s60, 786432\n"
"v_mfma_f32_16x16x32_bf16 v[216:219], a[184:187], a[172:175], 0\n"
"s_lshl_b32 s17, s17, 3\n"
"v_mfma_f32_16x16x32_bf16 v[220:223], a[188:191], a[172:175], 0\n"
"s_add_u32 s22, s16, s17\n"
"v_mfma_f32_16x16x32_bf16 v[224:227], a[180:183], a[176:179], 0\n"
"s_lshl_b32 s16, s57, 11\n"
"buffer_load_dwordx4 v46, s[28:31], s62 offen lds\n"
"v_mfma_f32_16x16x32_bf16 v[228:231], a[184:187], a[176:179], 0\n"
"s_lshl_b32 s17, s52, 7\n"
"v_mfma_f32_16x16x32_bf16 v[232:235], a[188:191], a[176:179], 0\n"
"s_waitcnt vmcnt(6) lgkmcnt(0)\n"
"s_barrier\n"
"ds_read_b64_tr_b16 a[180:181], v38 offset:22528\n"
"v_mfma_f32_16x16x32_bf16 v[128:131], a[84:87], a[48:51], v[128:131]\n"
"s_add_u32 s16, s16, s17\n"
"ds_read_b64_tr_b16 a[182:183], v38 offset:23040\n"
"v_mfma_f32_16x16x32_bf16 v[132:135], a[88:91], a[48:51], v[132:135]\n"
"s_add_u32 s70, s16, 524288\n"
"buffer_load_dwordx4 v47, s[28:31], s62 offen offset:1024 lds\n"
"v_mfma_f32_16x16x32_bf16 v[136:139], a[92:95], a[48:51], v[136:139]\n"
"ds_read_b64_tr_b16 a[184:185], v39 offset:22528\n"
"v_lshlrev_b32 v90, 9, v59\n"
"v_mfma_f32_16x16x32_bf16 v[140:143], a[84:87], a[52:55], v[140:143]\n"
"ds_read_b64_tr_b16 a[186:187], v39 offset:23040\n"
"v_mfma_f32_16x16x32_bf16 v[144:147], a[88:91], a[52:55], v[144:147]\n"
"ds_read_b64_tr_b16 a[188:189], v40 offset:22528\n"
"v_mfma_f32_16x16x32_bf16 v[148:151], a[92:95], a[52:55], v[148:151]\n"
"buffer_load_dwordx4 v48, s[28:31], s62 offen offset:2048 lds\n"
"v_mfma_f32_16x16x32_bf16 v[152:155], a[84:87], a[56:59], v[152:155]\n"
"s_add_u32 m0, s53, 9216\n"
"ds_read_b64_tr_b16 a[190:191], v40 offset:23040\n"
"s_lshl_b32 s61, s60, 7\n"
"v_mfma_f32_16x16x32_bf16 v[156:159], a[88:91], a[56:59], v[156:159]\n"
"ds_read_b128 a[144:147], v28 offset:4608\n"
"v_mfma_f32_16x16x32_bf16 v[160:163], a[92:95], a[56:59], v[160:163]\n"
"ds_read_b128 a[148:151], v28 offset:6656\n"
"v_mfma_f32_16x16x32_bf16 v[164:167], a[84:87], a[60:63], v[164:167]\n"
"buffer_load_dwordx4 v41, s[24:27], s61 offen lds\n"
"v_mfma_f32_16x16x32_bf16 v[168:171], a[88:91], a[60:63], v[168:171]\n"
"ds_read_b128 a[152:155], v30 offset:4608\n"
"v_mfma_f32_16x16x32_bf16 v[172:175], a[92:95], a[60:63], v[172:175]\n"
"ds_read_b128 a[156:159], v30 offset:6656\n"
"v_mfma_f32_16x16x32_bf16 v[176:179], a[84:87], a[64:67], v[176:179]\n"
"ds_read_b128 a[160:163], v32 offset:4608\n"
"v_mfma_f32_16x16x32_bf16 v[180:183], a[88:91], a[64:67], v[180:183]\n"
"buffer_load_dwordx4 v42, s[24:27], s61 offen offset:1024 lds\n"
"v_mfma_f32_16x16x32_bf16 v[184:187], a[92:95], a[64:67], v[184:187]\n"
"ds_read_b128 a[164:167], v32 offset:6656\n"
"v_mfma_f32_16x16x32_bf16 v[188:191], a[84:87], a[68:71], v[188:191]\n"
"ds_read_b128 a[168:171], v34 offset:4608\n"
"v_mfma_f32_16x16x32_bf16 v[192:195], a[88:91], a[68:71], v[192:195]\n"
"ds_read_b128 a[172:175], v34 offset:6656\n"
"v_mfma_f32_16x16x32_bf16 v[196:199], a[92:95], a[68:71], v[196:199]\n"
"buffer_load_dwordx4 v43, s[24:27], s61 offen offset:2048 lds\n"
"v_mfma_f32_16x16x32_bf16 v[200:203], a[84:87], a[72:75], v[200:203]\n"
"ds_read_b128 a[176:179], v36 offset:4608\n"
"v_mfma_f32_16x16x32_bf16 v[204:207], a[88:91], a[72:75], v[204:207]\n"
"v_mfma_f32_16x16x32_bf16 v[208:211], a[92:95], a[72:75], v[208:211]\n"
"v_mfma_f32_16x16x32_bf16 v[212:215], a[84:87], a[76:79], v[212:215]\n"
"buffer_load_dwordx4 v44, s[24:27], s61 offen offset:3072 lds\n"
"v_mfma_f32_16x16x32_bf16 v[216:219], a[88:91], a[76:79], v[216:219]\n"
"s_add_u32 m0, s53, 13312\n"
"v_mfma_f32_16x16x32_bf16 v[220:223], a[92:95], a[76:79], v[220:223]\n"
"v_mfma_f32_16x16x32_bf16 v[224:227], a[84:87], a[80:83], v[224:227]\n"
"v_mfma_f32_16x16x32_bf16 v[228:231], a[88:91], a[80:83], v[228:231]\n"
"s_mov_b32 exec_hi, 0\n"
"buffer_load_dwordx4 v45, s[24:27], s61 offen lds\n"
"s_mov_b32 exec_hi, -1\n"
"v_mfma_f32_16x16x32_bf16 v[232:235], a[92:95], a[80:83], v[232:235]\n"
"s_waitcnt vmcnt(10) lgkmcnt(0)\n"
"s_barrier\n"
"s_add_u32 m0, s53, 19456\n"
"s_add_u32 s62, s62, 393216\n"
"v_mfma_f32_16x16x32_bf16 v[128:131], a[180:183], a[144:147], v[128:131]\n"
"ds_read_b64_tr_b16 a[84:85], v38 offset:25600\n"
"v_mfma_f32_16x16x32_bf16 v[132:135], a[184:187], a[144:147], v[132:135]\n"
"ds_read_b64_tr_b16 a[86:87], v38 offset:26112\n"
"v_mfma_f32_16x16x32_bf16 v[136:139], a[188:191], a[144:147], v[136:139]\n"
"buffer_load_dwordx4 v46, s[28:31], s62 offen lds\n"
"v_mfma_f32_16x16x32_bf16 v[140:143], a[180:183], a[148:151], v[140:143]\n"
"ds_read_b64_tr_b16 a[88:89], v39 offset:25600\n"
"v_mfma_f32_16x16x32_bf16 v[144:147], a[184:187], a[148:151], v[144:147]\n"
"ds_read_b64_tr_b16 a[90:91], v39 offset:26112\n"
"v_mfma_f32_16x16x32_bf16 v[148:151], a[188:191], a[148:151], v[148:151]\n"
"buffer_load_dword v90, v90, s[44:47], s70 offen\n"
"v_mfma_f32_16x16x32_bf16 v[152:155], a[180:183], a[152:155], v[152:155]\n"
"s_lshl_b32 s16, s57, 10\n"
"buffer_load_dwordx4 v47, s[28:31], s62 offen offset:1024 lds\n"
"v_mfma_f32_16x16x32_bf16 v[156:159], a[184:187], a[152:155], v[156:159]\n"
"s_lshl_b32 s17, s52, 8\n"
"ds_read_b64_tr_b16 a[92:93], v40 offset:25600\n"
"v_mfma_f32_16x16x32_bf16 v[160:163], a[188:191], a[152:155], v[160:163]\n"
"s_add_u32 s16, s16, s17\n"
"ds_read_b64_tr_b16 a[94:95], v40 offset:26112\n"
"v_mfma_f32_16x16x32_bf16 v[164:167], a[180:183], a[156:159], v[164:167]\n"
"s_add_u32 s16, s16, 15728640\n"
"ds_read_b128 a[48:51], v29 offset:4608\n"
"v_mfma_f32_16x16x32_bf16 v[168:171], a[184:187], a[156:159], v[168:171]\n"
"buffer_load_dwordx4 v48, s[28:31], s62 offen offset:2048 lds\n"
"v_mfma_f32_16x16x32_bf16 v[172:175], a[188:191], a[156:159], v[172:175]\n"
"s_add_u32 m0, s53, 22528\n"
"ds_read_b128 a[52:55], v29 offset:6656\n"
"s_add_u32 s60, s74, 3\n"
"v_mfma_f32_16x16x32_bf16 v[176:179], a[180:183], a[160:163], v[176:179]\n"
"s_and_b32 s60, s60, 7\n"
"ds_read_b128 a[56:59], v31 offset:4608\n"
"s_mul_i32 s62, s60, 786432\n"
"v_mfma_f32_16x16x32_bf16 v[180:183], a[184:187], a[160:163], v[180:183]\n"
"ds_read_b128 a[60:63], v31 offset:6656\n"
"v_lshlrev_b32 v89, 2, v0\n"
"v_mfma_f32_16x16x32_bf16 v[184:187], a[188:191], a[160:163], v[184:187]\n"
"ds_read_b128 a[64:67], v33 offset:4608\n"
"v_mfma_f32_16x16x32_bf16 v[188:191], a[180:183], a[164:167], v[188:191]\n"
"buffer_load_dwordx4 v46, s[28:31], s62 offen lds\n"
"v_mfma_f32_16x16x32_bf16 v[192:195], a[184:187], a[164:167], v[192:195]\n"
"ds_read_b128 a[68:71], v33 offset:6656\n"
"v_mfma_f32_16x16x32_bf16 v[196:199], a[188:191], a[164:167], v[196:199]\n"
"ds_read_b128 a[72:75], v35 offset:4608\n"
"v_mfma_f32_16x16x32_bf16 v[200:203], a[180:183], a[168:171], v[200:203]\n"
"ds_read_b128 a[76:79], v35 offset:6656\n"
"v_mfma_f32_16x16x32_bf16 v[204:207], a[184:187], a[168:171], v[204:207]\n"
"ds_read_b128 a[80:83], v37 offset:4608\n"
"buffer_load_dwordx4 v47, s[28:31], s62 offen offset:1024 lds\n"
"buffer_load_dword v89, v89, s[44:47], s16 offen\n"
"v_mfma_f32_16x16x32_bf16 v[208:211], a[188:191], a[168:171], v[208:211]\n"
"s_mul_i32 s16, s57, 46080\n"
"v_mfma_f32_16x16x32_bf16 v[212:215], a[180:183], a[172:175], v[212:215]\n"
"s_add_u32 s71, s16, 16777216\n"
"v_mfma_f32_16x16x32_bf16 v[216:219], a[184:187], a[172:175], v[216:219]\n"
"v_lshrrev_b32 v60, 2, v0\n"
"v_mfma_f32_16x16x32_bf16 v[220:223], a[188:191], a[172:175], v[220:223]\n"
"buffer_load_dwordx4 v48, s[28:31], s62 offen offset:2048 lds\n"
"v_mfma_f32_16x16x32_bf16 v[224:227], a[180:183], a[176:179], v[224:227]\n"
"s_add_u32 m0, s53, 0\n"
"s_lshl_b32 s61, s60, 7\n"
"v_lshlrev_b32 v61, 7, v60\n"
"v_mfma_f32_16x16x32_bf16 v[228:231], a[184:187], a[176:179], v[228:231]\n"
"v_and_b32 v62, 3, v0\n"
"v_mfma_f32_16x16x32_bf16 v[232:235], a[188:191], a[176:179], v[232:235]\n"
"s_waitcnt vmcnt(8) lgkmcnt(0)\n"
"s_barrier\n"
"ds_read_b64_tr_b16 a[180:181], v38 offset:16384\n"
"v_mfma_f32_16x16x32_bf16 v[128:131], a[84:87], a[48:51], v[128:131]\n"
"buffer_load_dwordx4 v41, s[24:27], s61 offen lds\n"
"v_mfma_f32_16x16x32_bf16 v[132:135], a[88:91], a[48:51], v[132:135]\n"
"ds_read_b64_tr_b16 a[182:183], v38 offset:16896\n"
"v_lshl_add_u32 v61, v62, 3, v61\n"
"v_mfma_f32_16x16x32_bf16 v[136:139], a[92:95], a[48:51], v[136:139]\n"
"ds_read_b64_tr_b16 a[184:185], v39 offset:16384\n"
"v_bfe_u32 v60, v60, 1, 2\n"
"v_mfma_f32_16x16x32_bf16 v[140:143], a[84:87], a[52:55], v[140:143]\n"
"ds_read_b64_tr_b16 a[186:187], v39 offset:16896\n"
"v_xor_b32 v62, 0, v60\n"
"v_mfma_f32_16x16x32_bf16 v[144:147], a[88:91], a[52:55], v[144:147]\n"
"buffer_load_dwordx4 v42, s[24:27], s61 offen offset:1024 lds\n"
"v_mfma_f32_16x16x32_bf16 v[148:151], a[92:95], a[52:55], v[148:151]\n"
"ds_read_b64_tr_b16 a[188:189], v40 offset:16384\n"
"v_lshl_add_u32 v62, v62, 5, v61\n"
"v_mfma_f32_16x16x32_bf16 v[152:155], a[84:87], a[56:59], v[152:155]\n"
"s_xor_b32 s16, s52, 0\n"
"ds_read_b64_tr_b16 a[190:191], v40 offset:16896\n"
"v_mfma_f32_16x16x32_bf16 v[156:159], a[88:91], a[56:59], v[156:159]\n"
"s_mul_i32 s16, s16, 40960\n"
"ds_read_b128 a[144:147], v28 offset:9216\n"
"v_mfma_f32_16x16x32_bf16 v[160:163], a[92:95], a[56:59], v[160:163]\n"
"buffer_load_dwordx4 v43, s[24:27], s61 offen offset:2048 lds\n"
"v_mfma_f32_16x16x32_bf16 v[164:167], a[84:87], a[60:63], v[164:167]\n"
"ds_read_b128 a[148:151], v28 offset:11264\n"
"v_add_u32 v96, s16, v62\n"
"v_mfma_f32_16x16x32_bf16 v[168:171], a[88:91], a[60:63], v[168:171]\n"
"s_xor_b32 s17, s52, 1\n"
"ds_read_b128 a[152:155], v30 offset:9216\n"
"v_mfma_f32_16x16x32_bf16 v[172:175], a[92:95], a[60:63], v[172:175]\n"
"s_mul_i32 s17, s17, 40960\n"
"ds_read_b128 a[156:159], v30 offset:11264\n"
"v_mfma_f32_16x16x32_bf16 v[176:179], a[84:87], a[64:67], v[176:179]\n"
"buffer_load_dwordx4 v44, s[24:27], s61 offen offset:3072 lds\n"
"v_mfma_f32_16x16x32_bf16 v[180:183], a[88:91], a[64:67], v[180:183]\n"
"s_add_u32 m0, s53, 4096\n"
"ds_read_b128 a[160:163], v32 offset:9216\n"
"v_add_u32 v97, s17, v62\n"
"v_mfma_f32_16x16x32_bf16 v[184:187], a[92:95], a[64:67], v[184:187]\n"
"s_xor_b32 s18, s52, 2\n"
"ds_read_b128 a[164:167], v32 offset:11264\n"
"v_mfma_f32_16x16x32_bf16 v[188:191], a[84:87], a[68:71], v[188:191]\n"
"s_mul_i32 s18, s18, 40960\n"
"ds_read_b128 a[168:171], v34 offset:9216\n"
"v_mfma_f32_16x16x32_bf16 v[192:195], a[88:91], a[68:71], v[192:195]\n"
"s_mov_b32 exec_hi, 0\n"
"buffer_load_dwordx4 v45, s[24:27], s61 offen lds\n"
"s_mov_b32 exec_hi, -1\n"
"v_mfma_f32_16x16x32_bf16 v[196:199], a[92:95], a[68:71], v[196:199]\n"
"s_add_u32 m0, s53, 25600\n"
"ds_read_b128 a[172:175], v34 offset:11264\n"
"s_add_u32 s62, s62, 393216\n"
"v_mfma_f32_16x16x32_bf16 v[200:203], a[84:87], a[72:75], v[200:203]\n"
"ds_read_b128 a[176:179], v36 offset:9216\n"
"v_add_u32 v98, s18, v62\n"
"v_mfma_f32_16x16x32_bf16 v[204:207], a[88:91], a[72:75], v[204:207]\n"
"s_xor_b32 s19, s52, 3\n"
"v_mfma_f32_16x16x32_bf16 v[208:211], a[92:95], a[72:75], v[208:211]\n"
"s_mul_i32 s19, s19, 40960\n"
"buffer_load_dwordx4 v46, s[28:31], s62 offen lds\n"
"v_mfma_f32_16x16x32_bf16 v[212:215], a[84:87], a[76:79], v[212:215]\n"
"v_add_u32 v99, s19, v62\n"
"v_mfma_f32_16x16x32_bf16 v[216:219], a[88:91], a[76:79], v[216:219]\n"
"v_xor_b32 v62, 1, v60\n"
"v_mfma_f32_16x16x32_bf16 v[220:223], a[92:95], a[76:79], v[220:223]\n"
"v_lshl_add_u32 v62, v62, 5, v61\n"
"v_mfma_f32_16x16x32_bf16 v[224:227], a[84:87], a[80:83], v[224:227]\n"
".Lpad4_%=:\n"
"s_cbranch_execz .Lwb_%=\n"
"buffer_load_dwordx4 v47, s[28:31], s62 offen offset:1024 lds\n"
"v_mfma_f32_16x16x32_bf16 v[228:231], a[88:91], a[80:83], v[228:231]\n"
"v_add_u32 v100, s16, v62\n"
"v_mfma_f32_16x16x32_bf16 v[232:235], a[92:95], a[80:83], v[232:235]\n"
"s_waitcnt vmcnt(11) lgkmcnt(0)\n"
"s_barrier\n"
"ds_read_b64_tr_b16 a[84:85], v38 offset:19456\n"
"v_mfma_f32_16x16x32_bf16 v[128:131], a[180:183], a[144:147], v[128:131]\n"
"ds_read_b64_tr_b16 a[86:87], v38 offset:19968\n"
"v_add_u32 v101, s17, v62\n"
"v_mfma_f32_16x16x32_bf16 v[132:135], a[184:187], a[144:147], v[132:135]\n"
"ds_read_b64_tr_b16 a[88:89], v39 offset:19456\n"
"v_add_u32 v102, s18, v62\n"
"v_mfma_f32_16x16x32_bf16 v[136:139], a[188:191], a[144:147], v[136:139]\n"
"buffer_load_dwordx4 v48, s[28:31], s62 offen offset:2048 lds\n"
"v_mfma_f32_16x16x32_bf16 v[140:143], a[180:183], a[148:151], v[140:143]\n"
"s_add_u32 m0, s53, 16384\n"
"s_add_u32 s60, s74, 4\n"
"s_and_b32 s60, s60, 7\n"
"v_mfma_f32_16x16x32_bf16 v[144:147], a[184:187], a[148:151], v[144:147]\n"
"s_mul_i32 s62, s60, 786432\n"
"ds_read_b64_tr_b16 a[90:91], v39 offset:19968\n"
"v_add_u32 v103, s19, v62\n"
"v_mfma_f32_16x16x32_bf16 v[148:151], a[188:191], a[148:151], v[148:151]\n"
"ds_read_b64_tr_b16 a[92:93], v40 offset:19456\n"
"v_xor_b32 v62, 2, v60\n"
"v_mfma_f32_16x16x32_bf16 v[152:155], a[180:183], a[152:155], v[152:155]\n"
"buffer_load_dwordx4 v46, s[28:31], s62 offen lds\n"
"v_mfma_f32_16x16x32_bf16 v[156:159], a[184:187], a[152:155], v[156:159]\n"
"ds_read_b64_tr_b16 a[94:95], v40 offset:19968\n"
"v_lshl_add_u32 v62, v62, 5, v61\n"
"v_mfma_f32_16x16x32_bf16 v[160:163], a[188:191], a[152:155], v[160:163]\n"
"ds_read_b128 a[48:51], v29 offset:9216\n"
"v_add_u32 v104, s16, v62\n"
"v_mfma_f32_16x16x32_bf16 v[164:167], a[180:183], a[156:159], v[164:167]\n"
"ds_read_b128 a[52:55], v29 offset:11264\n"
"v_add_u32 v105, s17, v62\n"
"v_mfma_f32_16x16x32_bf16 v[168:171], a[184:187], a[156:159], v[168:171]\n"
"buffer_load_dwordx4 v47, s[28:31], s62 offen offset:1024 lds\n"
"v_mfma_f32_16x16x32_bf16 v[172:175], a[188:191], a[156:159], v[172:175]\n"
"ds_read_b128 a[56:59], v31 offset:9216\n"
"v_add_u32 v106, s18, v62\n"
"v_mfma_f32_16x16x32_bf16 v[176:179], a[180:183], a[160:163], v[176:179]\n"
"ds_read_b128 a[60:63], v31 offset:11264\n"
"v_add_u32 v107, s19, v62\n"
"v_mfma_f32_16x16x32_bf16 v[180:183], a[184:187], a[160:163], v[180:183]\n"
"ds_read_b128 a[64:67], v33 offset:9216\n"
"v_xor_b32 v62, 3, v60\n"
"v_mfma_f32_16x16x32_bf16 v[184:187], a[188:191], a[160:163], v[184:187]\n"
"ds_read_b128 a[68:71], v33 offset:11264\n"
"v_lshl_add_u32 v62, v62, 5, v61\n"
"v_mfma_f32_16x16x32_bf16 v[188:191], a[180:183], a[164:167], v[188:191]\n"
"ds_read_b128 a[72:75], v35 offset:9216\n"
"v_add_u32 v108, s16, v62\n"
"v_mfma_f32_16x16x32_bf16 v[192:195], a[184:187], a[164:167], v[192:195]\n"
"buffer_load_dwordx4 v48, s[28:31], s62 offen offset:2048 lds\n"
"v_mfma_f32_16x16x32_bf16 v[196:199], a[188:191], a[164:167], v[196:199]\n"
"s_add_u32 m0, s53, 4608\n"
"ds_read_b128 a[76:79], v35 offset:11264\n"
"s_lshl_b32 s61, s60, 7\n"
"v_mfma_f32_16x16x32_bf16 v[200:203], a[180:183], a[168:171], v[200:203]\n"
"ds_read_b128 a[80:83], v37 offset:9216\n"
"v_add_u32 v109, s17, v62\n"
"v_mfma_f32_16x16x32_bf16 v[204:207], a[184:187], a[168:171], v[204:207]\n"
"v_add_u32 v110, s18, v62\n"
"v_mfma_f32_16x16x32_bf16 v[208:211], a[188:191], a[168:171], v[208:211]\n"
"buffer_load_dwordx4 v41, s[24:27], s61 offen lds\n"
"v_mfma_f32_16x16x32_bf16 v[212:215], a[180:183], a[172:175], v[212:215]\n"
"v_add_u32 v111, s19, v62\n"
"v_mfma_f32_16x16x32_bf16 v[216:219], a[184:187], a[172:175], v[216:219]\n"
"v_mov_b32 v22, 0xff800000\n"
"v_mfma_f32_16x16x32_bf16 v[220:223], a[188:191], a[172:175], v[220:223]\n"
"v_mov_b32 v23, 0xff800000\n"
"v_mfma_f32_16x16x32_bf16 v[224:227], a[180:183], a[176:179], v[224:227]\n"
"buffer_load_dwordx4 v42, s[24:27], s61 offen offset:1024 lds\n"
"v_mfma_f32_16x16x32_bf16 v[228:231], a[184:187], a[176:179], v[228:231]\n"
"v_mov_b32 v24, 0xff800000\n"
"v_mfma_f32_16x16x32_bf16 v[232:235], a[188:191], a[176:179], v[232:235]\n"
"s_waitcnt vmcnt(8) lgkmcnt(0)\n"
"s_barrier\n"
"ds_read_b64_tr_b16 a[180:181], v38 offset:22528\n"
"v_mfma_f32_16x16x32_bf16 v[128:131], a[84:87], a[48:51], v[128:131]\n"
"ds_read_b64_tr_b16 a[182:183], v38 offset:23040\n"
"v_mul_u32_u24 v60, 40960, v59\n"
"v_mfma_f32_16x16x32_bf16 v[132:135], a[88:91], a[48:51], v[132:135]\n"
"buffer_load_dwordx4 v43, s[24:27], s61 offen offset:2048 lds\n"
"v_mfma_f32_16x16x32_bf16 v[136:139], a[92:95], a[48:51], v[136:139]\n"
"ds_read_b64_tr_b16 a[184:185], v39 offset:22528\n"
"v_bfe_u32 v61, v0, 2, 2\n"
"v_mfma_f32_16x16x32_bf16 v[140:143], a[84:87], a[52:55], v[140:143]\n"
"ds_read_b64_tr_b16 a[186:187], v39 offset:23040\n"
"v_mul_u32_u24 v61, 320, v61\n"
"v_mfma_f32_16x16x32_bf16 v[144:147], a[88:91], a[52:55], v[144:147]\n"
"ds_read_b64_tr_b16 a[188:189], v40 offset:22528\n"
"v_and_b32 v62, 3, v0\n"
"v_mfma_f32_16x16x32_bf16 v[148:151], a[92:95], a[52:55], v[148:151]\n"
"buffer_load_dwordx4 v44, s[24:27], s61 offen offset:3072 lds\n"
"v_mfma_f32_16x16x32_bf16 v[152:155], a[84:87], a[56:59], v[152:155]\n"
"s_add_u32 m0, s53, 8704\n"
"ds_read_b64_tr_b16 a[190:191], v40 offset:23040\n"
"v_lshl_add_u32 v61, v62, 3, v61\n"
"v_mfma_f32_16x16x32_bf16 v[156:159], a[88:91], a[56:59], v[156:159]\n"
"ds_read_b128 a[144:147], v28 offset:0\n"
"v_add_u32 v60, v60, v61\n"
"v_mfma_f32_16x16x32_bf16 v[160:163], a[92:95], a[56:59], v[160:163]\n"
"ds_read_b128 a[148:151], v28 offset:2048\n"
"v_and_b32 v61, 1, v59\n"
"v_mfma_f32_16x16x32_bf16 v[164:167], a[84:87], a[60:63], v[164:167]\n"
"s_mov_b32 exec_hi, 0\n"
"buffer_load_dwordx4 v45, s[24:27], s61 offen lds\n"
"s_mov_b32 exec_hi, -1\n"
"v_mfma_f32_16x16x32_bf16 v[168:171], a[88:91], a[60:63], v[168:171]\n"
"s_add_u32 m0, s53, 19456\n"
"ds_read_b128 a[152:155], v30 offset:0\n"
"s_add_u32 s62, s62, 393216\n"
"v_mfma_f32_16x16x32_bf16 v[172:175], a[92:95], a[60:63], v[172:175]\n"
"s_mul_i32 s18, s52, 3\n"
"ds_read_b128 a[156:159], v30 offset:2048\n"
"v_mfma_f32_16x16x32_bf16 v[176:179], a[84:87], a[64:67], v[176:179]\n"
"s_add_u32 s16, s18, 3\n"
"ds_read_b128 a[160:163], v32 offset:0\n"
"v_mfma_f32_16x16x32_bf16 v[180:183], a[88:91], a[64:67], v[180:183]\n"
"buffer_load_dwordx4 v46, s[28:31], s62 offen lds\n"
"v_mfma_f32_16x16x32_bf16 v[184:187], a[92:95], a[64:67], v[184:187]\n"
"s_sub_u32 s17, s16, 10\n"
"s_cselect_b32 s16, s16, s17\n"
"ds_read_b128 a[164:167], v32 offset:2048\n"
"v_mfma_f32_16x16x32_bf16 v[188:191], a[84:87], a[68:71], v[188:191]\n"
"ds_read_b128 a[168:171], v34 offset:0\n"
"v_xor_b32 v62, s16, v61\n"
"v_mfma_f32_16x16x32_bf16 v[192:195], a[88:91], a[68:71], v[192:195]\n"
"ds_read_b128 a[172:175], v34 offset:2048\n"
"v_lshl_add_u32 v62, v62, 5, v60\n"
"v_mfma_f32_16x16x32_bf16 v[196:199], a[92:95], a[68:71], v[196:199]\n"
"ds_read_b128 a[176:179], v36 offset:0\n"
"v_accvgpr_write_b32 a112, v62\n"
"v_mfma_f32_16x16x32_bf16 v[200:203], a[84:87], a[72:75], v[200:203]\n"
"s_add_u32 s16, s18, 4\n"
"buffer_load_dwordx4 v47, s[28:31], s62 offen offset:1024 lds\n"
"v_mfma_f32_16x16x32_bf16 v[204:207], a[88:91], a[72:75], v[204:207]\n"
"s_sub_u32 s17, s16, 10\n"
"s_cselect_b32 s16, s16, s17\n"
"v_mfma_f32_16x16x32_bf16 v[208:211], a[92:95], a[72:75], v[208:211]\n"
"v_xor_b32 v62, s16, v61\n"
"v_mfma_f32_16x16x32_bf16 v[212:215], a[84:87], a[76:79], v[212:215]\n"
"v_lshl_add_u32 v62, v62, 5, v60\n"
"v_mfma_f32_16x16x32_bf16 v[216:219], a[88:91], a[76:79], v[216:219]\n"
"buffer_load_dwordx4 v48, s[28:31], s62 offen offset:2048 lds\n"
"v_mfma_f32_16x16x32_bf16 v[220:223], a[92:95], a[76:79], v[220:223]\n"
"v_accvgpr_write_b32 a113, v62\n"
"v_mfma_f32_16x16x32_bf16 v[224:227], a[84:87], a[80:83], v[224:227]\n"
"s_add_u32 m0, s53, 22528\n"
"s_add_u32 s60, s74, 5\n"
"s_and_b32 s60, s60, 7\n"
"v_mfma_f32_16x16x32_bf16 v[228:231], a[88:91], a[80:83], v[228:231]\n"
"s_mul_i32 s62, s60, 786432\n"
"s_add_u32 s16, s18, 5\n"
"v_mfma_f32_16x16x32_bf16 v[232:235], a[92:95], a[80:83], v[232:235]\n"
"s_waitcnt vmcnt(11) lgkmcnt(0)\n"
"s_barrier\n"
"ds_read_b64_tr_b16 a[84:85], v38 offset:25600\n"
"v_mfma_f32_16x16x32_bf16 v[128:131], a[180:183], a[144:147], v[128:131]\n"
"buffer_load_dwordx4 v46, s[28:31], s62 offen lds\n"
"v_mfma_f32_16x16x32_bf16 v[132:135], a[184:187], a[144:147], v[132:135]\n"
"s_sub_u32 s17, s16, 10\n"
"s_cselect_b32 s16, s16, s17\n"
"ds_read_b64_tr_b16 a[86:87], v38 offset:26112\n"
"v_mfma_f32_16x16x32_bf16 v[136:139], a[188:191], a[144:147], v[136:139]\n"
"ds_read_b64_tr_b16 a[88:89], v39 offset:25600\n"
"v_xor_b32 v62, s16, v61\n"
"v_mfma_f32_16x16x32_bf16 v[140:143], a[180:183], a[148:151], v[140:143]\n"
"ds_read_b64_tr_b16 a[90:91], v39 offset:26112\n"
"v_lshl_add_u32 v62, v62, 5, v60\n"
"v_mfma_f32_16x16x32_bf16 v[144:147], a[184:187], a[148:151], v[144:147]\n"
"buffer_load_dwordx4 v47, s[28:31], s62 offen offset:1024 lds\n"
"v_mfma_f32_16x16x32_bf16 v[148:151], a[188:191], a[148:151], v[148:151]\n"
"ds_read_b64_tr_b16 a[92:93], v40 offset:25600\n"
"v_accvgpr_write_b32 a114, v62\n"
"v_mfma_f32_16x16x32_bf16 v[152:155], a[180:183], a[152:155], v[152:155]\n"
"s_add_u32 s16, s18, 0\n"
"ds_read_b64_tr_b16 a[94:95], v40 offset:26112\n"
"v_mfma_f32_16x16x32_bf16 v[156:159], a[184:187], a[152:155], v[156:159]\n"
"s_sub_u32 s17, s16, 10\n"
"s_cselect_b32 s16, s16, s17\n"
"ds_read_b128 a[48:51], v29 offset:0\n"
"v_mfma_f32_16x16x32_bf16 v[160:163], a[188:191], a[152:155], v[160:163]\n"
"buffer_load_dwordx4 v48, s[28:31], s62 offen offset:2048 lds\n"
"v_mfma_f32_16x16x32_bf16 v[164:167], a[180:183], a[156:159], v[164:167]\n"
"s_add_u32 m0, s53, 9216\n"
"ds_read_b128 a[52:55], v29 offset:2048\n"
"s_lshl_b32 s61, s60, 7\n"
"v_mfma_f32_16x16x32_bf16 v[168:171], a[184:187], a[156:159], v[168:171]\n"
"ds_read_b128 a[56:59], v31 offset:0\n"
"v_xor_b32 v62, s16, v61\n"
"v_mfma_f32_16x16x32_bf16 v[172:175], a[188:191], a[156:159], v[172:175]\n"
"ds_read_b128 a[60:63], v31 offset:2048\n"
"v_lshl_add_u32 v62, v62, 5, v60\n"
"v_mfma_f32_16x16x32_bf16 v[176:179], a[180:183], a[160:163], v[176:179]\n"
"buffer_load_dwordx4 v41, s[24:27], s61 offen lds\n"
"v_mfma_f32_16x16x32_bf16 v[180:183], a[184:187], a[160:163], v[180:183]\n"
"ds_read_b128 a[64:67], v33 offset:0\n"
"v_accvgpr_write_b32 a115, v62\n"
"v_mfma_f32_16x16x32_bf16 v[184:187], a[188:191], a[160:163], v[184:187]\n"
"s_add_u32 s16, s18, 6\n"
"ds_read_b128 a[68:71], v33 offset:2048\n"
"v_mfma_f32_16x16x32_bf16 v[188:191], a[180:183], a[164:167], v[188:191]\n"
"s_sub_u32 s17, s16, 10\n"
"s_cselect_b32 s16, s16, s17\n"
"ds_read_b128 a[72:75], v35 offset:0\n"
"v_mfma_f32_16x16x32_bf16 v[192:195], a[184:187], a[164:167], v[192:195]\n"
"ds_read_b128 a[76:79], v35 offset:2048\n"
"v_xor_b32 v62, s16, v61\n"
"v_mfma_f32_16x16x32_bf16 v[196:199], a[188:191], a[164:167], v[196:199]\n"
"ds_read_b128 a[80:83], v37 offset:0\n"
"v_lshl_add_u32 v62, v62, 5, v60\n"
"v_mfma_f32_16x16x32_bf16 v[200:203], a[180:183], a[168:171], v[200:203]\n"
"buffer_load_dwordx4 v42, s[24:27], s61 offen offset:1024 lds\n"
"v_mfma_f32_16x16x32_bf16 v[204:207], a[184:187], a[168:171], v[204:207]\n"
"v_accvgpr_write_b32 a116, v62\n"
"v_mfma_f32_16x16x32_bf16 v[208:211], a[188:191], a[168:171], v[208:211]\n"
"s_add_u32 s16, s18, 7\n"
"v_mfma_f32_16x16x32_bf16 v[212:215], a[180:183], a[172:175], v[212:215]\n"
"s_sub_u32 s17, s16, 10\n"
"s_cselect_b32 s16, s16, s17\n"
"v_mfma_f32_16x16x32_bf16 v[216:219], a[184:187], a[172:175], v[216:219]\n"
"buffer_load_dwordx4 v43, s[24:27], s61 offen offset:2048 lds\n"
"v_mfma_f32_16x16x32_bf16 v[220:223], a[188:191], a[172:175], v[220:223]\n"
"v_xor_b32 v62, s16, v61\n"
"v_mfma_f32_16x16x32_bf16 v[224:227], a[180:183], a[176:179], v[224:227]\n"
"v_lshl_add_u32 v62, v62, 5, v60\n"
"v_mfma_f32_16x16x32_bf16 v[228:231], a[184:187], a[176:179], v[228:231]\n"
"v_accvgpr_write_b32 a117, v62\n"
"v_mfma_f32_16x16x32_bf16 v[232:235], a[188:191], a[176:179], v[232:235]\n"
"s_waitcnt vmcnt(9) lgkmcnt(0)\n"
"s_barrier\n"
"ds_read_b64_tr_b16 a[180:181], v38 offset:16384\n"
"v_mfma_f32_16x16x32_bf16 v[128:131], a[84:87], a[48:51], v[128:131]\n"
"s_add_u32 s16, s18, 1\n"
"buffer_load_dwordx4 v44, s[24:27], s61 offen offset:3072 lds\n"
"v_mfma_f32_16x16x32_bf16 v[132:135], a[88:91], a[48:51], v[132:135]\n"
"s_add_u32 m0, s53, 13312\n"
"ds_read_b64_tr_b16 a[182:183], v38 offset:16896\n"
"v_mfma_f32_16x16x32_bf16 v[136:139], a[92:95], a[48:51], v[136:139]\n"
"s_sub_u32 s17, s16, 10\n"
"s_cselect_b32 s16, s16, s17\n"
"ds_read_b64_tr_b16 a[184:185], v39 offset:16384\n"
"v_mfma_f32_16x16x32_bf16 v[140:143], a[84:87], a[52:55], v[140:143]\n"
"ds_read_b64_tr_b16 a[186:187], v39 offset:16896\n"
"v_xor_b32 v62, s16, v61\n"
"v_mfma_f32_16x16x32_bf16 v[144:147], a[88:91], a[52:55], v[144:147]\n"
"s_mov_b32 exec_hi, 0\n"
"buffer_load_dwordx4 v45, s[24:27], s61 offen lds\n"
"s_mov_b32 exec_hi, -1\n"
"v_mfma_f32_16x16x32_bf16 v[148:151], a[92:95], a[52:55], v[148:151]\n"
"s_add_u32 m0, s53, 25600\n"
"ds_read_b64_tr_b16 a[188:189], v40 offset:16384\n"
"s_add_u32 s62, s62, 393216\n"
"v_mfma_f32_16x16x32_bf16 v[152:155], a[84:87], a[56:59], v[152:155]\n"
"ds_read_b64_tr_b16 a[190:191], v40 offset:16896\n"
"v_lshl_add_u32 v62, v62, 5, v60\n"
"v_mfma_f32_16x16x32_bf16 v[156:159], a[88:91], a[56:59], v[156:159]\n"
"ds_read_b128 a[144:147], v28 offset:4608\n"
"v_accvgpr_write_b32 a118, v62\n"
"v_mfma_f32_16x16x32_bf16 v[160:163], a[92:95], a[56:59], v[160:163]\n"
"s_add_u32 s16, s18, 8\n"
"buffer_load_dwordx4 v46, s[28:31], s62 offen lds\n"
"v_mfma_f32_16x16x32_bf16 v[164:167], a[84:87], a[60:63], v[164:167]\n"
"s_sub_u32 s17, s16, 10\n"
"s_cselect_b32 s16, s16, s17\n"
"ds_read_b128 a[148:151], v28 offset:6656\n"
"v_mfma_f32_16x16x32_bf16 v[168:171], a[88:91], a[60:63], v[168:171]\n"
"ds_read_b128 a[152:155], v30 offset:4608\n"
"v_xor_b32 v62, s16, v61\n"
"v_mfma_f32_16x16x32_bf16 v[172:175], a[92:95], a[60:63], v[172:175]\n"
"ds_read_b128 a[156:159], v30 offset:6656\n"
"v_lshl_add_u32 v62, v62, 5, v60\n"
"v_mfma_f32_16x16x32_bf16 v[176:179], a[84:87], a[64:67], v[176:179]\n"
"buffer_load_dwordx4 v47, s[28:31], s62 offen offset:1024 lds\n"
"v_mfma_f32_16x16x32_bf16 v[180:183], a[88:91], a[64:67], v[180:183]\n"
"ds_read_b128 a[160:163], v32 offset:4608\n"
"v_accvgpr_write_b32 a119, v62\n"
"v_mfma_f32_16x16x32_bf16 v[184:187], a[92:95], a[64:67], v[184:187]\n"
"s_add_u32 s16, s18, 2\n"
"ds_read_b128 a[164:167], v32 offset:6656\n"
"v_mfma_f32_16x16x32_bf16 v[188:191], a[84:87], a[68:71], v[188:191]\n"
"s_sub_u32 s17, s16, 10\n"
"s_cselect_b32 s16, s16, s17\n"
"ds_read_b128 a[168:171], v34 offset:4608\n"
"v_mfma_f32_16x16x32_bf16 v[192:195], a[88:91], a[68:71], v[192:195]\n"
"ds_read_b128 a[172:175], v34 offset:6656\n"
"v_xor_b32 v62, s16, v61\n"
"v_mfma_f32_16x16x32_bf16 v[196:199], a[92:95], a[68:71], v[196:199]\n"
"buffer_load_dwordx4 v48, s[28:31], s62 offen offset:2048 lds\n"
"v_mfma_f32_16x16x32_bf16 v[200:203], a[84:87], a[72:75], v[200:203]\n"
"s_add_u32 m0, s53, 28672\n"
"ds_read_b128 a[176:179], v36 offset:4608\n"
"s_mov_b32 s63, 0\n"
"v_mfma_f32_16x16x32_bf16 v[204:207], a[88:91], a[72:75], v[204:207]\n"
"v_lshl_add_u32 v62, v62, 5, v60\n"
"v_mfma_f32_16x16x32_bf16 v[208:211], a[92:95], a[72:75], v[208:211]\n"
"buffer_load_dwordx4 v2, s[32:35], s63 offen nt lds\n"
"v_accvgpr_write_b32 a120, v62\n"
"s_nop 15\n"
"s_nop 8\n"
"v_mfma_f32_16x16x32_bf16 v[212:215], a[84:87], a[76:79], v[212:215]\n"
"s_add_u32 s16, s18, 9\n"
"buffer_load_dwordx4 v3, s[32:35], s63 offen offset:1024 nt lds\n"
"s_nop 15\n"
"s_nop 9\n"
"v_mfma_f32_16x16x32_bf16 v[216:219], a[88:91], a[76:79], v[216:219]\n"
"s_sub_u32 s17, s16, 10\n"
"s_cselect_b32 s16, s16, s17\n"
"buffer_load_dwordx4 v4, s[32:35], s63 offen offset:2048 nt lds\n"
"s_nop 15\n"
"s_nop 9\n"
"v_mfma_f32_16x16x32_bf16 v[220:223], a[92:95], a[76:79], v[220:223]\n"
"buffer_load_dwordx4 v5, s[32:35], s63 offen offset:3072 nt lds\n"
"s_add_u32 m0, s53, 32768\n"
"v_xor_b32 v62, s16, v61\n"
"s_nop 15\n"
"s_nop 7\n"
"v_mfma_f32_16x16x32_bf16 v[224:227], a[84:87], a[80:83], v[224:227]\n"
"buffer_load_dwordx4 v6, s[32:35], s63 offen nt lds\n"
"v_lshl_add_u32 v62, v62, 5, v60\n"
"s_nop 15\n"
"s_nop 9\n"
"v_mfma_f32_16x16x32_bf16 v[228:231], a[88:91], a[80:83], v[228:231]\n"
"buffer_load_dwordx4 v7, s[32:35], s63 offen offset:1024 nt lds\n"
"s_add_u32 m0, s53, 34816\n"
"s_mov_b32 s63, 24576\n"
"v_accvgpr_write_b32 a121, v62\n"
"s_nop 15\n"
"s_nop 6\n"
"v_mfma_f32_16x16x32_bf16 v[232:235], a[92:95], a[80:83], v[232:235]\n"
"s_waitcnt vmcnt(17) lgkmcnt(0)\n"
"s_barrier\n"
"buffer_load_dwordx4 v2, s[32:35], s63 offen nt lds\n"
"s_and_b32 s18, s52, 1\n"
"s_nop 15\n"
"s_nop 8\n"
"v_mfma_f32_16x16x32_bf16 v[128:131], a[180:183], a[144:147], v[128:131]\n"
"s_lshl_b32 s18, s18, 1\n"
"buffer_load_dwordx4 v3, s[32:35], s63 offen offset:1024 nt lds\n"
"s_nop 15\n"
"s_nop 9\n"
"v_mfma_f32_16x16x32_bf16 v[132:135], a[184:187], a[144:147], v[132:135]\n"
"s_mul_i32 s19, s52, 5120\n"
"buffer_load_dwordx4 v4, s[32:35], s63 offen offset:2048 nt lds\n"
"s_nop 15\n"
"s_nop 9\n"
"v_mfma_f32_16x16x32_bf16 v[136:139], a[188:191], a[144:147], v[136:139]\n"
"buffer_load_dwordx4 v5, s[32:35], s63 offen offset:3072 nt lds\n"
"s_add_u32 m0, s53, 38912\n"
"v_add_u32 v60, 0, v0\n"
"s_nop 15\n"
"s_nop 7\n"
"v_mfma_f32_16x16x32_bf16 v[140:143], a[180:183], a[148:151], v[140:143]\n"
"buffer_load_dwordx4 v6, s[32:35], s63 offen nt lds\n"
"v_mul_u32_u24 v61, 205, v60\n"
"s_nop 15\n"
"s_nop 9\n"
"v_mfma_f32_16x16x32_bf16 v[144:147], a[184:187], a[148:151], v[144:147]\n"
"buffer_load_dwordx4 v7, s[32:35], s63 offen offset:1024 nt lds\n"
"s_mov_b32 s63, 98304\n"
"v_lshrrev_b32 v61, 12, v61\n"
"s_nop 15\n"
"s_nop 7\n"
"v_mfma_f32_16x16x32_bf16 v[148:151], a[188:191], a[148:151], v[148:151]\n"
"ds_read_b64_tr_b16 a[84:85], v38 offset:19456\n"
"buffer_load_dwordx4 a[0:3], v2, s[32:35], s63 offen offset:0 nt\n"
"v_mul_u32_u24 v62, 20, v61\n"
"s_nop 15\n"
"s_nop 7\n"
"v_mfma_f32_16x16x32_bf16 v[152:155], a[180:183], a[152:155], v[152:155]\n"
"ds_read_b64_tr_b16 a[86:87], v38 offset:19968\n"
"buffer_load_dwordx4 a[4:7], v3, s[32:35], s63 offen offset:1024 nt\n"
"v_sub_u32 v60, v60, v62\n"
"s_nop 15\n"
"s_nop 6\n"
"v_mfma_f32_16x16x32_bf16 v[156:159], a[184:187], a[152:155], v[156:159]\n"
"ds_read_b64_tr_b16 a[88:89], v39 offset:19456\n"
"buffer_load_dwordx4 a[8:11], v4, s[32:35], s63 offen offset:2048 nt\n"
"v_xor_b32 v60, s18, v60\n"
"s_nop 15\n"
"s_nop 7\n"
"v_mfma_f32_16x16x32_bf16 v[160:163], a[188:191], a[152:155], v[160:163]\n"
"ds_read_b64_tr_b16 a[90:91], v39 offset:19968\n"
"buffer_load_dwordx4 a[12:15], v5, s[32:35], s63 offen offset:3072 nt\n"
"v_lshrrev_b32 v62, 2, v61\n"
"s_nop 15\n"
"s_nop 6\n"
"v_mfma_f32_16x16x32_bf16 v[164:167], a[180:183], a[156:159], v[164:167]\n"
"ds_read_b64_tr_b16 a[92:93], v40 offset:19456\n"
"buffer_load_dwordx4 a[16:19], v6, s[32:35], s63 offen offset:0 nt\n"
"v_mad_u32_u24 v61, v62, 12, v61\n"
"s_nop 15\n"
"s_nop 7\n"
"v_mfma_f32_16x16x32_bf16 v[168:171], a[184:187], a[156:159], v[168:171]\n"
"ds_read_b64_tr_b16 a[94:95], v40 offset:19968\n"
"buffer_load_dwordx4 a[20:23], v7, s[32:35], s63 offen offset:1024 nt\n"
"s_mov_b32 s63, 122880\n"
"v_mul_u32_u24 v61, 1280, v61\n"
"s_nop 15\n"
"s_nop 5\n"
"v_mfma_f32_16x16x32_bf16 v[172:175], a[188:191], a[156:159], v[172:175]\n"
"ds_read_b128 a[48:51], v29 offset:4608\n"
"buffer_load_dwordx4 a[24:27], v2, s[32:35], s63 offen offset:0 nt\n"
"v_lshl_add_u32 v60, v60, 4, v61\n"
"s_nop 15\n"
"s_nop 7\n"
"v_mfma_f32_16x16x32_bf16 v[176:179], a[180:183], a[160:163], v[176:179]\n"
"ds_read_b128 a[52:55], v29 offset:6656\n"
"buffer_load_dwordx4 a[28:31], v3, s[32:35], s63 offen offset:1024 nt\n"
"v_add_u32 v60, s19, v60\n"
"s_nop 15\n"
"s_nop 6\n"
"v_mfma_f32_16x16x32_bf16 v[180:183], a[184:187], a[160:163], v[180:183]\n"
"ds_read_b128 a[56:59], v31 offset:4608\n"
"buffer_load_dwordx4 a[32:35], v4, s[32:35], s63 offen offset:2048 nt\n"
"v_accvgpr_write_b32 a122, v60\n"
"s_nop 15\n"
"s_nop 7\n"
"v_mfma_f32_16x16x32_bf16 v[184:187], a[188:191], a[160:163], v[184:187]\n"
"ds_read_b128 a[60:63], v31 offset:6656\n"
"buffer_load_dwordx4 a[36:39], v5, s[32:35], s63 offen offset:3072 nt\n"
"v_add_u32 v60, 64, v0\n"
"s_nop 15\n"
"s_nop 6\n"
"v_mfma_f32_16x16x32_bf16 v[188:191], a[180:183], a[164:167], v[188:191]\n"
"ds_read_b128 a[64:67], v33 offset:4608\n"
"buffer_load_dwordx4 a[40:43], v6, s[32:35], s63 offen offset:0 nt\n"
"v_mul_u32_u24 v61, 205, v60\n"
"s_nop 15\n"
"s_nop 7\n"
"v_mfma_f32_16x16x32_bf16 v[192:195], a[184:187], a[164:167], v[192:195]\n"
"ds_read_b128 a[68:71], v33 offset:6656\n"
"buffer_load_dwordx4 a[44:47], v7, s[32:35], s63 offen offset:1024 nt\n"
"s_mov_b32 s63, 147456\n"
"v_lshrrev_b32 v61, 12, v61\n"
"s_nop 15\n"
"s_nop 5\n"
"v_mfma_f32_16x16x32_bf16 v[196:199], a[188:191], a[164:167], v[196:199]\n"
"ds_read_b128 a[72:75], v35 offset:4608\n"
"buffer_load_dwordx4 a[96:99], v2, s[32:35], s63 offen offset:0 nt\n"
"v_mul_u32_u24 v62, 20, v61\n"
"s_nop 15\n"
"s_nop 7\n"
"v_mfma_f32_16x16x32_bf16 v[200:203], a[180:183], a[168:171], v[200:203]\n"
"ds_read_b128 a[76:79], v35 offset:6656\n"
"buffer_load_dwordx4 a[100:103], v3, s[32:35], s63 offen offset:1024 nt\n"
"v_sub_u32 v60, v60, v62\n"
"s_nop 15\n"
"s_nop 6\n"
"v_mfma_f32_16x16x32_bf16 v[204:207], a[184:187], a[168:171], v[204:207]\n"
"ds_read_b128 a[80:83], v37 offset:4608\n"
"buffer_load_dwordx4 a[104:107], v4, s[32:35], s63 offen offset:2048 nt\n"
"v_xor_b32 v60, s18, v60\n"
"s_nop 15\n"
"s_nop 7\n"
"v_mfma_f32_16x16x32_bf16 v[208:211], a[188:191], a[168:171], v[208:211]\n"
"buffer_load_dwordx4 a[108:111], v5, s[32:35], s63 offen offset:3072 nt\n"
"v_lshrrev_b32 v62, 2, v61\n"
"s_nop 15\n"
"s_nop 8\n"
"v_mfma_f32_16x16x32_bf16 v[212:215], a[180:183], a[172:175], v[212:215]\n"
"buffer_load_dwordx4 a[128:131], v6, s[32:35], s63 offen offset:0 nt\n"
"v_mad_u32_u24 v61, v62, 12, v61\n"
"s_nop 15\n"
"s_nop 9\n"
"v_mfma_f32_16x16x32_bf16 v[216:219], a[184:187], a[172:175], v[216:219]\n"
"buffer_load_dwordx4 a[132:135], v7, s[32:35], s63 offen offset:1024 nt\n"
"s_add_u32 m0, s53, 16384\n"
"s_add_u32 s60, s74, 6\n"
"s_and_b32 s60, s60, 7\n"
"s_mul_i32 s62, s60, 786432\n"
"s_nop 15\n"
"s_nop 5\n"
"v_mfma_f32_16x16x32_bf16 v[220:223], a[188:191], a[172:175], v[220:223]\n"
"v_mul_u32_u24 v61, 1280, v61\n"
"v_mfma_f32_16x16x32_bf16 v[224:227], a[180:183], a[176:179], v[224:227]\n"
"v_lshl_add_u32 v60, v60, 4, v61\n"
"v_mfma_f32_16x16x32_bf16 v[228:231], a[184:187], a[176:179], v[228:231]\n"
"buffer_load_dwordx4 v46, s[28:31], s62 offen lds\n"
"v_mfma_f32_16x16x32_bf16 v[232:235], a[188:191], a[176:179], v[232:235]\n"
"s_waitcnt vmcnt(34) lgkmcnt(0)\n"
"s_barrier\n"
"ds_read_b64_tr_b16 a[180:181], v38 offset:22528\n"
"v_mfma_f32_16x16x32_bf16 v[128:131], a[84:87], a[48:51], v[128:131]\n"
"ds_read_b64_tr_b16 a[182:183], v38 offset:23040\n"
"v_add_u32 v60, s19, v60\n"
"v_mfma_f32_16x16x32_bf16 v[132:135], a[88:91], a[48:51], v[132:135]\n"
"ds_read_b64_tr_b16 a[184:185], v39 offset:22528\n"
"v_subrev_u32 v60, 1024, v60\n"
"v_mfma_f32_16x16x32_bf16 v[136:139], a[92:95], a[48:51], v[136:139]\n"
"buffer_load_dwordx4 v47, s[28:31], s62 offen offset:1024 lds\n"
"v_mfma_f32_16x16x32_bf16 v[140:143], a[84:87], a[52:55], v[140:143]\n"
"ds_read_b64_tr_b16 a[186:187], v39 offset:23040\n"
"v_accvgpr_write_b32 a123, v60\n"
"v_mfma_f32_16x16x32_bf16 v[144:147], a[88:91], a[52:55], v[144:147]\n"
"ds_read_b64_tr_b16 a[188:189], v40 offset:22528\n"
"v_add_u32 v60, 128, v0\n"
"v_mfma_f32_16x16x32_bf16 v[148:151], a[92:95], a[52:55], v[148:151]\n"
"ds_read_b64_tr_b16 a[190:191], v40 offset:23040\n"
"v_mul_u32_u24 v61, 205, v60\n"
"v_mfma_f32_16x16x32_bf16 v[152:155], a[84:87], a[56:59], v[152:155]\n"
"buffer_load_dwordx4 v48, s[28:31], s62 offen offset:2048 lds\n"
"v_mfma_f32_16x16x32_bf16 v[156:159], a[88:91], a[56:59], v[156:159]\n"
"s_add_u32 m0, s53, 0\n"
"ds_read_b128 a[144:147], v28 offset:9216\n"
"s_lshl_b32 s61, s60, 7\n"
"v_mfma_f32_16x16x32_bf16 v[160:163], a[92:95], a[56:59], v[160:163]\n"
"ds_read_b128 a[148:151], v28 offset:11264\n"
"v_lshrrev_b32 v61, 12, v61\n"
"v_mfma_f32_16x16x32_bf16 v[164:167], a[84:87], a[60:63], v[164:167]\n"
"ds_read_b128 a[152:155], v30 offset:9216\n"
"v_mul_u32_u24 v62, 20, v61\n"
"v_mfma_f32_16x16x32_bf16 v[168:171], a[88:91], a[60:63], v[168:171]\n"
"buffer_load_dwordx4 v41, s[24:27], s61 offen lds\n"
"v_mfma_f32_16x16x32_bf16 v[172:175], a[92:95], a[60:63], v[172:175]\n"
"ds_read_b128 a[156:159], v30 offset:11264\n"
"v_sub_u32 v60, v60, v62\n"
"v_mfma_f32_16x16x32_bf16 v[176:179], a[84:87], a[64:67], v[176:179]\n"
"ds_read_b128 a[160:163], v32 offset:9216\n"
"v_xor_b32 v60, s18, v60\n"
"v_mfma_f32_16x16x32_bf16 v[180:183], a[88:91], a[64:67], v[180:183]\n"
"ds_read_b128 a[164:167], v32 offset:11264\n"
"v_lshrrev_b32 v62, 2, v61\n"
"v_mfma_f32_16x16x32_bf16 v[184:187], a[92:95], a[64:67], v[184:187]\n"
"buffer_load_dwordx4 v42, s[24:27], s61 offen offset:1024 lds\n"
"v_mfma_f32_16x16x32_bf16 v[188:191], a[84:87], a[68:71], v[188:191]\n"
"ds_read_b128 a[168:171], v34 offset:9216\n"
"v_mad_u32_u24 v61, v62, 12, v61\n"
"v_mfma_f32_16x16x32_bf16 v[192:195], a[88:91], a[68:71], v[192:195]\n"
"ds_read_b128 a[172:175], v34 offset:11264\n"
"v_mul_u32_u24 v61, 1280, v61\n"
"v_mfma_f32_16x16x32_bf16 v[196:199], a[92:95], a[68:71], v[196:199]\n"
"ds_read_b128 a[176:179], v36 offset:9216\n"
"v_lshl_add_u32 v60, v60, 4, v61\n"
"v_mfma_f32_16x16x32_bf16 v[200:203], a[84:87], a[72:75], v[200:203]\n"
"buffer_load_dwordx4 v43, s[24:27], s61 offen offset:2048 lds\n"
"v_mfma_f32_16x16x32_bf16 v[204:207], a[88:91], a[72:75], v[204:207]\n"
"v_add_u32 v60, s19, v60\n"
"v_mfma_f32_16x16x32_bf16 v[208:211], a[92:95], a[72:75], v[208:211]\n"
"v_subrev_u32 v60, 2048, v60\n"
"v_mfma_f32_16x16x32_bf16 v[212:215], a[84:87], a[76:79], v[212:215]\n"
"v_accvgpr_write_b32 a124, v60\n"
"v_mfma_f32_16x16x32_bf16 v[216:219], a[88:91], a[76:79], v[216:219]\n"
"buffer_load_dwordx4 v44, s[24:27], s61 offen offset:3072 lds\n"
"s_add_u32 m0, s53, 4096\n"
"v_mfma_f32_16x16x32_bf16 v[220:223], a[92:95], a[76:79], v[220:223]\n"
"v_add_u32 v60, 192, v0\n"
"v_mfma_f32_16x16x32_bf16 v[224:227], a[84:87], a[80:83], v[224:227]\n"
"v_mul_u32_u24 v61, 205, v60\n"
"v_mfma_f32_16x16x32_bf16 v[228:231], a[88:91], a[80:83], v[228:231]\n"
"s_mov_b32 exec_hi, 0\n"
"buffer_load_dwordx4 v45, s[24:27], s61 offen lds\n"
"s_mov_b32 exec_hi, -1\n"
"v_mfma_f32_16x16x32_bf16 v[232:235], a[92:95], a[80:83], v[232:235]\n"
"s_waitcnt vmcnt(38) lgkmcnt(0)\n"
"s_barrier\n"
"s_add_u32 m0, s53, 19456\n"
"s_add_u32 s62, s62, 393216\n"
"v_mfma_f32_16x16x32_bf16 v[128:131], a[180:183], a[144:147], v[128:131]\n"
"ds_read_b64_tr_b16 a[84:85], v38 offset:25600\n"
"v_lshrrev_b32 v61, 12, v61\n"
"v_mfma_f32_16x16x32_bf16 v[132:135], a[184:187], a[144:147], v[132:135]\n"
"ds_read_b64_tr_b16 a[86:87], v38 offset:26112\n"
"v_mul_u32_u24 v62, 20, v61\n"
"v_mfma_f32_16x16x32_bf16 v[136:139], a[188:191], a[144:147], v[136:139]\n"
"buffer_load_dwordx4 v46, s[28:31], s62 offen lds\n"
"v_mfma_f32_16x16x32_bf16 v[140:143], a[180:183], a[148:151], v[140:143]\n"
"ds_read_b64_tr_b16 a[88:89], v39 offset:25600\n"
"v_sub_u32 v60, v60, v62\n"
"v_mfma_f32_16x16x32_bf16 v[144:147], a[184:187], a[148:151], v[144:147]\n"
"ds_read_b64_tr_b16 a[90:91], v39 offset:26112\n"
"v_xor_b32 v60, s18, v60\n"
"v_mfma_f32_16x16x32_bf16 v[148:151], a[188:191], a[148:151], v[148:151]\n"
"ds_read_b64_tr_b16 a[92:93], v40 offset:25600\n"
"v_lshrrev_b32 v62, 2, v61\n"
"v_mfma_f32_16x16x32_bf16 v[152:155], a[180:183], a[152:155], v[152:155]\n"
"buffer_load_dwordx4 v47, s[28:31], s62 offen offset:1024 lds\n"
".Lwalk6_%=:\n"
"s_nop 0\n"
"s_nop 0\n"
"s_nop 0\n"
"s_nop 0\n"
"s_nop 0\n"
"s_nop 0\n"
"s_nop 0\n"
"s_nop 0\n"
"s_nop 0\n"
"s_nop 0\n"
"s_nop 0\n"
"s_nop 0\n"
"s_nop 0\n"
"s_nop 0\n"
"s_nop 0\n"
"s_nop 0\n"
"s_nop 0\n"
"s_nop 0\n"
"s_nop 0\n"
"s_nop 0\n"
"s_nop 0\n"
"s_nop 0\n"
"s_nop 0\n"
"s_nop 0\n"
"s_nop 0\n"
"s_nop 0\n"
"s_nop 0\n"
"s_nop 0\n"
"s_nop 0\n"
"s_nop 0\n"
"s_nop 0\n"
"s_nop 0\n"
"s_nop 0\n"
"s_nop 0\n"
"s_nop 0\n"
"s_nop 0\n"
"s_nop 0\n"
".Lpad5_%=:\n"
"s_cbranch_execz .Lwb_%=\n"
"s_mov_b64 exec, 0\n"
"s_mov_b32 s98, .Lpad10_%=-.Lentry_%=\n"
"s_cmp_eq_u32 s52, 1\n"
"s_cselect_b32 s98, .Lpad11_%=-.Lentry_%=, s98\n"
"s_cmp_eq_u32 s52, 2\n"
"s_cselect_b32 s98, .Lpad12_%=-.Lentry_%=, s98\n"
"s_cmp_eq_u32 s52, 3\n"
"s_cselect_b32 s98, .Lpad13_%=-.Lentry_%=, s98\n"
"s_getpc_b64 s[96:97]\n"
".Lwh60_%=:\n"
"s_sub_u32 s96, s96, .Lwh60_%=-.Lentry_%=\n"
"s_subb_u32 s97, s97, 0\n"
"s_add_u32 s96, s96, s98\n"
"s_addc_u32 s97, s97, 0\n"
"s_swappc_b64 vcc, s[96:97]\n"
"s_mov_b32 s98, .Lpad14_%=-.Lentry_%=\n"
"s_cmp_eq_u32 s52, 1\n"
"s_cselect_b32 s98, .Lpad15_%=-.Lentry_%=, s98\n"
"s_cmp_eq_u32 s52, 2\n"
"s_cselect_b32 s98, .Lpad16_%=-.Lentry_%=, s98\n"
"s_cmp_eq_u32 s52, 3\n"
"s_cselect_b32 s98, .Lpad16_%=-.Lentry_%=, s98\n"
"s_getpc_b64 s[96:97]\n"
".Lwh61_%=:\n"
"s_sub_u32 s96, s96, .Lwh61_%=-.Lentry_%=\n"
"s_subb_u32 s97, s97, 0\n"
"s_add_u32 s96, s96, s98\n"
"s_addc_u32 s97, s97, 0\n"
"s_swappc_b64 vcc, s[96:97]\n"
"s_mov_b64 exec, -1\n"
"v_mfma_f32_16x16x32_bf16 v[156:159], a[184:187], a[152:155], v[156:159]\n"
"ds_read_b64_tr_b16 a[94:95], v40 offset:26112\n"
"v_mad_u32_u24 v61, v62, 12, v61\n"
"v_mfma_f32_16x16x32_bf16 v[160:163], a[188:191], a[152:155], v[160:163]\n"
"ds_read_b128 a[48:51], v29 offset:9216\n"
"v_mul_u32_u24 v61, 1280, v61\n"
"v_mfma_f32_16x16x32_bf16 v[164:167], a[180:183], a[156:159], v[164:167]\n"
"ds_read_b128 a[52:55], v29 offset:11264\n"
"v_lshl_add_u32 v60, v60, 4, v61\n"
"v_mfma_f32_16x16x32_bf16 v[168:171], a[184:187], a[156:159], v[168:171]\n"
"buffer_load_dwordx4 v48, s[28:31], s62 offen offset:2048 lds\n"
"v_mfma_f32_16x16x32_bf16 v[172:175], a[188:191], a[156:159], v[172:175]\n"
"s_add_u32 m0, s53, 22528\n"
"s_add_u32 s60, s74, 7\n"
"s_and_b32 s60, s60, 7\n"
"v_mfma_f32_16x16x32_bf16 v[176:179], a[180:183], a[160:163], v[176:179]\n"
"s_mul_i32 s62, s60, 786432\n"
"ds_read_b128 a[56:59], v31 offset:9216\n"
"v_add_u32 v60, s19, v60\n"
"v_mfma_f32_16x16x32_bf16 v[180:183], a[184:187], a[160:163], v[180:183]\n"
"ds_read_b128 a[60:63], v31 offset:11264\n"
"v_subrev_u32 v60, 3072, v60\n"
"v_mfma_f32_16x16x32_bf16 v[184:187], a[188:191], a[160:163], v[184:187]\n"
"ds_read_b128 a[64:67], v33 offset:9216\n"
"v_accvgpr_write_b32 a125, v60\n"
"v_mfma_f32_16x16x32_bf16 v[188:191], a[180:183], a[164:167], v[188:191]\n"
"buffer_load_dwordx4 v46, s[28:31], s62 offen lds\n"
"v_mfma_f32_16x16x32_bf16 v[192:195], a[184:187], a[164:167], v[192:195]\n"
"ds_read_b128 a[68:71], v33 offset:11264\n"
"v_add_u32 v60, 256, v0\n"
"v_mfma_f32_16x16x32_bf16 v[196:199], a[188:191], a[164:167], v[196:199]\n"
"ds_read_b128 a[72:75], v35 offset:9216\n"
"v_mul_u32_u24 v61, 205, v60\n"
"v_mfma_f32_16x16x32_bf16 v[200:203], a[180:183], a[168:171], v[200:203]\n"
"ds_read_b128 a[76:79], v35 offset:11264\n"
"v_lshrrev_b32 v61, 12, v61\n"
"v_mfma_f32_16x16x32_bf16 v[204:207], a[184:187], a[168:171], v[204:207]\n"
"ds_read_b128 a[80:83], v37 offset:9216\n"
"buffer_load_dwordx4 v47, s[28:31], s62 offen offset:1024 lds\n"
"v_mfma_f32_16x16x32_bf16 v[208:211], a[188:191], a[168:171], v[208:211]\n"
"v_mul_u32_u24 v62, 20, v61\n"
"v_mfma_f32_16x16x32_bf16 v[212:215], a[180:183], a[172:175], v[212:215]\n"
"v_sub_u32 v60, v60, v62\n"
"v_mfma_f32_16x16x32_bf16 v[216:219], a[184:187], a[172:175], v[216:219]\n"
"v_xor_b32 v60, s18, v60\n"
"v_mfma_f32_16x16x32_bf16 v[220:223], a[188:191], a[172:175], v[220:223]\n"
"buffer_load_dwordx4 v48, s[28:31], s62 offen offset:2048 lds\n"
"v_mfma_f32_16x16x32_bf16 v[224:227], a[180:183], a[176:179], v[224:227]\n"
"s_add_u32 m0, s53, 4608\n"
"s_lshl_b32 s61, s60, 7\n"
"v_lshrrev_b32 v62, 2, v61\n"
"v_mfma_f32_16x16x32_bf16 v[228:231], a[184:187], a[176:179], v[228:231]\n"
"v_mad_u32_u24 v61, v62, 12, v61\n"
"v_mfma_f32_16x16x32_bf16 v[232:235], a[188:191], a[176:179], v[232:235]\n"
"s_waitcnt vmcnt(6) lgkmcnt(0)\n"
"s_barrier\n"
"ds_read_b64_tr_b16 a[180:181], v38 offset:16384\n"
"v_mfma_f32_16x16x32_bf16 v[128:131], a[84:87], a[48:51], v[128:131]\n"
"buffer_load_dwordx4 v41, s[24:27], s61 offen lds\n"
"v_mfma_f32_16x16x32_bf16 v[132:135], a[88:91], a[48:51], v[132:135]\n"
"ds_read_b64_tr_b16 a[182:183], v38 offset:16896\n"
"v_mul_u32_u24 v61, 1280, v61\n"
"v_mfma_f32_16x16x32_bf16 v[136:139], a[92:95], a[48:51], v[136:139]\n"
"ds_read_b64_tr_b16 a[184:185], v39 offset:16384\n"
"v_lshl_add_u32 v60, v60, 4, v61\n"
"v_mfma_f32_16x16x32_bf16 v[140:143], a[84:87], a[52:55], v[140:143]\n"
"ds_read_b64_tr_b16 a[186:187], v39 offset:16896\n"
"v_add_u32 v60, s19, v60\n"
"v_mfma_f32_16x16x32_bf16 v[144:147], a[88:91], a[52:55], v[144:147]\n"
"buffer_load_dwordx4 v42, s[24:27], s61 offen offset:1024 lds\n"
"v_mfma_f32_16x16x32_bf16 v[148:151], a[92:95], a[52:55], v[148:151]\n"
"ds_read_b64_tr_b16 a[188:189], v40 offset:16384\n"
"v_accvgpr_write_b32 a126, v60\n"
"v_mfma_f32_16x16x32_bf16 v[152:155], a[84:87], a[56:59], v[152:155]\n"
"s_and_b32 s18, s70, 0xffff3fff\n"
"ds_read_b64_tr_b16 a[190:191], v40 offset:16896\n"
"v_mfma_f32_16x16x32_bf16 v[156:159], a[88:91], a[56:59], v[156:159]\n"
"ds_read_b128 a[144:147], v28 offset:0\n"
"v_lshlrev_b32 v60, 14, v59\n"
"v_mfma_f32_16x16x32_bf16 v[160:163], a[92:95], a[56:59], v[160:163]\n"
"buffer_load_dwordx4 v43, s[24:27], s61 offen offset:2048 lds\n"
"v_mfma_f32_16x16x32_bf16 v[164:167], a[84:87], a[60:63], v[164:167]\n"
"ds_read_b128 a[148:151], v28 offset:2048\n"
"v_add_u32 v60, s18, v60\n"
"v_mfma_f32_16x16x32_bf16 v[168:171], a[88:91], a[60:63], v[168:171]\n"
"ds_read_b128 a[152:155], v30 offset:0\n"
"v_accvgpr_write_b32 a127, v60\n"
"v_mfma_f32_16x16x32_bf16 v[172:175], a[92:95], a[60:63], v[172:175]\n"
"ds_read_b128 a[156:159], v30 offset:2048\n"
"v_mfma_f32_16x16x32_bf16 v[176:179], a[84:87], a[64:67], v[176:179]\n"
"buffer_load_dwordx4 v44, s[24:27], s61 offen offset:3072 lds\n"
"v_mfma_f32_16x16x32_bf16 v[180:183], a[88:91], a[64:67], v[180:183]\n"
"s_add_u32 m0, s53, 8704\n"
"ds_read_b128 a[160:163], v32 offset:0\n"
"v_mfma_f32_16x16x32_bf16 v[184:187], a[92:95], a[64:67], v[184:187]\n"
"ds_read_b128 a[164:167], v32 offset:2048\n"
"v_mfma_f32_16x16x32_bf16 v[188:191], a[84:87], a[68:71], v[188:191]\n"
"ds_read_b128 a[168:171], v34 offset:0\n"
"v_mfma_f32_16x16x32_bf16 v[192:195], a[88:91], a[68:71], v[192:195]\n"
"s_mov_b32 exec_hi, 0\n"
"buffer_load_dwordx4 v45, s[24:27], s61 offen lds\n"
"s_mov_b32 exec_hi, -1\n"
"v_mfma_f32_16x16x32_bf16 v[196:199], a[92:95], a[68:71], v[196:199]\n"
"s_add_u32 m0, s53, 25600\n"
"ds_read_b128 a[172:175], v34 offset:2048\n"
"s_add_u32 s62, s62, 393216\n"
"v_mfma_f32_16x16x32_bf16 v[200:203], a[84:87], a[72:75], v[200:203]\n"
"ds_read_b128 a[176:179], v36 offset:0\n"
"v_mfma_f32_16x16x32_bf16 v[204:207], a[88:91], a[72:75], v[204:207]\n"
"v_mfma_f32_16x16x32_bf16 v[208:211], a[92:95], a[72:75], v[208:211]\n"
"buffer_load_dwordx4 v46, s[28:31], s62 offen lds\n"
"v_mfma_f32_16x16x32_bf16 v[212:215], a[84:87], a[76:79], v[212:215]\n"
"v_mfma_f32_16x16x32_bf16 v[216:219], a[88:91], a[76:79], v[216:219]\n"
"v_mfma_f32_16x16x32_bf16 v[220:223], a[92:95], a[76:79], v[220:223]\n"
"v_mfma_f32_16x16x32_bf16 v[224:227], a[84:87], a[80:83], v[224:227]\n"
"buffer_load_dwordx4 v47, s[28:31], s62 offen offset:1024 lds\n"
"v_mfma_f32_16x16x32_bf16 v[228:231], a[88:91], a[80:83], v[228:231]\n"
"v_mfma_f32_16x16x32_bf16 v[232:235], a[92:95], a[80:83], v[232:235]\n"
"s_waitcnt vmcnt(10) lgkmcnt(0)\n"
"s_barrier\n"
"ds_read_b64_tr_b16 a[84:85], v38 offset:19456\n"
"v_mfma_f32_16x16x32_bf16 v[128:131], a[180:183], a[144:147], v[128:131]\n"
"ds_read_b64_tr_b16 a[86:87], v38 offset:19968\n"
"v_mfma_f32_16x16x32_bf16 v[132:135], a[184:187], a[144:147], v[132:135]\n"
"ds_read_b64_tr_b16 a[88:89], v39 offset:19456\n"
"v_mfma_f32_16x16x32_bf16 v[136:139], a[188:191], a[144:147], v[136:139]\n"
"buffer_load_dwordx4 v48, s[28:31], s62 offen offset:2048 lds\n"
"v_mfma_f32_16x16x32_bf16 v[140:143], a[180:183], a[148:151], v[140:143]\n"
"s_add_u32 m0, s53, 16384\n"
"s_mov_b32 s60, 8\n"
"s_mul_i32 s62, s60, 786432\n"
"v_mfma_f32_16x16x32_bf16 v[144:147], a[184:187], a[148:151], v[144:147]\n"
"ds_read_b64_tr_b16 a[90:91], v39 offset:19968\n"
"v_mfma_f32_16x16x32_bf16 v[148:151], a[188:191], a[148:151], v[148:151]\n"
"ds_read_b64_tr_b16 a[92:93], v40 offset:19456\n"
"v_mfma_f32_16x16x32_bf16 v[152:155], a[180:183], a[152:155], v[152:155]\n"
"buffer_load_dwordx4 v46, s[28:31], s62 offen lds\n"
"v_mfma_f32_16x16x32_bf16 v[156:159], a[184:187], a[152:155], v[156:159]\n"
"ds_read_b64_tr_b16 a[94:95], v40 offset:19968\n"
"v_mfma_f32_16x16x32_bf16 v[160:163], a[188:191], a[152:155], v[160:163]\n"
"ds_read_b128 a[48:51], v29 offset:0\n"
"v_mfma_f32_16x16x32_bf16 v[164:167], a[180:183], a[156:159], v[164:167]\n"
"ds_read_b128 a[52:55], v29 offset:2048\n"
"v_mfma_f32_16x16x32_bf16 v[168:171], a[184:187], a[156:159], v[168:171]\n"
"buffer_load_dwordx4 v47, s[28:31], s62 offen offset:1024 lds\n"
"v_mfma_f32_16x16x32_bf16 v[172:175], a[188:191], a[156:159], v[172:175]\n"
"ds_read_b128 a[56:59], v31 offset:0\n"
"v_mfma_f32_16x16x32_bf16 v[176:179], a[180:183], a[160:163], v[176:179]\n"
"ds_read_b128 a[60:63], v31 offset:2048\n"
"v_mfma_f32_16x16x32_bf16 v[180:183], a[184:187], a[160:163], v[180:183]\n"
"ds_read_b128 a[64:67], v33 offset:0\n"
"v_mfma_f32_16x16x32_bf16 v[184:187], a[188:191], a[160:163], v[184:187]\n"
"ds_read_b128 a[68:71], v33 offset:2048\n"
"v_mfma_f32_16x16x32_bf16 v[188:191], a[180:183], a[164:167], v[188:191]\n"
"ds_read_b128 a[72:75], v35 offset:0\n"
"v_mfma_f32_16x16x32_bf16 v[192:195], a[184:187], a[164:167], v[192:195]\n"
"buffer_load_dwordx4 v48, s[28:31], s62 offen offset:2048 lds\n"
"v_mfma_f32_16x16x32_bf16 v[196:199], a[188:191], a[164:167], v[196:199]\n"
"s_add_u32 m0, s53, 9216\n"
"ds_read_b128 a[76:79], v35 offset:2048\n"
"s_lshl_b32 s61, s60, 7\n"
"v_mfma_f32_16x16x32_bf16 v[200:203], a[180:183], a[168:171], v[200:203]\n"
"ds_read_b128 a[80:83], v37 offset:0\n"
"v_mfma_f32_16x16x32_bf16 v[204:207], a[184:187], a[168:171], v[204:207]\n"
"v_mfma_f32_16x16x32_bf16 v[208:211], a[188:191], a[168:171], v[208:211]\n"
"buffer_load_dwordx4 v41, s[24:27], s61 offen lds\n"
"v_mfma_f32_16x16x32_bf16 v[212:215], a[180:183], a[172:175], v[212:215]\n"
"v_mfma_f32_16x16x32_bf16 v[216:219], a[184:187], a[172:175], v[216:219]\n"
"v_mfma_f32_16x16x32_bf16 v[220:223], a[188:191], a[172:175], v[220:223]\n"
"v_mfma_f32_16x16x32_bf16 v[224:227], a[180:183], a[176:179], v[224:227]\n"
"buffer_load_dwordx4 v42, s[24:27], s61 offen offset:1024 lds\n"
"v_mfma_f32_16x16x32_bf16 v[228:231], a[184:187], a[176:179], v[228:231]\n"
"v_mfma_f32_16x16x32_bf16 v[232:235], a[188:191], a[176:179], v[232:235]\n"
"s_waitcnt vmcnt(8) lgkmcnt(0)\n"
"s_barrier\n"
"ds_read_b64_tr_b16 a[180:181], v38 offset:22528\n"
"v_mfma_f32_16x16x32_bf16 v[128:131], a[84:87], a[48:51], v[128:131]\n"
"ds_read_b64_tr_b16 a[182:183], v38 offset:23040\n"
"v_mfma_f32_16x16x32_bf16 v[132:135], a[88:91], a[48:51], v[132:135]\n"
"buffer_load_dwordx4 v43, s[24:27], s61 offen offset:2048 lds\n"
"v_mfma_f32_16x16x32_bf16 v[136:139], a[92:95], a[48:51], v[136:139]\n"
"ds_read_b64_tr_b16 a[184:185], v39 offset:22528\n"
"v_mfma_f32_16x16x32_bf16 v[140:143], a[84:87], a[52:55], v[140:143]\n"
"ds_read_b64_tr_b16 a[186:187], v39 offset:23040\n"
"v_mfma_f32_16x16x32_bf16 v[144:147], a[88:91], a[52:55], v[144:147]\n"
"ds_read_b64_tr_b16 a[188:189], v40 offset:22528\n"
"v_mfma_f32_16x16x32_bf16 v[148:151], a[92:95], a[52:55], v[148:151]\n"
"buffer_load_dwordx4 v44, s[24:27], s61 offen offset:3072 lds\n"
"v_mfma_f32_16x16x32_bf16 v[152:155], a[84:87], a[56:59], v[152:155]\n"
"s_add_u32 m0, s53, 13312\n"
"ds_read_b64_tr_b16 a[190:191], v40 offset:23040\n"
"v_mfma_f32_16x16x32_bf16 v[156:159], a[88:91], a[56:59], v[156:159]\n"
"ds_read_b128 a[144:147], v28 offset:4608\n"
"v_mfma_f32_16x16x32_bf16 v[160:163], a[92:95], a[56:59], v[160:163]\n"
"ds_read_b128 a[148:151], v28 offset:6656\n"
"v_mfma_f32_16x16x32_bf16 v[164:167], a[84:87], a[60:63], v[164:167]\n"
"s_mov_b32 exec_hi, 0\n"
"buffer_load_dwordx4 v45, s[24:27], s61 offen lds\n"
"s_mov_b32 exec_hi, -1\n"
"v_mfma_f32_16x16x32_bf16 v[168:171], a[88:91], a[60:63], v[168:171]\n"
"s_add_u32 m0, s53, 19456\n"
"ds_read_b128 a[152:155], v30 offset:4608\n"
"s_add_u32 s62, s62, 393216\n"
"v_mfma_f32_16x16x32_bf16 v[172:175], a[92:95], a[60:63], v[172:175]\n"
"ds_read_b128 a[156:159], v30 offset:6656\n"
"v_mfma_f32_16x16x32_bf16 v[176:179], a[84:87], a[64:67], v[176:179]\n"
"ds_read_b128 a[160:163], v32 offset:4608\n"
"v_mfma_f32_16x16x32_bf16 v[180:183], a[88:91], a[64:67], v[180:183]\n"
"buffer_load_dwordx4 v46, s[28:31], s62 offen lds\n"
"v_mfma_f32_16x16x32_bf16 v[184:187], a[92:95], a[64:67], v[184:187]\n"
"ds_read_b128 a[164:167], v32 offset:6656\n"
"v_mfma_f32_16x16x32_bf16 v[188:191], a[84:87], a[68:71], v[188:191]\n"
"ds_read_b128 a[168:171], v34 offset:4608\n"
"v_mfma_f32_16x16x32_bf16 v[192:195], a[88:91], a[68:71], v[192:195]\n"
"ds_read_b128 a[172:175], v34 offset:6656\n"
"v_mfma_f32_16x16x32_bf16 v[196:199], a[92:95], a[68:71], v[196:199]\n"
"ds_read_b128 a[176:179], v36 offset:4608\n"
"v_mfma_f32_16x16x32_bf16 v[200:203], a[84:87], a[72:75], v[200:203]\n"
"buffer_load_dwordx4 v47, s[28:31], s62 offen offset:1024 lds\n"
"v_mfma_f32_16x16x32_bf16 v[204:207], a[88:91], a[72:75], v[204:207]\n"
"v_mfma_f32_16x16x32_bf16 v[208:211], a[92:95], a[72:75], v[208:211]\n"
"v_mfma_f32_16x16x32_bf16 v[212:215], a[84:87], a[76:79], v[212:215]\n"
"v_mfma_f32_16x16x32_bf16 v[216:219], a[88:91], a[76:79], v[216:219]\n"
"buffer_load_dwordx4 v48, s[28:31], s62 offen offset:2048 lds\n"
"v_mfma_f32_16x16x32_bf16 v[220:223], a[92:95], a[76:79], v[220:223]\n"
"v_mfma_f32_16x16x32_bf16 v[224:227], a[84:87], a[80:83], v[224:227]\n"
"s_add_u32 m0, s53, 22528\n"
"s_mov_b32 s60, 9\n"
"s_mul_i32 s62, s60, 786432\n"
"v_mfma_f32_16x16x32_bf16 v[228:231], a[88:91], a[80:83], v[228:231]\n"
"v_mfma_f32_16x16x32_bf16 v[232:235], a[92:95], a[80:83], v[232:235]\n"
"s_waitcnt vmcnt(11) lgkmcnt(0)\n"
"s_barrier\n"
"ds_read_b64_tr_b16 a[84:85], v38 offset:25600\n"
"v_mfma_f32_16x16x32_bf16 v[128:131], a[180:183], a[144:147], v[128:131]\n"
"buffer_load_dwordx4 v46, s[28:31], s62 offen lds\n"
"v_mfma_f32_16x16x32_bf16 v[132:135], a[184:187], a[144:147], v[132:135]\n"
"ds_read_b64_tr_b16 a[86:87], v38 offset:26112\n"
"v_mfma_f32_16x16x32_bf16 v[136:139], a[188:191], a[144:147], v[136:139]\n"
"ds_read_b64_tr_b16 a[88:89], v39 offset:25600\n"
"v_mfma_f32_16x16x32_bf16 v[140:143], a[180:183], a[148:151], v[140:143]\n"
"ds_read_b64_tr_b16 a[90:91], v39 offset:26112\n"
"v_mfma_f32_16x16x32_bf16 v[144:147], a[184:187], a[148:151], v[144:147]\n"
"buffer_load_dwordx4 v47, s[28:31], s62 offen offset:1024 lds\n"
"v_mfma_f32_16x16x32_bf16 v[148:151], a[188:191], a[148:151], v[148:151]\n"
"ds_read_b64_tr_b16 a[92:93], v40 offset:25600\n"
"v_mfma_f32_16x16x32_bf16 v[152:155], a[180:183], a[152:155], v[152:155]\n"
"ds_read_b64_tr_b16 a[94:95], v40 offset:26112\n"
"v_mfma_f32_16x16x32_bf16 v[156:159], a[184:187], a[152:155], v[156:159]\n"
"ds_read_b128 a[48:51], v29 offset:4608\n"
"v_mfma_f32_16x16x32_bf16 v[160:163], a[188:191], a[152:155], v[160:163]\n"
"buffer_load_dwordx4 v48, s[28:31], s62 offen offset:2048 lds\n"
"v_mfma_f32_16x16x32_bf16 v[164:167], a[180:183], a[156:159], v[164:167]\n"
"s_add_u32 m0, s53, 0\n"
"ds_read_b128 a[52:55], v29 offset:6656\n"
"s_lshl_b32 s61, s60, 7\n"
"v_mfma_f32_16x16x32_bf16 v[168:171], a[184:187], a[156:159], v[168:171]\n"
"ds_read_b128 a[56:59], v31 offset:4608\n"
"v_mfma_f32_16x16x32_bf16 v[172:175], a[188:191], a[156:159], v[172:175]\n"
"ds_read_b128 a[60:63], v31 offset:6656\n"
"v_mfma_f32_16x16x32_bf16 v[176:179], a[180:183], a[160:163], v[176:179]\n"
"buffer_load_dwordx4 v41, s[24:27], s61 offen lds\n"
"v_mfma_f32_16x16x32_bf16 v[180:183], a[184:187], a[160:163], v[180:183]\n"
"ds_read_b128 a[64:67], v33 offset:4608\n"
"v_mfma_f32_16x16x32_bf16 v[184:187], a[188:191], a[160:163], v[184:187]\n"
"ds_read_b128 a[68:71], v33 offset:6656\n"
"v_mfma_f32_16x16x32_bf16 v[188:191], a[180:183], a[164:167], v[188:191]\n"
"ds_read_b128 a[72:75], v35 offset:4608\n"
"v_mfma_f32_16x16x32_bf16 v[192:195], a[184:187], a[164:167], v[192:195]\n"
"ds_read_b128 a[76:79], v35 offset:6656\n"
"v_mfma_f32_16x16x32_bf16 v[196:199], a[188:191], a[164:167], v[196:199]\n"
"ds_read_b128 a[80:83], v37 offset:4608\n"
"v_mfma_f32_16x16x32_bf16 v[200:203], a[180:183], a[168:171], v[200:203]\n"
"buffer_load_dwordx4 v42, s[24:27], s61 offen offset:1024 lds\n"
"v_mfma_f32_16x16x32_bf16 v[204:207], a[184:187], a[168:171], v[204:207]\n"
"v_mfma_f32_16x16x32_bf16 v[208:211], a[188:191], a[168:171], v[208:211]\n"
"v_mfma_f32_16x16x32_bf16 v[212:215], a[180:183], a[172:175], v[212:215]\n"
"v_mfma_f32_16x16x32_bf16 v[216:219], a[184:187], a[172:175], v[216:219]\n"
"buffer_load_dwordx4 v43, s[24:27], s61 offen offset:2048 lds\n"
"v_mfma_f32_16x16x32_bf16 v[220:223], a[188:191], a[172:175], v[220:223]\n"
"v_mfma_f32_16x16x32_bf16 v[224:227], a[180:183], a[176:179], v[224:227]\n"
"v_mfma_f32_16x16x32_bf16 v[228:231], a[184:187], a[176:179], v[228:231]\n"
"v_mfma_f32_16x16x32_bf16 v[232:235], a[188:191], a[176:179], v[232:235]\n"
"s_waitcnt vmcnt(9) lgkmcnt(0)\n"
"s_barrier\n"
"ds_read_b64_tr_b16 a[180:181], v38 offset:16384\n"
"v_mfma_f32_16x16x32_bf16 v[128:131], a[84:87], a[48:51], v[128:131]\n"
"buffer_load_dwordx4 v44, s[24:27], s61 offen offset:3072 lds\n"
"v_mfma_f32_16x16x32_bf16 v[132:135], a[88:91], a[48:51], v[132:135]\n"
"s_add_u32 m0, s53, 4096\n"
"ds_read_b64_tr_b16 a[182:183], v38 offset:16896\n"
"v_mfma_f32_16x16x32_bf16 v[136:139], a[92:95], a[48:51], v[136:139]\n"
"ds_read_b64_tr_b16 a[184:185], v39 offset:16384\n"
"v_mfma_f32_16x16x32_bf16 v[140:143], a[84:87], a[52:55], v[140:143]\n"
"ds_read_b64_tr_b16 a[186:187], v39 offset:16896\n"
"v_mfma_f32_16x16x32_bf16 v[144:147], a[88:91], a[52:55], v[144:147]\n"
"s_mov_b32 exec_hi, 0\n"
"buffer_load_dwordx4 v45, s[24:27], s61 offen lds\n"
"s_mov_b32 exec_hi, -1\n"
"v_mfma_f32_16x16x32_bf16 v[148:151], a[92:95], a[52:55], v[148:151]\n"
"s_add_u32 m0, s53, 25600\n"
"ds_read_b64_tr_b16 a[188:189], v40 offset:16384\n"
"s_add_u32 s62, s62, 393216\n"
"v_mfma_f32_16x16x32_bf16 v[152:155], a[84:87], a[56:59], v[152:155]\n"
"ds_read_b64_tr_b16 a[190:191], v40 offset:16896\n"
"v_mfma_f32_16x16x32_bf16 v[156:159], a[88:91], a[56:59], v[156:159]\n"
"ds_read_b128 a[144:147], v28 offset:9216\n"
"v_mfma_f32_16x16x32_bf16 v[160:163], a[92:95], a[56:59], v[160:163]\n"
"buffer_load_dwordx4 v46, s[28:31], s62 offen lds\n"
"v_mfma_f32_16x16x32_bf16 v[164:167], a[84:87], a[60:63], v[164:167]\n"
"ds_read_b128 a[148:151], v28 offset:11264\n"
"v_mfma_f32_16x16x32_bf16 v[168:171], a[88:91], a[60:63], v[168:171]\n"
"ds_read_b128 a[152:155], v30 offset:9216\n"
"v_mfma_f32_16x16x32_bf16 v[172:175], a[92:95], a[60:63], v[172:175]\n"
"ds_read_b128 a[156:159], v30 offset:11264\n"
"v_mfma_f32_16x16x32_bf16 v[176:179], a[84:87], a[64:67], v[176:179]\n"
"buffer_load_dwordx4 v47, s[28:31], s62 offen offset:1024 lds\n"
"v_mfma_f32_16x16x32_bf16 v[180:183], a[88:91], a[64:67], v[180:183]\n"
"ds_read_b128 a[160:163], v32 offset:9216\n"
"v_mfma_f32_16x16x32_bf16 v[184:187], a[92:95], a[64:67], v[184:187]\n"
"ds_read_b128 a[164:167], v32 offset:11264\n"
"v_mfma_f32_16x16x32_bf16 v[188:191], a[84:87], a[68:71], v[188:191]\n"
"ds_read_b128 a[168:171], v34 offset:9216\n"
"v_mfma_f32_16x16x32_bf16 v[192:195], a[88:91], a[68:71], v[192:195]\n"
"ds_read_b128 a[172:175], v34 offset:11264\n"
"v_mfma_f32_16x16x32_bf16 v[196:199], a[92:95], a[68:71], v[196:199]\n"
"buffer_load_dwordx4 v48, s[28:31], s62 offen offset:2048 lds\n"
"v_mfma_f32_16x16x32_bf16 v[200:203], a[84:87], a[72:75], v[200:203]\n"
"s_mov_b32 s63, 172032\n"
"ds_read_b128 a[176:179], v36 offset:9216\n"
"v_mfma_f32_16x16x32_bf16 v[204:207], a[88:91], a[72:75], v[204:207]\n"
"v_mfma_f32_16x16x32_bf16 v[208:211], a[92:95], a[72:75], v[208:211]\n"
"buffer_load_dwordx4 a[136:139], v2, s[32:35], s63 offen offset:0 nt\n"
"v_mfma_f32_16x16x32_bf16 v[212:215], a[84:87], a[76:79], v[212:215]\n"
"buffer_load_dwordx4 a[140:143], v3, s[32:35], s63 offen offset:1024 nt\n"
"v_mfma_f32_16x16x32_bf16 v[216:219], a[88:91], a[76:79], v[216:219]\n"
"buffer_load_dwordx4 a[244:247], v4, s[32:35], s63 offen offset:2048 nt\n"
"v_mfma_f32_16x16x32_bf16 v[220:223], a[92:95], a[76:79], v[220:223]\n"
"buffer_load_dwordx4 a[248:251], v5, s[32:35], s63 offen offset:3072 nt\n"
"v_mfma_f32_16x16x32_bf16 v[224:227], a[84:87], a[80:83], v[224:227]\n"
"buffer_load_dwordx4 a[252:255], v6, s[32:35], s63 offen offset:0 nt\n"
"v_mfma_f32_16x16x32_bf16 v[228:231], a[88:91], a[80:83], v[228:231]\n"
"buffer_load_dwordx4 v[252:255], v7, s[32:35], s63 offen offset:1024 nt\n"
"v_mfma_f32_16x16x32_bf16 v[232:235], a[92:95], a[80:83], v[232:235]\n"
"s_waitcnt vmcnt(17) lgkmcnt(0)\n"
"s_barrier\n"
"ds_read_b64_tr_b16 a[84:85], v38 offset:19456\n"
"v_mfma_f32_16x16x32_bf16 v[128:131], a[180:183], a[144:147], v[128:131]\n"
"ds_read_b64_tr_b16 a[86:87], v38 offset:19968\n"
"v_mfma_f32_16x16x32_bf16 v[132:135], a[184:187], a[144:147], v[132:135]\n"
"ds_read_b64_tr_b16 a[88:89], v39 offset:19456\n"
"v_mfma_f32_16x16x32_bf16 v[136:139], a[188:191], a[144:147], v[136:139]\n"
"ds_read_b64_tr_b16 a[90:91], v39 offset:19968\n"
"v_mfma_f32_16x16x32_bf16 v[140:143], a[180:183], a[148:151], v[140:143]\n"
"ds_read_b64_tr_b16 a[92:93], v40 offset:19456\n"
"v_mfma_f32_16x16x32_bf16 v[144:147], a[184:187], a[148:151], v[144:147]\n"
"ds_read_b64_tr_b16 a[94:95], v40 offset:19968\n"
"v_mfma_f32_16x16x32_bf16 v[148:151], a[188:191], a[148:151], v[148:151]\n"
"ds_read_b128 a[48:51], v29 offset:9216\n"
"v_mfma_f32_16x16x32_bf16 v[152:155], a[180:183], a[152:155], v[152:155]\n"
"ds_read_b128 a[52:55], v29 offset:11264\n"
"v_mfma_f32_16x16x32_bf16 v[156:159], a[184:187], a[152:155], v[156:159]\n"
"ds_read_b128 a[56:59], v31 offset:9216\n"
"v_mfma_f32_16x16x32_bf16 v[160:163], a[188:191], a[152:155], v[160:163]\n"
"ds_read_b128 a[60:63], v31 offset:11264\n"
"v_mfma_f32_16x16x32_bf16 v[164:167], a[180:183], a[156:159], v[164:167]\n"
"ds_read_b128 a[64:67], v33 offset:9216\n"
"v_mfma_f32_16x16x32_bf16 v[168:171], a[184:187], a[156:159], v[168:171]\n"
"ds_read_b128 a[68:71], v33 offset:11264\n"
"v_mfma_f32_16x16x32_bf16 v[172:175], a[188:191], a[156:159], v[172:175]\n"
"ds_read_b128 a[72:75], v35 offset:9216\n"
"v_mfma_f32_16x16x32_bf16 v[176:179], a[180:183], a[160:163], v[176:179]\n"
"ds_read_b128 a[76:79], v35 offset:11264\n"
"v_mfma_f32_16x16x32_bf16 v[180:183], a[184:187], a[160:163], v[180:183]\n"
"ds_read_b128 a[80:83], v37 offset:9216\n"
"v_mfma_f32_16x16x32_bf16 v[184:187], a[188:191], a[160:163], v[184:187]\n"
"v_mfma_f32_16x16x32_bf16 v[188:191], a[180:183], a[164:167], v[188:191]\n"
"v_mfma_f32_16x16x32_bf16 v[192:195], a[184:187], a[164:167], v[192:195]\n"
"v_mfma_f32_16x16x32_bf16 v[196:199], a[188:191], a[164:167], v[196:199]\n"
"v_mfma_f32_16x16x32_bf16 v[200:203], a[180:183], a[168:171], v[200:203]\n"
"v_mfma_f32_16x16x32_bf16 v[204:207], a[184:187], a[168:171], v[204:207]\n"
"v_mfma_f32_16x16x32_bf16 v[208:211], a[188:191], a[168:171], v[208:211]\n"
"v_mfma_f32_16x16x32_bf16 v[212:215], a[180:183], a[172:175], v[212:215]\n"
"v_mfma_f32_16x16x32_bf16 v[216:219], a[184:187], a[172:175], v[216:219]\n"
"v_mfma_f32_16x16x32_bf16 v[220:223], a[188:191], a[172:175], v[220:223]\n"
"v_mfma_f32_16x16x32_bf16 v[224:227], a[180:183], a[176:179], v[224:227]\n"
"s_add_u32 m0, s53, 16384\n"
"s_mov_b32 s63, 49152\n"
"v_mfma_f32_16x16x32_bf16 v[228:231], a[184:187], a[176:179], v[228:231]\n"
"v_mfma_f32_16x16x32_bf16 v[232:235], a[188:191], a[176:179], v[232:235]\n"
"s_waitcnt vmcnt(6) lgkmcnt(0)\n"
"s_barrier\n"
"ds_read_b64_tr_b16 a[180:181], v38 offset:22528\n"
"ds_read_b64_tr_b16 a[182:183], v38 offset:23040\n"
"v_mfma_f32_16x16x32_bf16 v[128:131], a[84:87], a[48:51], v[128:131]\n"
"ds_read_b64_tr_b16 a[184:185], v39 offset:22528\n"
"v_mfma_f32_16x16x32_bf16 v[132:135], a[88:91], a[48:51], v[132:135]\n"
"ds_read_b64_tr_b16 a[186:187], v39 offset:23040\n"
"v_mfma_f32_16x16x32_bf16 v[136:139], a[92:95], a[48:51], v[136:139]\n"
"ds_read_b64_tr_b16 a[188:189], v40 offset:22528\n"
"ds_read_b64_tr_b16 a[190:191], v40 offset:23040\n"
"buffer_load_dwordx4 v2, s[32:35], s63 offen nt lds\n"
"v_mfma_f32_16x16x32_bf16 v[140:143], a[84:87], a[52:55], v[140:143]\n"
"ds_read_b128 a[144:147], v28 offset:0\n"
"v_mfma_f32_16x16x32_bf16 v[144:147], a[88:91], a[52:55], v[144:147]\n"
"ds_read_b128 a[148:151], v28 offset:2048\n"
"v_mfma_f32_16x16x32_bf16 v[148:151], a[92:95], a[52:55], v[148:151]\n"
"ds_read_b128 a[152:155], v30 offset:0\n"
"ds_read_b128 a[156:159], v30 offset:2048\n"
"v_mfma_f32_16x16x32_bf16 v[152:155], a[84:87], a[56:59], v[152:155]\n"
"ds_read_b128 a[160:163], v32 offset:0\n"
"v_mfma_f32_16x16x32_bf16 v[156:159], a[88:91], a[56:59], v[156:159]\n"
"ds_read_b128 a[164:167], v32 offset:2048\n"
"buffer_load_dwordx4 v3, s[32:35], s63 offen offset:1024 nt lds\n"
"v_mfma_f32_16x16x32_bf16 v[160:163], a[92:95], a[56:59], v[160:163]\n"
"ds_read_b128 a[168:171], v34 offset:0\n"
"ds_read_b128 a[172:175], v34 offset:2048\n"
"v_mfma_f32_16x16x32_bf16 v[164:167], a[84:87], a[60:63], v[164:167]\n"
"ds_read_b128 a[176:179], v36 offset:0\n"
"v_mfma_f32_16x16x32_bf16 v[168:171], a[88:91], a[60:63], v[168:171]\n"
"ds_read_b64_tr_b16 a[228:229], v38 offset:25600\n"
"v_mfma_f32_16x16x32_bf16 v[172:175], a[92:95], a[60:63], v[172:175]\n"
"ds_read_b64_tr_b16 a[230:231], v38 offset:26112\n"
"v_mfma_f32_16x16x32_bf16 v[176:179], a[84:87], a[64:67], v[176:179]\n"
"ds_read_b64_tr_b16 a[232:233], v39 offset:25600\n"
"ds_read_b64_tr_b16 a[234:235], v39 offset:26112\n"
"buffer_load_dwordx4 v4, s[32:35], s63 offen offset:2048 nt lds\n"
"v_mfma_f32_16x16x32_bf16 v[180:183], a[88:91], a[64:67], v[180:183]\n"
"ds_read_b64_tr_b16 a[236:237], v40 offset:25600\n"
"v_mfma_f32_16x16x32_bf16 v[184:187], a[92:95], a[64:67], v[184:187]\n"
"ds_read_b64_tr_b16 a[238:239], v40 offset:26112\n"
"v_mfma_f32_16x16x32_bf16 v[188:191], a[84:87], a[68:71], v[188:191]\n"
"ds_read_b128 a[192:195], v29 offset:0\n"
"ds_read_b128 a[196:199], v29 offset:2048\n"
"v_mfma_f32_16x16x32_bf16 v[192:195], a[88:91], a[68:71], v[192:195]\n"
"ds_read_b128 a[200:203], v31 offset:0\n"
"v_mfma_f32_16x16x32_bf16 v[196:199], a[92:95], a[68:71], v[196:199]\n"
"ds_read_b128 a[204:207], v31 offset:2048\n"
"buffer_load_dwordx4 v5, s[32:35], s63 offen offset:3072 nt lds\n"
"v_mfma_f32_16x16x32_bf16 v[200:203], a[84:87], a[72:75], v[200:203]\n"
"ds_read_b128 a[208:211], v33 offset:0\n"
"ds_read_b128 a[212:215], v33 offset:2048\n"
"v_mfma_f32_16x16x32_bf16 v[204:207], a[88:91], a[72:75], v[204:207]\n"
"s_add_u32 m0, s53, 20480\n"
"ds_read_b128 a[216:219], v35 offset:0\n"
"v_mfma_f32_16x16x32_bf16 v[208:211], a[92:95], a[72:75], v[208:211]\n"
"ds_read_b128 a[220:223], v35 offset:2048\n"
"v_mfma_f32_16x16x32_bf16 v[212:215], a[84:87], a[76:79], v[212:215]\n"
"ds_read_b128 a[224:227], v37 offset:0\n"
"v_mfma_f32_16x16x32_bf16 v[216:219], a[88:91], a[76:79], v[216:219]\n"
"buffer_load_dwordx4 v6, s[32:35], s63 offen nt lds\n"
"v_mfma_f32_16x16x32_bf16 v[220:223], a[92:95], a[76:79], v[220:223]\n"
"v_mfma_f32_16x16x32_bf16 v[224:227], a[84:87], a[80:83], v[224:227]\n"
"v_mfma_f32_16x16x32_bf16 v[228:231], a[88:91], a[80:83], v[228:231]\n"
"v_mfma_f32_16x16x32_bf16 v[232:235], a[92:95], a[80:83], v[232:235]\n"
"s_waitcnt lgkmcnt(0)\n"
"s_barrier\n"
"v_mfma_f32_16x16x32_bf16 v[224:227], a[180:183], a[176:179], v[224:227]\n"
"ds_read_b128 a[48:51], v20 offset:28672\n"
"buffer_load_dwordx4 v7, s[32:35], s63 offen offset:1024 nt lds\n"
"s_add_u32 m0, s53, 22528\n"
"v_mfma_f32_16x16x32_bf16 v[224:227], a[228:231], a[224:227], v[224:227]\n"
"s_mov_b32 s63, 73728\n"
"ds_read_b128 a[52:55], v21 offset:28672\n"
"v_readfirstlane_b32 s58, v124\n"
"v_readfirstlane_b32 s59, v125\n"
"v_mfma_f32_16x16x32_bf16 v[228:231], a[184:187], a[176:179], v[228:231]\n"
"ds_read_b128 a[56:59], v20 offset:28800\n"
"v_mfma_f32_16x16x32_bf16 v[228:231], a[232:235], a[224:227], v[228:231]\n"
"ds_read_b128 a[60:63], v21 offset:28800\n"
"v_mfma_f32_16x16x32_bf16 v[232:235], a[188:191], a[176:179], v[232:235]\n"
"ds_read_b128 a[64:67], v20 offset:28928\n"
"v_mul_f32 v224, s64, v224\n"
"v_mul_f32 v225, s64, v225\n"
"v_mul_f32 v226, s64, v226\n"
"v_mul_f32 v227, s64, v227\n"
"v_cvt_pk_bf16_f32 v64, v224, v225\n"
"v_cvt_pk_bf16_f32 v65, v226, v227\n"
"v_mfma_f32_16x16x32_bf16 v[232:235], a[236:239], a[224:227], v[232:235]\n"
"buffer_load_dwordx4 v2, s[32:35], s63 offen nt lds\n"
"v_mfma_f32_16x16x32_bf16 v[128:131], a[180:183], a[144:147], v[128:131]\n"
"ds_read_b128 a[68:71], v21 offset:28928\n"
"ds_write_b64 v55, v[64:65]\n"
"v_mul_f32 v228, s64, v228\n"
"v_mul_f32 v229, s64, v229\n"
"v_mul_f32 v230, s64, v230\n"
"v_mul_f32 v231, s64, v231\n"
"v_cvt_pk_bf16_f32 v66, v228, v229\n"
"v_cvt_pk_bf16_f32 v67, v230, v231\n"
"v_mfma_f32_16x16x32_bf16 v[128:131], a[228:231], a[192:195], v[128:131]\n"
"ds_write_b64 v56, v[66:67]\n"
"v_mfma_f32_16x16x32_bf16 v[132:135], a[184:187], a[144:147], v[132:135]\n"
"ds_read_b128 a[72:75], v12 offset:28672\n"
"v_mul_f32 v232, s64, v232\n"
"v_mul_f32 v233, s64, v233\n"
"v_mul_f32 v234, s64, v234\n"
"v_mul_f32 v235, s64, v235\n"
"v_cvt_pk_bf16_f32 v68, v232, v233\n"
"v_cvt_pk_bf16_f32 v69, v234, v235\n"
"v_mfma_f32_16x16x32_bf16 v[132:135], a[232:235], a[192:195], v[132:135]\n"
"buffer_load_dwordx4 v3, s[32:35], s63 offen offset:1024 nt lds\n"
"v_mfma_f32_16x16x32_bf16 v[136:139], a[188:191], a[144:147], v[136:139]\n"
"ds_read_b128 a[76:79], v13 offset:28672\n"
"ds_write_b64 v57, v[68:69]\n"
"v_mul_f32 v128, s64, v128\n"
"v_mul_f32 v129, s64, v129\n"
"v_mul_f32 v130, s64, v130\n"
"v_mul_f32 v131, s64, v131\n"
"v_cvt_pk_bf16_f32 v70, v128, v129\n"
"v_cvt_pk_bf16_f32 v71, v130, v131\n"
"v_mfma_f32_16x16x32_bf16 v[136:139], a[236:239], a[192:195], v[136:139]\n"
"ds_write_b64 v49, v[70:71] offset:0\n"
"v_mfma_f32_16x16x32_bf16 v[140:143], a[180:183], a[148:151], v[140:143]\n"
"ds_read_b128 a[80:83], v12 offset:28800\n"
"v_mul_f32 v132, s64, v132\n"
"v_mul_f32 v133, s64, v133\n"
"v_mul_f32 v134, s64, v134\n"
"v_mul_f32 v135, s64, v135\n"
"v_cvt_pk_bf16_f32 v72, v132, v133\n"
"v_cvt_pk_bf16_f32 v73, v134, v135\n"
"v_mfma_f32_16x16x32_bf16 v[140:143], a[228:231], a[196:199], v[140:143]\n"
"ds_write_b64 v50, v[72:73] offset:0\n"
"v_mfma_f32_16x16x32_bf16 v[144:147], a[184:187], a[148:151], v[144:147]\n"
".Lpad6_%=:\n"
"s_cbranch_execz .Lwb_%=\n"
"ds_read_b128 a[84:87], v13 offset:28800\n"
"v_mul_f32 v136, s64, v136\n"
"v_mul_f32 v137, s64, v137\n"
"v_mul_f32 v138, s64, v138\n"
"v_mul_f32 v139, s64, v139\n"
"v_cvt_pk_bf16_f32 v74, v136, v137\n"
"v_cvt_pk_bf16_f32 v75, v138, v139\n"
"v_mfma_f32_16x16x32_bf16 v[144:147], a[232:235], a[196:199], v[144:147]\n"
"buffer_load_dwordx4 v4, s[32:35], s63 offen offset:2048 nt lds\n"
"v_mfma_f32_16x16x32_bf16 v[148:151], a[188:191], a[148:151], v[148:151]\n"
"ds_read_b128 a[88:91], v12 offset:28928\n"
"ds_write_b64 v51, v[74:75] offset:0\n"
"v_mul_f32 v140, s64, v140\n"
"v_mul_f32 v141, s64, v141\n"
"v_mul_f32 v142, s64, v142\n"
"v_mul_f32 v143, s64, v143\n"
"v_cvt_pk_bf16_f32 v76, v140, v141\n"
"v_cvt_pk_bf16_f32 v77, v142, v143\n"
"v_mfma_f32_16x16x32_bf16 v[148:151], a[236:239], a[196:199], v[148:151]\n"
"ds_write_b64 v49, v[76:77] offset:6144\n"
"v_mfma_f32_16x16x32_bf16 v[152:155], a[180:183], a[152:155], v[152:155]\n"
"ds_read_b128 a[92:95], v13 offset:28928\n"
"v_mul_f32 v144, s64, v144\n"
"v_mul_f32 v145, s64, v145\n"
"v_mul_f32 v146, s64, v146\n"
"v_mul_f32 v147, s64, v147\n"
"v_cvt_pk_bf16_f32 v78, v144, v145\n"
"v_cvt_pk_bf16_f32 v79, v146, v147\n"
"v_mfma_f32_16x16x32_bf16 v[152:155], a[228:231], a[200:203], v[152:155]\n"
"buffer_load_dwordx4 v5, s[32:35], s63 offen offset:3072 nt lds\n"
"v_mfma_f32_16x16x32_bf16 v[156:159], a[184:187], a[152:155], v[156:159]\n"
"ds_write_b64 v50, v[78:79] offset:6144\n"
"v_mul_f32 v148, s64, v148\n"
"v_mul_f32 v149, s64, v149\n"
"v_mul_f32 v150, s64, v150\n"
"v_mul_f32 v151, s64, v151\n"
"v_cvt_pk_bf16_f32 v64, v148, v149\n"
"v_cvt_pk_bf16_f32 v65, v150, v151\n"
"v_mfma_f32_16x16x32_bf16 v[156:159], a[232:235], a[200:203], v[156:159]\n"
"s_add_u32 m0, s53, 26624\n"
"ds_write_b64 v51, v[64:65] offset:6144\n"
"v_mfma_f32_16x16x32_bf16 v[160:163], a[188:191], a[152:155], v[160:163]\n"
"v_mul_f32 v152, s64, v152\n"
"v_mul_f32 v153, s64, v153\n"
"v_mul_f32 v154, s64, v154\n"
"v_mul_f32 v155, s64, v155\n"
"v_cvt_pk_bf16_f32 v66, v152, v153\n"
"v_cvt_pk_bf16_f32 v67, v154, v155\n"
"v_mfma_f32_16x16x32_bf16 v[160:163], a[236:239], a[200:203], v[160:163]\n"
"ds_write_b64 v49, v[66:67] offset:40960\n"
"v_mfma_f32_16x16x32_bf16 v[164:167], a[180:183], a[156:159], v[164:167]\n"
"v_mul_f32 v156, s64, v156\n"
"v_mul_f32 v157, s64, v157\n"
"v_mul_f32 v158, s64, v158\n"
"v_mul_f32 v159, s64, v159\n"
"v_cvt_pk_bf16_f32 v68, v156, v157\n"
"v_cvt_pk_bf16_f32 v69, v158, v159\n"
"v_mfma_f32_16x16x32_bf16 v[164:167], a[228:231], a[204:207], v[164:167]\n"
"buffer_load_dwordx4 v6, s[32:35], s63 offen nt lds\n"
"v_mfma_f32_16x16x32_bf16 v[168:171], a[184:187], a[156:159], v[168:171]\n"
"ds_write_b64 v50, v[68:69] offset:40960\n"
"v_mul_f32 v160, s64, v160\n"
"v_mul_f32 v161, s64, v161\n"
"v_mul_f32 v162, s64, v162\n"
"v_mul_f32 v163, s64, v163\n"
"v_cvt_pk_bf16_f32 v70, v160, v161\n"
"v_cvt_pk_bf16_f32 v71, v162, v163\n"
"v_mfma_f32_16x16x32_bf16 v[168:171], a[232:235], a[204:207], v[168:171]\n"
"ds_write_b64 v51, v[70:71] offset:40960\n"
"v_mfma_f32_16x16x32_bf16 v[172:175], a[188:191], a[156:159], v[172:175]\n"
"v_mul_f32 v164, s64, v164\n"
"v_mul_f32 v165, s64, v165\n"
"v_mul_f32 v166, s64, v166\n"
"v_mul_f32 v167, s64, v167\n"
"v_cvt_pk_bf16_f32 v72, v164, v165\n"
"v_cvt_pk_bf16_f32 v73, v166, v167\n"
"v_mfma_f32_16x16x32_bf16 v[172:175], a[236:239], a[204:207], v[172:175]\n"
"buffer_load_dwordx4 v7, s[32:35], s63 offen offset:1024 nt lds\n"
"v_mfma_f32_16x16x32_bf16 v[176:179], a[180:183], a[160:163], v[176:179]\n"
"s_waitcnt lgkmcnt(13)\n"
"s_barrier\n"
"ds_write_b64 v49, v[72:73] offset:47104\n"
"v_mul_f32 v168, s64, v168\n"
"v_mul_f32 v169, s64, v169\n"
"v_mul_f32 v170, s64, v170\n"
"v_mul_f32 v171, s64, v171\n"
"v_cvt_pk_bf16_f32 v74, v168, v169\n"
"v_cvt_pk_bf16_f32 v75, v170, v171\n"
"v_mfma_f32_16x16x32_bf16 v[176:179], a[228:231], a[208:211], v[176:179]\n"
"ds_write_b64 v50, v[74:75] offset:47104\n"
"v_mfma_f32_16x16x32_bf16 v[180:183], a[184:187], a[160:163], v[180:183]\n"
"ds_read_b128 a[192:195], v1 offset:12288\n"
"v_mul_f32 v172, s64, v172\n"
"v_mul_f32 v173, s64, v173\n"
"v_mul_f32 v174, s64, v174\n"
"v_mul_f32 v175, s64, v175\n"
"v_cvt_pk_bf16_f32 v76, v172, v173\n"
"v_cvt_pk_bf16_f32 v77, v174, v175\n"
"v_mfma_f32_16x16x32_bf16 v[180:183], a[232:235], a[208:211], v[180:183]\n"
"ds_write_b64 v51, v[76:77] offset:47104\n"
"v_mfma_f32_16x16x32_bf16 v[184:187], a[188:191], a[160:163], v[184:187]\n"
"ds_read_b128 a[196:199], v1 offset:13312\n"
"v_mul_f32 v176, s64, v176\n"
"v_mul_f32 v177, s64, v177\n"
"v_mul_f32 v178, s64, v178\n"
"v_mul_f32 v179, s64, v179\n"
"v_cvt_pk_bf16_f32 v78, v176, v177\n"
"v_cvt_pk_bf16_f32 v79, v178, v179\n"
"v_mfma_f32_16x16x32_bf16 v[184:187], a[236:239], a[208:211], v[184:187]\n"
"ds_write_b64 v52, v[78:79] offset:0\n"
"v_mfma_f32_16x16x32_bf16 v[188:191], a[180:183], a[164:167], v[188:191]\n"
"ds_read_b128 a[200:203], v1 offset:53248\n"
"v_mul_f32 v180, s64, v180\n"
"v_mul_f32 v181, s64, v181\n"
"v_mul_f32 v182, s64, v182\n"
"v_mul_f32 v183, s64, v183\n"
"v_cvt_pk_bf16_f32 v64, v180, v181\n"
"v_cvt_pk_bf16_f32 v65, v182, v183\n"
"v_mfma_f32_16x16x32_bf16 v[188:191], a[228:231], a[212:215], v[188:191]\n"
"ds_write_b64 v53, v[64:65] offset:0\n"
"v_mfma_f32_16x16x32_bf16 v[192:195], a[184:187], a[164:167], v[192:195]\n"
"ds_read_b128 a[204:207], v1 offset:54272\n"
"v_mul_f32 v184, s64, v184\n"
"v_mul_f32 v185, s64, v185\n"
"v_mul_f32 v186, s64, v186\n"
"v_mul_f32 v187, s64, v187\n"
"v_cvt_pk_bf16_f32 v66, v184, v185\n"
"v_cvt_pk_bf16_f32 v67, v186, v187\n"
"v_mfma_f32_16x16x32_bf16 v[192:195], a[232:235], a[212:215], v[192:195]\n"
"ds_write_b64 v54, v[66:67] offset:0\n"
"v_mfma_f32_16x16x32_bf16 v[196:199], a[188:191], a[164:167], v[196:199]\n"
"ds_read_b128 a[208:211], v25 offset:12288\n"
"v_mul_f32 v188, s64, v188\n"
"v_mul_f32 v189, s64, v189\n"
"v_mul_f32 v190, s64, v190\n"
"v_mul_f32 v191, s64, v191\n"
"v_cvt_pk_bf16_f32 v68, v188, v189\n"
"v_cvt_pk_bf16_f32 v69, v190, v191\n"
"v_mfma_f32_16x16x32_bf16 v[196:199], a[236:239], a[212:215], v[196:199]\n"
"ds_write_b64 v52, v[68:69] offset:6144\n"
"v_mfma_f32_16x16x32_bf16 v[200:203], a[180:183], a[168:171], v[200:203]\n"
"ds_read_b128 a[212:215], v25 offset:13312\n"
"v_mul_f32 v192, s64, v192\n"
"v_mul_f32 v193, s64, v193\n"
"v_mul_f32 v194, s64, v194\n"
"v_mul_f32 v195, s64, v195\n"
"v_cvt_pk_bf16_f32 v70, v192, v193\n"
"v_cvt_pk_bf16_f32 v71, v194, v195\n"
"v_mfma_f32_16x16x32_bf16 v[200:203], a[228:231], a[216:219], v[200:203]\n"
"ds_write_b64 v53, v[70:71] offset:6144\n"
"v_mfma_f32_16x16x32_bf16 v[204:207], a[184:187], a[168:171], v[204:207]\n"
"v_mul_f32 v196, s64, v196\n"
"v_mul_f32 v197, s64, v197\n"
"v_mul_f32 v198, s64, v198\n"
"v_mul_f32 v199, s64, v199\n"
"v_cvt_pk_bf16_f32 v72, v196, v197\n"
"v_cvt_pk_bf16_f32 v73, v198, v199\n"
"v_mfma_f32_16x16x32_bf16 v[204:207], a[232:235], a[216:219], v[204:207]\n"
"ds_write_b64 v54, v[72:73] offset:6144\n"
"v_mfma_f32_16x16x32_bf16 v[208:211], a[188:191], a[168:171], v[208:211]\n"
"v_mul_f32 v200, s64, v200\n"
"v_mul_f32 v201, s64, v201\n"
"v_mul_f32 v202, s64, v202\n"
"v_mul_f32 v203, s64, v203\n"
"v_cvt_pk_bf16_f32 v74, v200, v201\n"
"v_cvt_pk_bf16_f32 v75, v202, v203\n"
"v_mfma_f32_16x16x32_bf16 v[208:211], a[236:239], a[216:219], v[208:211]\n"
"ds_write_b64 v52, v[74:75] offset:40960\n"
"v_mfma_f32_16x16x32_bf16 v[212:215], a[180:183], a[172:175], v[212:215]\n"
"v_mul_f32 v204, s64, v204\n"
"v_mul_f32 v205, s64, v205\n"
"v_mul_f32 v206, s64, v206\n"
"v_mul_f32 v207, s64, v207\n"
"v_cvt_pk_bf16_f32 v76, v204, v205\n"
"v_cvt_pk_bf16_f32 v77, v206, v207\n"
"v_mfma_f32_16x16x32_bf16 v[212:215], a[228:231], a[220:223], v[212:215]\n"
"ds_write_b64 v53, v[76:77] offset:40960\n"
"v_mfma_f32_16x16x32_bf16 v[216:219], a[184:187], a[172:175], v[216:219]\n"
"v_mul_f32 v208, s64, v208\n"
"v_mul_f32 v209, s64, v209\n"
"v_mul_f32 v210, s64, v210\n"
"v_mul_f32 v211, s64, v211\n"
"v_cvt_pk_bf16_f32 v78, v208, v209\n"
"v_cvt_pk_bf16_f32 v79, v210, v211\n"
"v_mfma_f32_16x16x32_bf16 v[216:219], a[232:235], a[220:223], v[216:219]\n"
"ds_write_b64 v54, v[78:79] offset:40960\n"
"v_mfma_f32_16x16x32_bf16 v[220:223], a[188:191], a[172:175], v[220:223]\n"
"v_mul_f32 v212, s64, v212\n"
"v_mul_f32 v213, s64, v213\n"
"v_mul_f32 v214, s64, v214\n"
"v_mul_f32 v215, s64, v215\n"
"v_cvt_pk_bf16_f32 v64, v212, v213\n"
"v_cvt_pk_bf16_f32 v65, v214, v215\n"
"v_mfma_f32_16x16x32_bf16 v[220:223], a[236:239], a[220:223], v[220:223]\n"
"s_waitcnt lgkmcnt(3)\n"
"ds_write_b64 v52, v[64:65] offset:47104\n"
"v_mfma_f32_16x16x32_bf16 v[80:83], a[48:51], a[192:195], 0\n"
"v_mul_f32 v216, s64, v216\n"
"v_mul_f32 v217, s64, v217\n"
"v_mul_f32 v218, s64, v218\n"
"v_mul_f32 v219, s64, v219\n"
"v_cvt_pk_bf16_f32 v66, v216, v217\n"
"v_cvt_pk_bf16_f32 v67, v218, v219\n"
"v_mfma_f32_16x16x32_bf16 v[80:83], a[52:55], a[196:199], v[80:83]\n"
"ds_write_b64 v53, v[66:67] offset:47104\n"
"v_mfma_f32_16x16x32_bf16 v[80:83], a[56:59], a[200:203], v[80:83]\n"
"v_mul_f32 v220, s64, v220\n"
"v_mul_f32 v221, s64, v221\n"
"v_mul_f32 v222, s64, v222\n"
"v_mul_f32 v223, s64, v223\n"
"v_cvt_pk_bf16_f32 v68, v220, v221\n"
"v_mfma_f32_16x16x32_bf16 v[80:83], a[60:63], a[204:207], v[80:83]\n"
"v_cvt_pk_bf16_f32 v69, v222, v223\n"
"v_mfma_f32_16x16x32_bf16 v[80:83], a[64:67], a[208:211], v[80:83]\n"
"ds_write_b64 v54, v[68:69] offset:47104\n"
"v_mfma_f32_16x16x32_bf16 v[80:83], a[68:71], a[212:215], v[80:83]\n"
"s_waitcnt vmcnt(12)\n"
"s_waitcnt lgkmcnt(0)\n"
"s_nop 7\n"
"s_branch .Ljoin_%=\n"
".Lve2_%=:\n"
".Lvara_%=:\n"
"s_add_u32 m0, s53, 22528\n"
"s_add_u32 s60, s74, 1\n"
"s_and_b32 s60, s60, 7\n"
"s_mul_i32 s62, s60, 786432\n"
"buffer_load_dwordx4 v46, s[28:31], s62 offen lds\n"
"buffer_load_dwordx4 v47, s[28:31], s62 offen offset:1024 lds\n"
"buffer_load_dwordx4 v48, s[28:31], s62 offen offset:2048 lds\n"
"s_add_u32 m0, s53, 4608\n"
"s_lshl_b32 s61, s60, 7\n"
"buffer_load_dwordx4 v41, s[24:27], s61 offen lds\n"
"buffer_load_dwordx4 v42, s[24:27], s61 offen offset:1024 lds\n"
"buffer_load_dwordx4 v43, s[24:27], s61 offen offset:2048 lds\n"
"buffer_load_dwordx4 v44, s[24:27], s61 offen offset:3072 lds\n"
"s_add_u32 m0, s53, 8704\n"
"s_mov_b32 exec_hi, 0\n"
"buffer_load_dwordx4 v45, s[24:27], s61 offen lds\n"
"s_mov_b32 exec_hi, -1\n"
".Lwalk3_%=:\n"
"s_mov_b64 exec, 0\n"
"s_mov_b32 s98, .Lpad7_%=-.Lentry_%=\n"
"s_cmp_eq_u32 s52, 1\n"
"s_cselect_b32 s98, .Lpad8_%=-.Lentry_%=, s98\n"
"s_cmp_eq_u32 s52, 2\n"
"s_cselect_b32 s98, .Lpad9_%=-.Lentry_%=, s98\n"
"s_cmp_eq_u32 s52, 3\n"
"s_cselect_b32 s98, .Lpad9_%=-.Lentry_%=, s98\n"
"s_getpc_b64 s[96:97]\n"
".Lwh30_%=:\n"
"s_sub_u32 s96, s96, .Lwh30_%=-.Lentry_%=\n"
"s_subb_u32 s97, s97, 0\n"
"s_add_u32 s96, s96, s98\n"
"s_addc_u32 s97, s97, 0\n"
"s_swappc_b64 vcc, s[96:97]\n"
"s_mov_b64 exec, -1\n"
"s_waitcnt vmcnt(13)\n"
"s_barrier\n"
"ds_read_b64_tr_b16 a[180:181], v38 offset:16384\n"
"ds_read_b64_tr_b16 a[182:183], v38 offset:16896\n"
"ds_read_b64_tr_b16 a[184:185], v39 offset:16384\n"
"ds_read_b64_tr_b16 a[186:187], v39 offset:16896\n"
"ds_read_b64_tr_b16 a[188:189], v40 offset:16384\n"
"ds_read_b64_tr_b16 a[190:191], v40 offset:16896\n"
"ds_read_b128 a[144:147], v28 offset:0\n"
"ds_read_b128 a[148:151], v28 offset:2048\n"
"ds_read_b128 a[152:155], v30 offset:0\n"
"ds_read_b128 a[156:159], v30 offset:2048\n"
"ds_read_b128 a[160:163], v32 offset:0\n"
"ds_read_b128 a[164:167], v32 offset:2048\n"
"ds_read_b128 a[168:171], v34 offset:0\n"
"ds_read_b128 a[172:175], v34 offset:2048\n"
"ds_read_b128 a[176:179], v36 offset:0\n"
"s_waitcnt vmcnt(10) lgkmcnt(0)\n"
"s_barrier\n"
"v_mfma_f32_16x16x32_bf16 v[128:131], a[180:183], a[144:147], 0\n"
"s_add_u32 m0, s53, 25600\n"
"ds_read_b64_tr_b16 a[84:85], v38 offset:19456\n"
"s_add_u32 s62, s62, 393216\n"
"v_mfma_f32_16x16x32_bf16 v[132:135], a[184:187], a[144:147], 0\n"
"s_and_b32 s16, s16, 3\n"
"buffer_load_dwordx4 v46, s[28:31], s62 offen lds\n"
"v_mfma_f32_16x16x32_bf16 v[136:139], a[188:191], a[144:147], 0\n"
"s_mul_i32 s16, s16, 245760\n"
"ds_read_b64_tr_b16 a[86:87], v38 offset:19968\n"
"v_mfma_f32_16x16x32_bf16 v[140:143], a[180:183], a[148:151], 0\n"
"s_mul_i32 s17, s56, 320\n"
"ds_read_b64_tr_b16 a[88:89], v39 offset:19456\n"
"v_mfma_f32_16x16x32_bf16 v[144:147], a[184:187], a[148:151], 0\n"
"s_add_u32 s11, s16, s17\n"
"ds_read_b64_tr_b16 a[90:91], v39 offset:19968\n"
"v_mfma_f32_16x16x32_bf16 v[148:151], a[188:191], a[148:151], 0\n"
"s_lshr_b32 s16, s57, 3\n"
"buffer_load_dwordx4 v47, s[28:31], s62 offen offset:1024 lds\n"
"v_mfma_f32_16x16x32_bf16 v[152:155], a[180:183], a[152:155], 0\n"
"s_lshl_b32 s16, s16, 2\n"
"ds_read_b64_tr_b16 a[92:93], v40 offset:19456\n"
"v_mfma_f32_16x16x32_bf16 v[156:159], a[184:187], a[152:155], 0\n"
"s_add_u32 s16, s16, s52\n"
"ds_read_b64_tr_b16 a[94:95], v40 offset:19968\n"
"v_mfma_f32_16x16x32_bf16 v[160:163], a[188:191], a[152:155], 0\n"
"s_lshr_b32 s17, s16, 5\n"
"ds_read_b128 a[48:51], v29 offset:0\n"
"v_mfma_f32_16x16x32_bf16 v[164:167], a[180:183], a[156:159], 0\n"
"s_mul_i32 s17, s17, 245760\n"
"ds_read_b128 a[52:55], v29 offset:2048\n"
"v_mfma_f32_16x16x32_bf16 v[168:171], a[184:187], a[156:159], 0\n"
"s_and_b32 s16, s16, 31\n"
"buffer_load_dwordx4 v48, s[28:31], s62 offen offset:2048 lds\n"
"v_mfma_f32_16x16x32_bf16 v[172:175], a[188:191], a[156:159], 0\n"
"s_lshl_b32 s4, s56, 2\n"
"s_add_u32 s4, s4, s52\n"
"s_mul_i32 s4, s4, 11520\n"
"v_mfma_f32_16x16x32_bf16 v[176:179], a[180:183], a[160:163], 0\n"
"s_mul_i32 s16, s16, 2560\n"
"v_min_u32 v94, 17, v0\n"
"v_lshrrev_b32 v95, 1, v94\n"
"v_mfma_f32_16x16x32_bf16 v[180:183], a[184:187], a[160:163], 0\n"
"ds_read_b128 a[56:59], v31 offset:0\n"
"v_mul_u32_u24 v95, 1024, v95\n"
"v_lshl_add_u32 v94, v94, 7, v95\n"
"v_mfma_f32_16x16x32_bf16 v[184:187], a[188:191], a[160:163], 0\n"
"s_add_u32 s12, s16, s17\n"
"ds_read_b128 a[60:63], v31 offset:2048\n"
"v_mfma_f32_16x16x32_bf16 v[188:191], a[180:183], a[164:167], 0\n"
"s_lshr_b32 s16, s52, 1\n"
"ds_read_b128 a[64:67], v33 offset:0\n"
"v_mfma_f32_16x16x32_bf16 v[192:195], a[184:187], a[164:167], 0\n"
"ds_read_b128 a[68:71], v33 offset:2048\n"
"buffer_load_dword v94, v94, s[24:27], s4 offen offset:1024\n"
"s_lshl_b32 s4, s55, 2\n"
"s_add_u32 s4, s4, s52\n"
"s_mul_i32 s4, s4, 49152\n"
"s_add_u32 s4, s4, 6291456\n"
"v_min_u32 v91, 11, v0\n"
"v_mfma_f32_16x16x32_bf16 v[196:199], a[188:191], a[164:167], 0\n"
"ds_read_b128 a[72:75], v35 offset:0\n"
"v_mul_u32_u24 v95, 86, v91\n"
"v_lshrrev_b32 v95, 8, v95\n"
"v_mfma_f32_16x16x32_bf16 v[200:203], a[180:183], a[168:171], 0\n"
"ds_read_b128 a[76:79], v35 offset:2048\n"
"v_mul_u32_u24 v95, 11904, v95\n"
"v_lshl_add_u32 v91, v91, 7, v95\n"
"v_mfma_f32_16x16x32_bf16 v[204:207], a[184:187], a[168:171], 0\n"
"s_lshl_b32 s16, s16, 10\n"
"ds_read_b128 a[80:83], v37 offset:0\n"
"v_mfma_f32_16x16x32_bf16 v[208:211], a[188:191], a[168:171], 0\n"
"s_and_b32 s17, s52, 1\n"
"buffer_load_dword v91, v91, s[28:31], s4 offen offset:2048\n"
"v_mfma_f32_16x16x32_bf16 v[212:215], a[180:183], a[172:175], 0\n"
"s_add_u32 m0, s53, 16384\n"
"s_add_u32 s60, s74, 2\n"
"s_and_b32 s60, s60, 7\n"
"v_mfma_f32_16x16x32_bf16 v[216:219], a[184:187], a[172:175], 0\n"
"s_mul_i32 s62, s60, 786432\n"
"s_lshl_b32 s17, s17, 3\n"
"v_mfma_f32_16x16x32_bf16 v[220:223], a[188:191], a[172:175], 0\n"
"s_add_u32 s22, s16, s17\n"
"v_mfma_f32_16x16x32_bf16 v[224:227], a[180:183], a[176:179], 0\n"
"s_lshl_b32 s16, s57, 11\n"
"buffer_load_dwordx4 v46, s[28:31], s62 offen lds\n"
"v_mfma_f32_16x16x32_bf16 v[228:231], a[184:187], a[176:179], 0\n"
"s_lshl_b32 s17, s52, 7\n"
"v_mfma_f32_16x16x32_bf16 v[232:235], a[188:191], a[176:179], 0\n"
"s_waitcnt vmcnt(6) lgkmcnt(0)\n"
"s_barrier\n"
"ds_read_b64_tr_b16 a[180:181], v38 offset:22528\n"
"v_mfma_f32_16x16x32_bf16 v[128:131], a[84:87], a[48:51], v[128:131]\n"
"s_add_u32 s16, s16, s17\n"
"ds_read_b64_tr_b16 a[182:183], v38 offset:23040\n"
"v_mfma_f32_16x16x32_bf16 v[132:135], a[88:91], a[48:51], v[132:135]\n"
"s_add_u32 s70, s16, 524288\n"
"buffer_load_dwordx4 v47, s[28:31], s62 offen offset:1024 lds\n"
"v_mfma_f32_16x16x32_bf16 v[136:139], a[92:95], a[48:51], v[136:139]\n"
"ds_read_b64_tr_b16 a[184:185], v39 offset:22528\n"
"v_lshlrev_b32 v90, 9, v59\n"
"v_mfma_f32_16x16x32_bf16 v[140:143], a[84:87], a[52:55], v[140:143]\n"
"ds_read_b64_tr_b16 a[186:187], v39 offset:23040\n"
"v_mfma_f32_16x16x32_bf16 v[144:147], a[88:91], a[52:55], v[144:147]\n"
"ds_read_b64_tr_b16 a[188:189], v40 offset:22528\n"
"v_mfma_f32_16x16x32_bf16 v[148:151], a[92:95], a[52:55], v[148:151]\n"
"ds_read_b64_tr_b16 a[190:191], v40 offset:23040\n"
"v_mfma_f32_16x16x32_bf16 v[152:155], a[84:87], a[56:59], v[152:155]\n"
"buffer_load_dwordx4 v48, s[28:31], s62 offen offset:2048 lds\n"
"v_mfma_f32_16x16x32_bf16 v[156:159], a[88:91], a[56:59], v[156:159]\n"
"s_add_u32 m0, s53, 9216\n"
"ds_read_b128 a[144:147], v28 offset:4608\n"
"s_lshl_b32 s61, s60, 7\n"
"v_mfma_f32_16x16x32_bf16 v[160:163], a[92:95], a[56:59], v[160:163]\n"
"ds_read_b128 a[148:151], v28 offset:6656\n"
"v_mfma_f32_16x16x32_bf16 v[164:167], a[84:87], a[60:63], v[164:167]\n"
"ds_read_b128 a[152:155], v30 offset:4608\n"
"v_mfma_f32_16x16x32_bf16 v[168:171], a[88:91], a[60:63], v[168:171]\n"
"buffer_load_dwordx4 v41, s[24:27], s61 offen lds\n"
"v_mfma_f32_16x16x32_bf16 v[172:175], a[92:95], a[60:63], v[172:175]\n"
"ds_read_b128 a[156:159], v30 offset:6656\n"
"v_mfma_f32_16x16x32_bf16 v[176:179], a[84:87], a[64:67], v[176:179]\n"
"ds_read_b128 a[160:163], v32 offset:4608\n"
"v_mfma_f32_16x16x32_bf16 v[180:183], a[88:91], a[64:67], v[180:183]\n"
"ds_read_b128 a[164:167], v32 offset:6656\n"
"v_mfma_f32_16x16x32_bf16 v[184:187], a[92:95], a[64:67], v[184:187]\n"
"ds_read_b128 a[168:171], v34 offset:4608\n"
"v_mfma_f32_16x16x32_bf16 v[188:191], a[84:87], a[68:71], v[188:191]\n"
"buffer_load_dwordx4 v42, s[24:27], s61 offen offset:1024 lds\n"
"v_mfma_f32_16x16x32_bf16 v[192:195], a[88:91], a[68:71], v[192:195]\n"
"ds_read_b128 a[172:175], v34 offset:6656\n"
"v_mfma_f32_16x16x32_bf16 v[196:199], a[92:95], a[68:71], v[196:199]\n"
"ds_read_b128 a[176:179], v36 offset:4608\n"
"v_mfma_f32_16x16x32_bf16 v[200:203], a[84:87], a[72:75], v[200:203]\n"
"v_mfma_f32_16x16x32_bf16 v[204:207], a[88:91], a[72:75], v[204:207]\n"
"buffer_load_dword v90, v90, s[44:47], s70 offen\n"
"v_mfma_f32_16x16x32_bf16 v[208:211], a[92:95], a[72:75], v[208:211]\n"
"s_lshl_b32 s16, s57, 10\n"
"v_mfma_f32_16x16x32_bf16 v[212:215], a[84:87], a[76:79], v[212:215]\n"
"s_lshl_b32 s17, s52, 8\n"
"buffer_load_dwordx4 v43, s[24:27], s61 offen offset:2048 lds\n"
"v_mfma_f32_16x16x32_bf16 v[216:219], a[88:91], a[76:79], v[216:219]\n"
"buffer_load_dwordx4 v44, s[24:27], s61 offen offset:3072 lds\n"
"s_add_u32 m0, s53, 13312\n"
"v_mfma_f32_16x16x32_bf16 v[220:223], a[92:95], a[76:79], v[220:223]\n"
"s_add_u32 s16, s16, s17\n"
"v_mfma_f32_16x16x32_bf16 v[224:227], a[84:87], a[80:83], v[224:227]\n"
"s_add_u32 s16, s16, 15728640\n"
"v_mfma_f32_16x16x32_bf16 v[228:231], a[88:91], a[80:83], v[228:231]\n"
"s_mov_b32 exec_hi, 0\n"
"buffer_load_dwordx4 v45, s[24:27], s61 offen lds\n"
"s_mov_b32 exec_hi, -1\n"
"v_mfma_f32_16x16x32_bf16 v[232:235], a[92:95], a[80:83], v[232:235]\n"
"s_waitcnt vmcnt(11) lgkmcnt(0)\n"
"s_barrier\n"
"s_add_u32 m0, s53, 19456\n"
"s_add_u32 s62, s62, 393216\n"
"v_mfma_f32_16x16x32_bf16 v[128:131], a[180:183], a[144:147], v[128:131]\n"
"ds_read_b64_tr_b16 a[84:85], v38 offset:25600\n"
"v_lshlrev_b32 v89, 2, v0\n"
"v_mfma_f32_16x16x32_bf16 v[132:135], a[184:187], a[144:147], v[132:135]\n"
"ds_read_b64_tr_b16 a[86:87], v38 offset:26112\n"
"v_mfma_f32_16x16x32_bf16 v[136:139], a[188:191], a[144:147], v[136:139]\n"
"buffer_load_dwordx4 v46, s[28:31], s62 offen lds\n"
"v_mfma_f32_16x16x32_bf16 v[140:143], a[180:183], a[148:151], v[140:143]\n"
"ds_read_b64_tr_b16 a[88:89], v39 offset:25600\n"
"v_mfma_f32_16x16x32_bf16 v[144:147], a[184:187], a[148:151], v[144:147]\n"
"ds_read_b64_tr_b16 a[90:91], v39 offset:26112\n"
"v_mfma_f32_16x16x32_bf16 v[148:151], a[188:191], a[148:151], v[148:151]\n"
"ds_read_b64_tr_b16 a[92:93], v40 offset:25600\n"
"v_mfma_f32_16x16x32_bf16 v[152:155], a[180:183], a[152:155], v[152:155]\n"
"buffer_load_dwordx4 v47, s[28:31], s62 offen offset:1024 lds\n"
"v_mfma_f32_16x16x32_bf16 v[156:159], a[184:187], a[152:155], v[156:159]\n"
"ds_read_b64_tr_b16 a[94:95], v40 offset:26112\n"
"v_mfma_f32_16x16x32_bf16 v[160:163], a[188:191], a[152:155], v[160:163]\n"
"ds_read_b128 a[48:51], v29 offset:4608\n"
"v_mfma_f32_16x16x32_bf16 v[164:167], a[180:183], a[156:159], v[164:167]\n"
"ds_read_b128 a[52:55], v29 offset:6656\n"
"v_mfma_f32_16x16x32_bf16 v[168:171], a[184:187], a[156:159], v[168:171]\n"
"buffer_load_dwordx4 v48, s[28:31], s62 offen offset:2048 lds\n"
"v_mfma_f32_16x16x32_bf16 v[172:175], a[188:191], a[156:159], v[172:175]\n"
"s_add_u32 m0, s53, 22528\n"
"ds_read_b128 a[56:59], v31 offset:4608\n"
"s_add_u32 s60, s74, 3\n"
"v_mfma_f32_16x16x32_bf16 v[176:179], a[180:183], a[160:163], v[176:179]\n"
"s_and_b32 s60, s60, 7\n"
"ds_read_b128 a[60:63], v31 offset:6656\n"
"s_mul_i32 s62, s60, 786432\n"
"v_mfma_f32_16x16x32_bf16 v[180:183], a[184:187], a[160:163], v[180:183]\n"
"ds_read_b128 a[64:67], v33 offset:4608\n"
"v_mfma_f32_16x16x32_bf16 v[184:187], a[188:191], a[160:163], v[184:187]\n"
"ds_read_b128 a[68:71], v33 offset:6656\n"
"v_mfma_f32_16x16x32_bf16 v[188:191], a[180:183], a[164:167], v[188:191]\n"
"buffer_load_dwordx4 v46, s[28:31], s62 offen lds\n"
"v_mfma_f32_16x16x32_bf16 v[192:195], a[184:187], a[164:167], v[192:195]\n"
"ds_read_b128 a[72:75], v35 offset:4608\n"
"v_mfma_f32_16x16x32_bf16 v[196:199], a[188:191], a[164:167], v[196:199]\n"
"ds_read_b128 a[76:79], v35 offset:6656\n"
"v_mfma_f32_16x16x32_bf16 v[200:203], a[180:183], a[168:171], v[200:203]\n"
"ds_read_b128 a[80:83], v37 offset:4608\n"
"v_mfma_f32_16x16x32_bf16 v[204:207], a[184:187], a[168:171], v[204:207]\n"
"buffer_load_dword v89, v89, s[44:47], s16 offen\n"
"v_mfma_f32_16x16x32_bf16 v[208:211], a[188:191], a[168:171], v[208:211]\n"
"s_mul_i32 s16, s57, 46080\n"
"v_mfma_f32_16x16x32_bf16 v[212:215], a[180:183], a[172:175], v[212:215]\n"
"s_add_u32 s71, s16, 16777216\n"
"buffer_load_dwordx4 v47, s[28:31], s62 offen offset:1024 lds\n"
"v_mfma_f32_16x16x32_bf16 v[216:219], a[184:187], a[172:175], v[216:219]\n"
"v_lshrrev_b32 v60, 2, v0\n"
"v_mfma_f32_16x16x32_bf16 v[220:223], a[188:191], a[172:175], v[220:223]\n"
"v_lshlrev_b32 v61, 7, v60\n"
"v_mfma_f32_16x16x32_bf16 v[224:227], a[180:183], a[176:179], v[224:227]\n"
"v_and_b32 v62, 3, v0\n"
"v_mfma_f32_16x16x32_bf16 v[228:231], a[184:187], a[176:179], v[228:231]\n"
"buffer_load_dwordx4 v48, s[28:31], s62 offen offset:2048 lds\n"
"v_mfma_f32_16x16x32_bf16 v[232:235], a[188:191], a[176:179], v[232:235]\n"
"s_waitcnt vmcnt(7) lgkmcnt(0)\n"
"s_barrier\n"
"s_add_u32 m0, s53, 0\n"
"v_mfma_f32_16x16x32_bf16 v[128:131], a[84:87], a[48:51], v[128:131]\n"
"s_lshl_b32 s61, s60, 7\n"
"ds_read_b64_tr_b16 a[180:181], v38 offset:16384\n"
"v_lshl_add_u32 v61, v62, 3, v61\n"
"v_mfma_f32_16x16x32_bf16 v[132:135], a[88:91], a[48:51], v[132:135]\n"
"ds_read_b64_tr_b16 a[182:183], v38 offset:16896\n"
"v_bfe_u32 v60, v60, 1, 2\n"
"v_mfma_f32_16x16x32_bf16 v[136:139], a[92:95], a[48:51], v[136:139]\n"
"buffer_load_dwordx4 v41, s[24:27], s61 offen lds\n"
"v_mfma_f32_16x16x32_bf16 v[140:143], a[84:87], a[52:55], v[140:143]\n"
"ds_read_b64_tr_b16 a[184:185], v39 offset:16384\n"
"v_xor_b32 v62, 0, v60\n"
"v_mfma_f32_16x16x32_bf16 v[144:147], a[88:91], a[52:55], v[144:147]\n"
"ds_read_b64_tr_b16 a[186:187], v39 offset:16896\n"
"v_lshl_add_u32 v62, v62, 5, v61\n"
"v_mfma_f32_16x16x32_bf16 v[148:151], a[92:95], a[52:55], v[148:151]\n"
"s_xor_b32 s16, s52, 0\n"
"ds_read_b64_tr_b16 a[188:189], v40 offset:16384\n"
"v_mfma_f32_16x16x32_bf16 v[152:155], a[84:87], a[56:59], v[152:155]\n"
"s_mul_i32 s16, s16, 40960\n"
"buffer_load_dwordx4 v42, s[24:27], s61 offen offset:1024 lds\n"
"v_mfma_f32_16x16x32_bf16 v[156:159], a[88:91], a[56:59], v[156:159]\n"
"ds_read_b64_tr_b16 a[190:191], v40 offset:16896\n"
"v_add_u32 v96, s16, v62\n"
"v_mfma_f32_16x16x32_bf16 v[160:163], a[92:95], a[56:59], v[160:163]\n"
"s_xor_b32 s17, s52, 1\n"
"ds_read_b128 a[144:147], v28 offset:9216\n"
"v_mfma_f32_16x16x32_bf16 v[164:167], a[84:87], a[60:63], v[164:167]\n"
"s_mul_i32 s17, s17, 40960\n"
"ds_read_b128 a[148:151], v28 offset:11264\n"
"v_mfma_f32_16x16x32_bf16 v[168:171], a[88:91], a[60:63], v[168:171]\n"
"buffer_load_dwordx4 v43, s[24:27], s61 offen offset:2048 lds\n"
"v_mfma_f32_16x16x32_bf16 v[172:175], a[92:95], a[60:63], v[172:175]\n"
"ds_read_b128 a[152:155], v30 offset:9216\n"
"v_add_u32 v97, s17, v62\n"
"v_mfma_f32_16x16x32_bf16 v[176:179], a[84:87], a[64:67], v[176:179]\n"
"s_xor_b32 s18, s52, 2\n"
"ds_read_b128 a[156:159], v30 offset:11264\n"
"v_mfma_f32_16x16x32_bf16 v[180:183], a[88:91], a[64:67], v[180:183]\n"
"s_mul_i32 s18, s18, 40960\n"
"ds_read_b128 a[160:163], v32 offset:9216\n"
"v_mfma_f32_16x16x32_bf16 v[184:187], a[92:95], a[64:67], v[184:187]\n"
"ds_read_b128 a[164:167], v32 offset:11264\n"
"v_add_u32 v98, s18, v62\n"
"v_mfma_f32_16x16x32_bf16 v[188:191], a[84:87], a[68:71], v[188:191]\n"
"s_xor_b32 s19, s52, 3\n"
"buffer_load_dwordx4 v44, s[24:27], s61 offen offset:3072 lds\n"
"v_mfma_f32_16x16x32_bf16 v[192:195], a[88:91], a[68:71], v[192:195]\n"
"s_add_u32 m0, s53, 4096\n"
"ds_read_b128 a[168:171], v34 offset:9216\n"
"s_mul_i32 s19, s19, 40960\n"
"v_mfma_f32_16x16x32_bf16 v[196:199], a[92:95], a[68:71], v[196:199]\n"
"ds_read_b128 a[172:175], v34 offset:11264\n"
"v_add_u32 v99, s19, v62\n"
"v_mfma_f32_16x16x32_bf16 v[200:203], a[84:87], a[72:75], v[200:203]\n"
"ds_read_b128 a[176:179], v36 offset:9216\n"
"v_xor_b32 v62, 1, v60\n"
"v_mfma_f32_16x16x32_bf16 v[204:207], a[88:91], a[72:75], v[204:207]\n"
"s_mov_b32 exec_hi, 0\n"
"buffer_load_dwordx4 v45, s[24:27], s61 offen lds\n"
"s_mov_b32 exec_hi, -1\n"
"v_mfma_f32_16x16x32_bf16 v[208:211], a[92:95], a[72:75], v[208:211]\n"
"v_lshl_add_u32 v62, v62, 5, v61\n"
"v_mfma_f32_16x16x32_bf16 v[212:215], a[84:87], a[76:79], v[212:215]\n"
"s_add_u32 m0, s53, 25600\n"
"s_add_u32 s62, s62, 393216\n"
"v_add_u32 v100, s16, v62\n"
"v_mfma_f32_16x16x32_bf16 v[216:219], a[88:91], a[76:79], v[216:219]\n"
"v_add_u32 v101, s17, v62\n"
"v_mfma_f32_16x16x32_bf16 v[220:223], a[92:95], a[76:79], v[220:223]\n"
"v_add_u32 v102, s18, v62\n"
"v_mfma_f32_16x16x32_bf16 v[224:227], a[84:87], a[80:83], v[224:227]\n"
"buffer_load_dwordx4 v46, s[28:31], s62 offen lds\n"
"v_mfma_f32_16x16x32_bf16 v[228:231], a[88:91], a[80:83], v[228:231]\n"
"v_add_u32 v103, s19, v62\n"
"v_mfma_f32_16x16x32_bf16 v[232:235], a[92:95], a[80:83], v[232:235]\n"
"s_waitcnt vmcnt(10) lgkmcnt(0)\n"
"s_barrier\n"
"ds_read_b64_tr_b16 a[84:85], v38 offset:19456\n"
"v_mfma_f32_16x16x32_bf16 v[128:131], a[180:183], a[144:147], v[128:131]\n"
"ds_read_b64_tr_b16 a[86:87], v38 offset:19968\n"
"v_xor_b32 v62, 2, v60\n"
"v_mfma_f32_16x16x32_bf16 v[132:135], a[184:187], a[144:147], v[132:135]\n"
"buffer_load_dwordx4 v47, s[28:31], s62 offen offset:1024 lds\n"
"v_mfma_f32_16x16x32_bf16 v[136:139], a[188:191], a[144:147], v[136:139]\n"
"ds_read_b64_tr_b16 a[88:89], v39 offset:19456\n"
"v_lshl_add_u32 v62, v62, 5, v61\n"
"v_mfma_f32_16x16x32_bf16 v[140:143], a[180:183], a[148:151], v[140:143]\n"
"ds_read_b64_tr_b16 a[90:91], v39 offset:19968\n"
"v_add_u32 v104, s16, v62\n"
"v_mfma_f32_16x16x32_bf16 v[144:147], a[184:187], a[148:151], v[144:147]\n"
"ds_read_b64_tr_b16 a[92:93], v40 offset:19456\n"
"v_add_u32 v105, s17, v62\n"
"v_mfma_f32_16x16x32_bf16 v[148:151], a[188:191], a[148:151], v[148:151]\n"
"ds_read_b64_tr_b16 a[94:95], v40 offset:19968\n"
"v_add_u32 v106, s18, v62\n"
"v_mfma_f32_16x16x32_bf16 v[152:155], a[180:183], a[152:155], v[152:155]\n"
"buffer_load_dwordx4 v48, s[28:31], s62 offen offset:2048 lds\n"
"v_mfma_f32_16x16x32_bf16 v[156:159], a[184:187], a[152:155], v[156:159]\n"
"s_add_u32 m0, s53, 16384\n"
"s_add_u32 s60, s74, 4\n"
"s_and_b32 s60, s60, 7\n"
"v_mfma_f32_16x16x32_bf16 v[160:163], a[188:191], a[152:155], v[160:163]\n"
"s_mul_i32 s62, s60, 786432\n"
"ds_read_b128 a[48:51], v29 offset:9216\n"
"v_add_u32 v107, s19, v62\n"
"v_mfma_f32_16x16x32_bf16 v[164:167], a[180:183], a[156:159], v[164:167]\n"
"ds_read_b128 a[52:55], v29 offset:11264\n"
"v_xor_b32 v62, 3, v60\n"
"v_mfma_f32_16x16x32_bf16 v[168:171], a[184:187], a[156:159], v[168:171]\n"
"buffer_load_dwordx4 v46, s[28:31], s62 offen lds\n"
"v_mfma_f32_16x16x32_bf16 v[172:175], a[188:191], a[156:159], v[172:175]\n"
"ds_read_b128 a[56:59], v31 offset:9216\n"
"v_lshl_add_u32 v62, v62, 5, v61\n"
"v_mfma_f32_16x16x32_bf16 v[176:179], a[180:183], a[160:163], v[176:179]\n"
".Lpad7_%=:\n"
"s_cbranch_execz .Lwb_%=\n"
"ds_read_b128 a[60:63], v31 offset:11264\n"
"v_add_u32 v108, s16, v62\n"
"v_mfma_f32_16x16x32_bf16 v[180:183], a[184:187], a[160:163], v[180:183]\n"
"ds_read_b128 a[64:67], v33 offset:9216\n"
"v_add_u32 v109, s17, v62\n"
"v_mfma_f32_16x16x32_bf16 v[184:187], a[188:191], a[160:163], v[184:187]\n"
"ds_read_b128 a[68:71], v33 offset:11264\n"
"v_add_u32 v110, s18, v62\n"
"v_mfma_f32_16x16x32_bf16 v[188:191], a[180:183], a[164:167], v[188:191]\n"
"ds_read_b128 a[72:75], v35 offset:9216\n"
"v_add_u32 v111, s19, v62\n"
"v_mfma_f32_16x16x32_bf16 v[192:195], a[184:187], a[164:167], v[192:195]\n"
"buffer_load_dwordx4 v47, s[28:31], s62 offen offset:1024 lds\n"
"v_mfma_f32_16x16x32_bf16 v[196:199], a[188:191], a[164:167], v[196:199]\n"
"ds_read_b128 a[76:79], v35 offset:11264\n"
"v_mov_b32 v22, 0xff800000\n"
"v_mfma_f32_16x16x32_bf16 v[200:203], a[180:183], a[168:171], v[200:203]\n"
"ds_read_b128 a[80:83], v37 offset:9216\n"
"v_mov_b32 v23, 0xff800000\n"
"v_mfma_f32_16x16x32_bf16 v[204:207], a[184:187], a[168:171], v[204:207]\n"
"v_mov_b32 v24, 0xff800000\n"
"v_mfma_f32_16x16x32_bf16 v[208:211], a[188:191], a[168:171], v[208:211]\n"
"buffer_load_dwordx4 v48, s[28:31], s62 offen offset:2048 lds\n"
"v_mfma_f32_16x16x32_bf16 v[212:215], a[180:183], a[172:175], v[212:215]\n"
"s_add_u32 m0, s53, 4608\n"
"s_lshl_b32 s61, s60, 7\n"
"v_mul_u32_u24 v60, 40960, v59\n"
"v_mfma_f32_16x16x32_bf16 v[216:219], a[184:187], a[172:175], v[216:219]\n"
"v_bfe_u32 v61, v0, 2, 2\n"
"v_mfma_f32_16x16x32_bf16 v[220:223], a[188:191], a[172:175], v[220:223]\n"
"v_mul_u32_u24 v61, 320, v61\n"
"v_mfma_f32_16x16x32_bf16 v[224:227], a[180:183], a[176:179], v[224:227]\n"
"buffer_load_dwordx4 v41, s[24:27], s61 offen lds\n"
"v_mfma_f32_16x16x32_bf16 v[228:231], a[184:187], a[176:179], v[228:231]\n"
"v_and_b32 v62, 3, v0\n"
"v_mfma_f32_16x16x32_bf16 v[232:235], a[188:191], a[176:179], v[232:235]\n"
"s_waitcnt vmcnt(7) lgkmcnt(0)\n"
"s_barrier\n"
"ds_read_b64_tr_b16 a[180:181], v38 offset:22528\n"
"v_mfma_f32_16x16x32_bf16 v[128:131], a[84:87], a[48:51], v[128:131]\n"
"ds_read_b64_tr_b16 a[182:183], v38 offset:23040\n"
"v_lshl_add_u32 v61, v62, 3, v61\n"
"v_mfma_f32_16x16x32_bf16 v[132:135], a[88:91], a[48:51], v[132:135]\n"
"ds_read_b64_tr_b16 a[184:185], v39 offset:22528\n"
"v_add_u32 v60, v60, v61\n"
"v_mfma_f32_16x16x32_bf16 v[136:139], a[92:95], a[48:51], v[136:139]\n"
"buffer_load_dwordx4 v42, s[24:27], s61 offen offset:1024 lds\n"
"v_mfma_f32_16x16x32_bf16 v[140:143], a[84:87], a[52:55], v[140:143]\n"
"ds_read_b64_tr_b16 a[186:187], v39 offset:23040\n"
"v_and_b32 v61, 1, v59\n"
"v_mfma_f32_16x16x32_bf16 v[144:147], a[88:91], a[52:55], v[144:147]\n"
"s_mul_i32 s18, s52, 3\n"
"ds_read_b64_tr_b16 a[188:189], v40 offset:22528\n"
"v_mfma_f32_16x16x32_bf16 v[148:151], a[92:95], a[52:55], v[148:151]\n"
"s_add_u32 s16, s18, 3\n"
"ds_read_b64_tr_b16 a[190:191], v40 offset:23040\n"
"v_mfma_f32_16x16x32_bf16 v[152:155], a[84:87], a[56:59], v[152:155]\n"
"buffer_load_dwordx4 v43, s[24:27], s61 offen offset:2048 lds\n"
"v_mfma_f32_16x16x32_bf16 v[156:159], a[88:91], a[56:59], v[156:159]\n"
"s_sub_u32 s17, s16, 10\n"
"s_cselect_b32 s16, s16, s17\n"
"ds_read_b128 a[144:147], v28 offset:0\n"
"v_mfma_f32_16x16x32_bf16 v[160:163], a[92:95], a[56:59], v[160:163]\n"
"ds_read_b128 a[148:151], v28 offset:2048\n"
"v_xor_b32 v62, s16, v61\n"
"v_mfma_f32_16x16x32_bf16 v[164:167], a[84:87], a[60:63], v[164:167]\n"
"ds_read_b128 a[152:155], v30 offset:0\n"
"v_lshl_add_u32 v62, v62, 5, v60\n"
"v_mfma_f32_16x16x32_bf16 v[168:171], a[88:91], a[60:63], v[168:171]\n"
"ds_read_b128 a[156:159], v30 offset:2048\n"
"v_accvgpr_write_b32 a112, v62\n"
"v_mfma_f32_16x16x32_bf16 v[172:175], a[92:95], a[60:63], v[172:175]\n"
"s_add_u32 s16, s18, 4\n"
"buffer_load_dwordx4 v44, s[24:27], s61 offen offset:3072 lds\n"
"v_mfma_f32_16x16x32_bf16 v[176:179], a[84:87], a[64:67], v[176:179]\n"
"s_sub_u32 s17, s16, 10\n"
"s_cselect_b32 s16, s16, s17\n"
"ds_read_b128 a[160:163], v32 offset:0\n"
"v_mfma_f32_16x16x32_bf16 v[180:183], a[88:91], a[64:67], v[180:183]\n"
"s_add_u32 m0, s53, 8704\n"
"ds_read_b128 a[164:167], v32 offset:2048\n"
"v_xor_b32 v62, s16, v61\n"
"v_mfma_f32_16x16x32_bf16 v[184:187], a[92:95], a[64:67], v[184:187]\n"
"ds_read_b128 a[168:171], v34 offset:0\n"
"v_lshl_add_u32 v62, v62, 5, v60\n"
"v_mfma_f32_16x16x32_bf16 v[188:191], a[84:87], a[68:71], v[188:191]\n"
"ds_read_b128 a[172:175], v34 offset:2048\n"
"v_accvgpr_write_b32 a113, v62\n"
"v_mfma_f32_16x16x32_bf16 v[192:195], a[88:91], a[68:71], v[192:195]\n"
"s_mov_b32 exec_hi, 0\n"
"buffer_load_dwordx4 v45, s[24:27], s61 offen lds\n"
"s_mov_b32 exec_hi, -1\n"
"v_mfma_f32_16x16x32_bf16 v[196:199], a[92:95], a[68:71], v[196:199]\n"
"s_add_u32 m0, s53, 19456\n"
"ds_read_b128 a[176:179], v36 offset:0\n"
"s_add_u32 s62, s62, 393216\n"
"v_mfma_f32_16x16x32_bf16 v[200:203], a[84:87], a[72:75], v[200:203]\n"
"s_add_u32 s16, s18, 5\n"
"v_mfma_f32_16x16x32_bf16 v[204:207], a[88:91], a[72:75], v[204:207]\n"
"s_sub_u32 s17, s16, 10\n"
"s_cselect_b32 s16, s16, s17\n"
"v_mfma_f32_16x16x32_bf16 v[208:211], a[92:95], a[72:75], v[208:211]\n"
"buffer_load_dwordx4 v46, s[28:31], s62 offen lds\n"
"v_mfma_f32_16x16x32_bf16 v[212:215], a[84:87], a[76:79], v[212:215]\n"
"v_xor_b32 v62, s16, v61\n"
"v_mfma_f32_16x16x32_bf16 v[216:219], a[88:91], a[76:79], v[216:219]\n"
"v_lshl_add_u32 v62, v62, 5, v60\n"
"v_mfma_f32_16x16x32_bf16 v[220:223], a[92:95], a[76:79], v[220:223]\n"
"v_accvgpr_write_b32 a114, v62\n"
"v_mfma_f32_16x16x32_bf16 v[224:227], a[84:87], a[80:83], v[224:227]\n"
"s_add_u32 s16, s18, 0\n"
"v_mfma_f32_16x16x32_bf16 v[228:231], a[88:91], a[80:83], v[228:231]\n"
"buffer_load_dwordx4 v47, s[28:31], s62 offen offset:1024 lds\n"
"v_mfma_f32_16x16x32_bf16 v[232:235], a[92:95], a[80:83], v[232:235]\n"
"s_waitcnt vmcnt(10) lgkmcnt(0)\n"
"s_barrier\n"
"ds_read_b64_tr_b16 a[84:85], v38 offset:25600\n"
"v_mfma_f32_16x16x32_bf16 v[128:131], a[180:183], a[144:147], v[128:131]\n"
"s_sub_u32 s17, s16, 10\n"
"s_cselect_b32 s16, s16, s17\n"
"ds_read_b64_tr_b16 a[86:87], v38 offset:26112\n"
"v_mfma_f32_16x16x32_bf16 v[132:135], a[184:187], a[144:147], v[132:135]\n"
"ds_read_b64_tr_b16 a[88:89], v39 offset:25600\n"
"v_xor_b32 v62, s16, v61\n"
"v_mfma_f32_16x16x32_bf16 v[136:139], a[188:191], a[144:147], v[136:139]\n"
"buffer_load_dwordx4 v48, s[28:31], s62 offen offset:2048 lds\n"
"v_mfma_f32_16x16x32_bf16 v[140:143], a[180:183], a[148:151], v[140:143]\n"
"ds_read_b64_tr_b16 a[90:91], v39 offset:26112\n"
"v_lshl_add_u32 v62, v62, 5, v60\n"
"v_mfma_f32_16x16x32_bf16 v[144:147], a[184:187], a[148:151], v[144:147]\n"
"s_add_u32 m0, s53, 22528\n"
"s_add_u32 s60, s74, 5\n"
"s_and_b32 s60, s60, 7\n"
"v_mfma_f32_16x16x32_bf16 v[148:151], a[188:191], a[148:151], v[148:151]\n"
"s_mul_i32 s62, s60, 786432\n"
"ds_read_b64_tr_b16 a[92:93], v40 offset:25600\n"
"v_accvgpr_write_b32 a115, v62\n"
"v_mfma_f32_16x16x32_bf16 v[152:155], a[180:183], a[152:155], v[152:155]\n"
"s_add_u32 s16, s18, 6\n"
"ds_read_b64_tr_b16 a[94:95], v40 offset:26112\n"
"v_mfma_f32_16x16x32_bf16 v[156:159], a[184:187], a[152:155], v[156:159]\n"
"buffer_load_dwordx4 v46, s[28:31], s62 offen lds\n"
"v_mfma_f32_16x16x32_bf16 v[160:163], a[188:191], a[152:155], v[160:163]\n"
"s_sub_u32 s17, s16, 10\n"
"s_cselect_b32 s16, s16, s17\n"
"ds_read_b128 a[48:51], v29 offset:0\n"
"v_mfma_f32_16x16x32_bf16 v[164:167], a[180:183], a[156:159], v[164:167]\n"
"ds_read_b128 a[52:55], v29 offset:2048\n"
"v_xor_b32 v62, s16, v61\n"
"v_mfma_f32_16x16x32_bf16 v[168:171], a[184:187], a[156:159], v[168:171]\n"
"ds_read_b128 a[56:59], v31 offset:0\n"
"v_lshl_add_u32 v62, v62, 5, v60\n"
"v_mfma_f32_16x16x32_bf16 v[172:175], a[188:191], a[156:159], v[172:175]\n"
"ds_read_b128 a[60:63], v31 offset:2048\n"
"v_accvgpr_write_b32 a116, v62\n"
"v_mfma_f32_16x16x32_bf16 v[176:179], a[180:183], a[160:163], v[176:179]\n"
"s_add_u32 s16, s18, 7\n"
"buffer_load_dwordx4 v47, s[28:31], s62 offen offset:1024 lds\n"
"v_mfma_f32_16x16x32_bf16 v[180:183], a[184:187], a[160:163], v[180:183]\n"
"s_sub_u32 s17, s16, 10\n"
"s_cselect_b32 s16, s16, s17\n"
"ds_read_b128 a[64:67], v33 offset:0\n"
"v_mfma_f32_16x16x32_bf16 v[184:187], a[188:191], a[160:163], v[184:187]\n"
"ds_read_b128 a[68:71], v33 offset:2048\n"
"v_xor_b32 v62, s16, v61\n"
"v_mfma_f32_16x16x32_bf16 v[188:191], a[180:183], a[164:167], v[188:191]\n"
"ds_read_b128 a[72:75], v35 offset:0\n"
"v_lshl_add_u32 v62, v62, 5, v60\n"
"v_mfma_f32_16x16x32_bf16 v[192:195], a[184:187], a[164:167], v[192:195]\n"
"ds_read_b128 a[76:79], v35 offset:2048\n"
"v_accvgpr_write_b32 a117, v62\n"
"v_mfma_f32_16x16x32_bf16 v[196:199], a[188:191], a[164:167], v[196:199]\n"
"s_add_u32 s16, s18, 1\n"
"buffer_load_dwordx4 v48, s[28:31], s62 offen offset:2048 lds\n"
"v_mfma_f32_16x16x32_bf16 v[200:203], a[180:183], a[168:171], v[200:203]\n"
"s_add_u32 m0, s53, 9216\n"
"ds_read_b128 a[80:83], v37 offset:0\n"
"s_lshl_b32 s61, s60, 7\n"
"v_mfma_f32_16x16x32_bf16 v[204:207], a[184:187], a[168:171], v[204:207]\n"
"s_sub_u32 s17, s16, 10\n"
"s_cselect_b32 s16, s16, s17\n"
"v_mfma_f32_16x16x32_bf16 v[208:211], a[188:191], a[168:171], v[208:211]\n"
"v_xor_b32 v62, s16, v61\n"
"v_mfma_f32_16x16x32_bf16 v[212:215], a[180:183], a[172:175], v[212:215]\n"
"buffer_load_dwordx4 v41, s[24:27], s61 offen lds\n"
"v_mfma_f32_16x16x32_bf16 v[216:219], a[184:187], a[172:175], v[216:219]\n"
"v_lshl_add_u32 v62, v62, 5, v60\n"
"v_mfma_f32_16x16x32_bf16 v[220:223], a[188:191], a[172:175], v[220:223]\n"
"v_accvgpr_write_b32 a118, v62\n"
"v_mfma_f32_16x16x32_bf16 v[224:227], a[180:183], a[176:179], v[224:227]\n"
"s_add_u32 s16, s18, 8\n"
"v_mfma_f32_16x16x32_bf16 v[228:231], a[184:187], a[176:179], v[228:231]\n"
"buffer_load_dwordx4 v42, s[24:27], s61 offen offset:1024 lds\n"
"v_mfma_f32_16x16x32_bf16 v[232:235], a[188:191], a[176:179], v[232:235]\n"
"s_waitcnt vmcnt(8) lgkmcnt(0)\n"
"s_barrier\n"
"ds_read_b64_tr_b16 a[180:181], v38 offset:16384\n"
"v_mfma_f32_16x16x32_bf16 v[128:131], a[84:87], a[48:51], v[128:131]\n"
"s_sub_u32 s17, s16, 10\n"
"s_cselect_b32 s16, s16, s17\n"
"ds_read_b64_tr_b16 a[182:183], v38 offset:16896\n"
"v_mfma_f32_16x16x32_bf16 v[132:135], a[88:91], a[48:51], v[132:135]\n"
"ds_read_b64_tr_b16 a[184:185], v39 offset:16384\n"
"v_xor_b32 v62, s16, v61\n"
"v_mfma_f32_16x16x32_bf16 v[136:139], a[92:95], a[48:51], v[136:139]\n"
"ds_read_b64_tr_b16 a[186:187], v39 offset:16896\n"
"v_lshl_add_u32 v62, v62, 5, v60\n"
"v_mfma_f32_16x16x32_bf16 v[140:143], a[84:87], a[52:55], v[140:143]\n"
"buffer_load_dwordx4 v43, s[24:27], s61 offen offset:2048 lds\n"
"v_mfma_f32_16x16x32_bf16 v[144:147], a[88:91], a[52:55], v[144:147]\n"
"ds_read_b64_tr_b16 a[188:189], v40 offset:16384\n"
"v_accvgpr_write_b32 a119, v62\n"
"v_mfma_f32_16x16x32_bf16 v[148:151], a[92:95], a[52:55], v[148:151]\n"
"s_add_u32 s16, s18, 2\n"
"ds_read_b64_tr_b16 a[190:191], v40 offset:16896\n"
"v_mfma_f32_16x16x32_bf16 v[152:155], a[84:87], a[56:59], v[152:155]\n"
"s_sub_u32 s17, s16, 10\n"
"s_cselect_b32 s16, s16, s17\n"
"ds_read_b128 a[144:147], v28 offset:4608\n"
"v_mfma_f32_16x16x32_bf16 v[156:159], a[88:91], a[56:59], v[156:159]\n"
"ds_read_b128 a[148:151], v28 offset:6656\n"
"v_xor_b32 v62, s16, v61\n"
"v_mfma_f32_16x16x32_bf16 v[160:163], a[92:95], a[56:59], v[160:163]\n"
"buffer_load_dwordx4 v44, s[24:27], s61 offen offset:3072 lds\n"
"v_mfma_f32_16x16x32_bf16 v[164:167], a[84:87], a[60:63], v[164:167]\n"
"s_add_u32 m0, s53, 13312\n"
"ds_read_b128 a[152:155], v30 offset:4608\n"
"v_lshl_add_u32 v62, v62, 5, v60\n"
"v_mfma_f32_16x16x32_bf16 v[168:171], a[88:91], a[60:63], v[168:171]\n"
"ds_read_b128 a[156:159], v30 offset:6656\n"
"v_accvgpr_write_b32 a120, v62\n"
"v_mfma_f32_16x16x32_bf16 v[172:175], a[92:95], a[60:63], v[172:175]\n"
"s_add_u32 s16, s18, 9\n"
"ds_read_b128 a[160:163], v32 offset:4608\n"
"v_mfma_f32_16x16x32_bf16 v[176:179], a[84:87], a[64:67], v[176:179]\n"
"s_mov_b32 exec_hi, 0\n"
"buffer_load_dwordx4 v45, s[24:27], s61 offen lds\n"
"s_mov_b32 exec_hi, -1\n"
"v_mfma_f32_16x16x32_bf16 v[180:183], a[88:91], a[64:67], v[180:183]\n"
"s_sub_u32 s17, s16, 10\n"
"s_cselect_b32 s16, s16, s17\n"
"ds_read_b128 a[164:167], v32 offset:6656\n"
"v_mfma_f32_16x16x32_bf16 v[184:187], a[92:95], a[64:67], v[184:187]\n"
"s_add_u32 m0, s53, 25600\n"
"ds_read_b128 a[168:171], v34 offset:4608\n"
"s_add_u32 s62, s62, 393216\n"
"v_mfma_f32_16x16x32_bf16 v[188:191], a[84:87], a[68:71], v[188:191]\n"
"ds_read_b128 a[172:175], v34 offset:6656\n"
"v_xor_b32 v62, s16, v61\n"
"v_mfma_f32_16x16x32_bf16 v[192:195], a[88:91], a[68:71], v[192:195]\n"
"ds_read_b128 a[176:179], v36 offset:4608\n"
"v_lshl_add_u32 v62, v62, 5, v60\n"
"v_mfma_f32_16x16x32_bf16 v[196:199], a[92:95], a[68:71], v[196:199]\n"
"buffer_load_dwordx4 v46, s[28:31], s62 offen lds\n"
"v_mfma_f32_16x16x32_bf16 v[200:203], a[84:87], a[72:75], v[200:203]\n"
"v_accvgpr_write_b32 a121, v62\n"
"v_mfma_f32_16x16x32_bf16 v[204:207], a[88:91], a[72:75], v[204:207]\n"
"s_and_b32 s18, s52, 1\n"
"v_mfma_f32_16x16x32_bf16 v[208:211], a[92:95], a[72:75], v[208:211]\n"
"s_lshl_b32 s18, s18, 1\n"
"v_mfma_f32_16x16x32_bf16 v[212:215], a[84:87], a[76:79], v[212:215]\n"
"s_mul_i32 s19, s52, 5120\n"
"buffer_load_dwordx4 v47, s[28:31], s62 offen offset:1024 lds\n"
"v_mfma_f32_16x16x32_bf16 v[216:219], a[88:91], a[76:79], v[216:219]\n"
"v_add_u32 v60, 0, v0\n"
"v_mfma_f32_16x16x32_bf16 v[220:223], a[92:95], a[76:79], v[220:223]\n"
"v_mul_u32_u24 v61, 205, v60\n"
"v_mfma_f32_16x16x32_bf16 v[224:227], a[84:87], a[80:83], v[224:227]\n"
"v_lshrrev_b32 v61, 12, v61\n"
"v_mfma_f32_16x16x32_bf16 v[228:231], a[88:91], a[80:83], v[228:231]\n"
"v_mul_u32_u24 v62, 20, v61\n"
"v_mfma_f32_16x16x32_bf16 v[232:235], a[92:95], a[80:83], v[232:235]\n"
"s_waitcnt vmcnt(10) lgkmcnt(0)\n"
"s_barrier\n"
"ds_read_b64_tr_b16 a[84:85], v38 offset:19456\n"
"v_mfma_f32_16x16x32_bf16 v[128:131], a[180:183], a[144:147], v[128:131]\n"
"buffer_load_dwordx4 v48, s[28:31], s62 offen offset:2048 lds\n"
"v_mfma_f32_16x16x32_bf16 v[132:135], a[184:187], a[144:147], v[132:135]\n"
"s_add_u32 m0, s53, 16384\n"
"s_add_u32 s60, s74, 6\n"
"s_and_b32 s60, s60, 7\n"
"v_mfma_f32_16x16x32_bf16 v[136:139], a[188:191], a[144:147], v[136:139]\n"
"s_mul_i32 s62, s60, 786432\n"
"ds_read_b64_tr_b16 a[86:87], v38 offset:19968\n"
"v_sub_u32 v60, v60, v62\n"
"v_mfma_f32_16x16x32_bf16 v[140:143], a[180:183], a[148:151], v[140:143]\n"
"ds_read_b64_tr_b16 a[88:89], v39 offset:19456\n"
"v_xor_b32 v60, s18, v60\n"
"v_mfma_f32_16x16x32_bf16 v[144:147], a[184:187], a[148:151], v[144:147]\n"
"buffer_load_dwordx4 v46, s[28:31], s62 offen lds\n"
"v_mfma_f32_16x16x32_bf16 v[148:151], a[188:191], a[148:151], v[148:151]\n"
"ds_read_b64_tr_b16 a[90:91], v39 offset:19968\n"
"v_lshrrev_b32 v62, 2, v61\n"
"v_mfma_f32_16x16x32_bf16 v[152:155], a[180:183], a[152:155], v[152:155]\n"
"ds_read_b64_tr_b16 a[92:93], v40 offset:19456\n"
"v_mad_u32_u24 v61, v62, 12, v61\n"
"v_mfma_f32_16x16x32_bf16 v[156:159], a[184:187], a[152:155], v[156:159]\n"
"ds_read_b64_tr_b16 a[94:95], v40 offset:19968\n"
"v_mul_u32_u24 v61, 1280, v61\n"
"v_mfma_f32_16x16x32_bf16 v[160:163], a[188:191], a[152:155], v[160:163]\n"
"buffer_load_dwordx4 v47, s[28:31], s62 offen offset:1024 lds\n"
"v_mfma_f32_16x16x32_bf16 v[164:167], a[180:183], a[156:159], v[164:167]\n"
"ds_read_b128 a[48:51], v29 offset:4608\n"
"v_lshl_add_u32 v60, v60, 4, v61\n"
"v_mfma_f32_16x16x32_bf16 v[168:171], a[184:187], a[156:159], v[168:171]\n"
"ds_read_b128 a[52:55], v29 offset:6656\n"
"v_add_u32 v60, s19, v60\n"
"v_mfma_f32_16x16x32_bf16 v[172:175], a[188:191], a[156:159], v[172:175]\n"
"ds_read_b128 a[56:59], v31 offset:4608\n"
"v_accvgpr_write_b32 a122, v60\n"
"v_mfma_f32_16x16x32_bf16 v[176:179], a[180:183], a[160:163], v[176:179]\n"
"ds_read_b128 a[60:63], v31 offset:6656\n"
"v_add_u32 v60, 64, v0\n"
"v_mfma_f32_16x16x32_bf16 v[180:183], a[184:187], a[160:163], v[180:183]\n"
"ds_read_b128 a[64:67], v33 offset:4608\n"
"v_mul_u32_u24 v61, 205, v60\n"
"v_mfma_f32_16x16x32_bf16 v[184:187], a[188:191], a[160:163], v[184:187]\n"
"buffer_load_dwordx4 v48, s[28:31], s62 offen offset:2048 lds\n"
"v_mfma_f32_16x16x32_bf16 v[188:191], a[180:183], a[164:167], v[188:191]\n"
"s_add_u32 m0, s53, 0\n"
"ds_read_b128 a[68:71], v33 offset:6656\n"
"s_lshl_b32 s61, s60, 7\n"
"v_mfma_f32_16x16x32_bf16 v[192:195], a[184:187], a[164:167], v[192:195]\n"
"ds_read_b128 a[72:75], v35 offset:4608\n"
"v_lshrrev_b32 v61, 12, v61\n"
"v_mfma_f32_16x16x32_bf16 v[196:199], a[188:191], a[164:167], v[196:199]\n"
"ds_read_b128 a[76:79], v35 offset:6656\n"
"v_mul_u32_u24 v62, 20, v61\n"
"v_mfma_f32_16x16x32_bf16 v[200:203], a[180:183], a[168:171], v[200:203]\n"
"ds_read_b128 a[80:83], v37 offset:4608\n"
"v_sub_u32 v60, v60, v62\n"
"v_mfma_f32_16x16x32_bf16 v[204:207], a[184:187], a[168:171], v[204:207]\n"
"buffer_load_dwordx4 v41, s[24:27], s61 offen lds\n"
"v_mfma_f32_16x16x32_bf16 v[208:211], a[188:191], a[168:171], v[208:211]\n"
"v_xor_b32 v60, s18, v60\n"
"v_mfma_f32_16x16x32_bf16 v[212:215], a[180:183], a[172:175], v[212:215]\n"
"v_lshrrev_b32 v62, 2, v61\n"
"v_mfma_f32_16x16x32_bf16 v[216:219], a[184:187], a[172:175], v[216:219]\n"
"v_mad_u32_u24 v61, v62, 12, v61\n"
"v_mfma_f32_16x16x32_bf16 v[220:223], a[188:191], a[172:175], v[220:223]\n"
"buffer_load_dwordx4 v42, s[24:27], s61 offen offset:1024 lds\n"
"v_mfma_f32_16x16x32_bf16 v[224:227], a[180:183], a[176:179], v[224:227]\n"
"v_mul_u32_u24 v61, 1280, v61\n"
"v_mfma_f32_16x16x32_bf16 v[228:231], a[184:187], a[176:179], v[228:231]\n"
"v_lshl_add_u32 v60, v60, 4, v61\n"
"v_mfma_f32_16x16x32_bf16 v[232:235], a[188:191], a[176:179], v[232:235]\n"
"s_waitcnt vmcnt(8) lgkmcnt(0)\n"
"s_barrier\n"
"ds_read_b64_tr_b16 a[180:181], v38 offset:22528\n"
"v_mfma_f32_16x16x32_bf16 v[128:131], a[84:87], a[48:51], v[128:131]\n"
"buffer_load_dwordx4 v43, s[24:27], s61 offen offset:2048 lds\n"
"v_mfma_f32_16x16x32_bf16 v[132:135], a[88:91], a[48:51], v[132:135]\n"
"ds_read_b64_tr_b16 a[182:183], v38 offset:23040\n"
"v_add_u32 v60, s19, v60\n"
"v_mfma_f32_16x16x32_bf16 v[136:139], a[92:95], a[48:51], v[136:139]\n"
"ds_read_b64_tr_b16 a[184:185], v39 offset:22528\n"
"v_subrev_u32 v60, 1024, v60\n"
"v_mfma_f32_16x16x32_bf16 v[140:143], a[84:87], a[52:55], v[140:143]\n"
"ds_read_b64_tr_b16 a[186:187], v39 offset:23040\n"
"v_accvgpr_write_b32 a123, v60\n"
"v_mfma_f32_16x16x32_bf16 v[144:147], a[88:91], a[52:55], v[144:147]\n"
"buffer_load_dwordx4 v44, s[24:27], s61 offen offset:3072 lds\n"
"v_mfma_f32_16x16x32_bf16 v[148:151], a[92:95], a[52:55], v[148:151]\n"
"ds_read_b64_tr_b16 a[188:189], v40 offset:22528\n"
"v_add_u32 v60, 128, v0\n"
"v_mfma_f32_16x16x32_bf16 v[152:155], a[84:87], a[56:59], v[152:155]\n"
"s_add_u32 m0, s53, 4096\n"
"ds_read_b64_tr_b16 a[190:191], v40 offset:23040\n"
"v_mul_u32_u24 v61, 205, v60\n"
"v_mfma_f32_16x16x32_bf16 v[156:159], a[88:91], a[56:59], v[156:159]\n"
"ds_read_b128 a[144:147], v28 offset:9216\n"
"v_lshrrev_b32 v61, 12, v61\n"
"v_mfma_f32_16x16x32_bf16 v[160:163], a[92:95], a[56:59], v[160:163]\n"
"ds_read_b128 a[148:151], v28 offset:11264\n"
"v_mul_u32_u24 v62, 20, v61\n"
"v_mfma_f32_16x16x32_bf16 v[164:167], a[84:87], a[60:63], v[164:167]\n"
"s_mov_b32 exec_hi, 0\n"
"buffer_load_dwordx4 v45, s[24:27], s61 offen lds\n"
"s_mov_b32 exec_hi, -1\n"
"v_mfma_f32_16x16x32_bf16 v[168:171], a[88:91], a[60:63], v[168:171]\n"
"s_add_u32 m0, s53, 19456\n"
"ds_read_b128 a[152:155], v30 offset:9216\n"
"s_add_u32 s62, s62, 393216\n"
"v_mfma_f32_16x16x32_bf16 v[172:175], a[92:95], a[60:63], v[172:175]\n"
"ds_read_b128 a[156:159], v30 offset:11264\n"
"v_sub_u32 v60, v60, v62\n"
"v_mfma_f32_16x16x32_bf16 v[176:179], a[84:87], a[64:67], v[176:179]\n"
"ds_read_b128 a[160:163], v32 offset:9216\n"
"v_xor_b32 v60, s18, v60\n"
"v_mfma_f32_16x16x32_bf16 v[180:183], a[88:91], a[64:67], v[180:183]\n"
"buffer_load_dwordx4 v46, s[28:31], s62 offen lds\n"
"v_mfma_f32_16x16x32_bf16 v[184:187], a[92:95], a[64:67], v[184:187]\n"
"ds_read_b128 a[164:167], v32 offset:11264\n"
"v_lshrrev_b32 v62, 2, v61\n"
"v_mfma_f32_16x16x32_bf16 v[188:191], a[84:87], a[68:71], v[188:191]\n"
"ds_read_b128 a[168:171], v34 offset:9216\n"
"v_mad_u32_u24 v61, v62, 12, v61\n"
"v_mfma_f32_16x16x32_bf16 v[192:195], a[88:91], a[68:71], v[192:195]\n"
"ds_read_b128 a[172:175], v34 offset:11264\n"
"v_mul_u32_u24 v61, 1280, v61\n"
"v_mfma_f32_16x16x32_bf16 v[196:199], a[92:95], a[68:71], v[196:199]\n"
"ds_read_b128 a[176:179], v36 offset:9216\n"
"v_lshl_add_u32 v60, v60, 4, v61\n"
"v_mfma_f32_16x16x32_bf16 v[200:203], a[84:87], a[72:75], v[200:203]\n"
"buffer_load_dwordx4 v47, s[28:31], s62 offen offset:1024 lds\n"
"v_mfma_f32_16x16x32_bf16 v[204:207], a[88:91], a[72:75], v[204:207]\n"
"v_add_u32 v60, s19, v60\n"
"v_mfma_f32_16x16x32_bf16 v[208:211], a[92:95], a[72:75], v[208:211]\n"
"v_subrev_u32 v60, 2048, v60\n"
"v_mfma_f32_16x16x32_bf16 v[212:215], a[84:87], a[76:79], v[212:215]\n"
"v_accvgpr_write_b32 a124, v60\n"
"v_mfma_f32_16x16x32_bf16 v[216:219], a[88:91], a[76:79], v[216:219]\n"
"v_add_u32 v60, 192, v0\n"
"v_mfma_f32_16x16x32_bf16 v[220:223], a[92:95], a[76:79], v[220:223]\n"
"buffer_load_dwordx4 v48, s[28:31], s62 offen offset:2048 lds\n"
"v_mfma_f32_16x16x32_bf16 v[224:227], a[84:87], a[80:83], v[224:227]\n"
"s_add_u32 m0, s53, 22528\n"
"s_add_u32 s60, s74, 7\n"
"s_and_b32 s60, s60, 7\n"
"v_mfma_f32_16x16x32_bf16 v[228:231], a[88:91], a[80:83], v[228:231]\n"
"s_mul_i32 s62, s60, 786432\n"
"v_mul_u32_u24 v61, 205, v60\n"
"v_mfma_f32_16x16x32_bf16 v[232:235], a[92:95], a[80:83], v[232:235]\n"
"s_waitcnt vmcnt(11) lgkmcnt(0)\n"
"s_barrier\n"
"ds_read_b64_tr_b16 a[84:85], v38 offset:25600\n"
"v_mfma_f32_16x16x32_bf16 v[128:131], a[180:183], a[144:147], v[128:131]\n"
"buffer_load_dwordx4 v46, s[28:31], s62 offen lds\n"
"v_mfma_f32_16x16x32_bf16 v[132:135], a[184:187], a[144:147], v[132:135]\n"
"ds_read_b64_tr_b16 a[86:87], v38 offset:26112\n"
"v_lshrrev_b32 v61, 12, v61\n"
"v_mfma_f32_16x16x32_bf16 v[136:139], a[188:191], a[144:147], v[136:139]\n"
"ds_read_b64_tr_b16 a[88:89], v39 offset:25600\n"
"v_mul_u32_u24 v62, 20, v61\n"
"v_mfma_f32_16x16x32_bf16 v[140:143], a[180:183], a[148:151], v[140:143]\n"
"ds_read_b64_tr_b16 a[90:91], v39 offset:26112\n"
"v_sub_u32 v60, v60, v62\n"
"v_mfma_f32_16x16x32_bf16 v[144:147], a[184:187], a[148:151], v[144:147]\n"
"ds_read_b64_tr_b16 a[92:93], v40 offset:25600\n"
"v_xor_b32 v60, s18, v60\n"
"v_mfma_f32_16x16x32_bf16 v[148:151], a[188:191], a[148:151], v[148:151]\n"
"buffer_load_dwordx4 v47, s[28:31], s62 offen offset:1024 lds\n"
"v_mfma_f32_16x16x32_bf16 v[152:155], a[180:183], a[152:155], v[152:155]\n"
"ds_read_b64_tr_b16 a[94:95], v40 offset:26112\n"
"v_lshrrev_b32 v62, 2, v61\n"
"v_mfma_f32_16x16x32_bf16 v[156:159], a[184:187], a[152:155], v[156:159]\n"
"ds_read_b128 a[48:51], v29 offset:9216\n"
"v_mad_u32_u24 v61, v62, 12, v61\n"
"v_mfma_f32_16x16x32_bf16 v[160:163], a[188:191], a[152:155], v[160:163]\n"
"ds_read_b128 a[52:55], v29 offset:11264\n"
"v_mul_u32_u24 v61, 1280, v61\n"
"v_mfma_f32_16x16x32_bf16 v[164:167], a[180:183], a[156:159], v[164:167]\n"
"buffer_load_dwordx4 v48, s[28:31], s62 offen offset:2048 lds\n"
"v_mfma_f32_16x16x32_bf16 v[168:171], a[184:187], a[156:159], v[168:171]\n"
"ds_read_b128 a[56:59], v31 offset:9216\n"
"v_lshl_add_u32 v60, v60, 4, v61\n"
"v_mfma_f32_16x16x32_bf16 v[172:175], a[188:191], a[156:159], v[172:175]\n"
"s_add_u32 m0, s53, 4608\n"
"ds_read_b128 a[60:63], v31 offset:11264\n"
"s_lshl_b32 s61, s60, 7\n"
"v_mfma_f32_16x16x32_bf16 v[176:179], a[180:183], a[160:163], v[176:179]\n"
"ds_read_b128 a[64:67], v33 offset:9216\n"
"v_add_u32 v60, s19, v60\n"
"v_mfma_f32_16x16x32_bf16 v[180:183], a[184:187], a[160:163], v[180:183]\n"
"ds_read_b128 a[68:71], v33 offset:11264\n"
"v_subrev_u32 v60, 3072, v60\n"
"v_mfma_f32_16x16x32_bf16 v[184:187], a[188:191], a[160:163], v[184:187]\n"
"buffer_load_dwordx4 v41, s[24:27], s61 offen lds\n"
"v_mfma_f32_16x16x32_bf16 v[188:191], a[180:183], a[164:167], v[188:191]\n"
"ds_read_b128 a[72:75], v35 offset:9216\n"
"v_accvgpr_write_b32 a125, v60\n"
"v_mfma_f32_16x16x32_bf16 v[192:195], a[184:187], a[164:167], v[192:195]\n"
"ds_read_b128 a[76:79], v35 offset:11264\n"
"v_add_u32 v60, 256, v0\n"
"v_mfma_f32_16x16x32_bf16 v[196:199], a[188:191], a[164:167], v[196:199]\n"
"ds_read_b128 a[80:83], v37 offset:9216\n"
"v_mul_u32_u24 v61, 205, v60\n"
"v_mfma_f32_16x16x32_bf16 v[200:203], a[180:183], a[168:171], v[200:203]\n"
"v_lshrrev_b32 v61, 12, v61\n"
"v_mfma_f32_16x16x32_bf16 v[204:207], a[184:187], a[168:171], v[204:207]\n"
"buffer_load_dwordx4 v42, s[24:27], s61 offen offset:1024 lds\n"
"v_mfma_f32_16x16x32_bf16 v[208:211], a[188:191], a[168:171], v[208:211]\n"
"v_mul_u32_u24 v62, 20, v61\n"
"v_mfma_f32_16x16x32_bf16 v[212:215], a[180:183], a[172:175], v[212:215]\n"
"v_sub_u32 v60, v60, v62\n"
"v_mfma_f32_16x16x32_bf16 v[216:219], a[184:187], a[172:175], v[216:219]\n"
"v_xor_b32 v60, s18, v60\n"
"v_mfma_f32_16x16x32_bf16 v[220:223], a[188:191], a[172:175], v[220:223]\n"
"buffer_load_dwordx4 v43, s[24:27], s61 offen offset:2048 lds\n"
"v_mfma_f32_16x16x32_bf16 v[224:227], a[180:183], a[176:179], v[224:227]\n"
"v_lshrrev_b32 v62, 2, v61\n"
"v_mfma_f32_16x16x32_bf16 v[228:231], a[184:187], a[176:179], v[228:231]\n"
"v_mad_u32_u24 v61, v62, 12, v61\n"
"v_mfma_f32_16x16x32_bf16 v[232:235], a[188:191], a[176:179], v[232:235]\n"
"s_waitcnt vmcnt(9) lgkmcnt(0)\n"
"s_barrier\n"
"ds_read_b64_tr_b16 a[180:181], v38 offset:16384\n"
"v_mfma_f32_16x16x32_bf16 v[128:131], a[84:87], a[48:51], v[128:131]\n"
"ds_read_b64_tr_b16 a[182:183], v38 offset:16896\n"
"v_mul_u32_u24 v61, 1280, v61\n"
"v_mfma_f32_16x16x32_bf16 v[132:135], a[88:91], a[48:51], v[132:135]\n"
"buffer_load_dwordx4 v44, s[24:27], s61 offen offset:3072 lds\n"
"v_mfma_f32_16x16x32_bf16 v[136:139], a[92:95], a[48:51], v[136:139]\n"
"ds_read_b64_tr_b16 a[184:185], v39 offset:16384\n"
"v_lshl_add_u32 v60, v60, 4, v61\n"
"v_mfma_f32_16x16x32_bf16 v[140:143], a[84:87], a[52:55], v[140:143]\n"
"s_add_u32 m0, s53, 8704\n"
"ds_read_b64_tr_b16 a[186:187], v39 offset:16896\n"
"v_add_u32 v60, s19, v60\n"
"v_mfma_f32_16x16x32_bf16 v[144:147], a[88:91], a[52:55], v[144:147]\n"
"ds_read_b64_tr_b16 a[188:189], v40 offset:16384\n"
"v_accvgpr_write_b32 a126, v60\n"
"v_mfma_f32_16x16x32_bf16 v[148:151], a[92:95], a[52:55], v[148:151]\n"
"s_and_b32 s18, s70, 0xffff3fff\n"
"ds_read_b64_tr_b16 a[190:191], v40 offset:16896\n"
"v_mfma_f32_16x16x32_bf16 v[152:155], a[84:87], a[56:59], v[152:155]\n"
"s_mov_b32 exec_hi, 0\n"
"buffer_load_dwordx4 v45, s[24:27], s61 offen lds\n"
"s_mov_b32 exec_hi, -1\n"
"v_mfma_f32_16x16x32_bf16 v[156:159], a[88:91], a[56:59], v[156:159]\n"
"s_add_u32 m0, s53, 25600\n"
"ds_read_b128 a[144:147], v28 offset:0\n"
"s_add_u32 s62, s62, 393216\n"
"v_mfma_f32_16x16x32_bf16 v[160:163], a[92:95], a[56:59], v[160:163]\n"
"ds_read_b128 a[148:151], v28 offset:2048\n"
"v_lshlrev_b32 v60, 14, v59\n"
"v_mfma_f32_16x16x32_bf16 v[164:167], a[84:87], a[60:63], v[164:167]\n"
"ds_read_b128 a[152:155], v30 offset:0\n"
"v_add_u32 v60, s18, v60\n"
"v_mfma_f32_16x16x32_bf16 v[168:171], a[88:91], a[60:63], v[168:171]\n"
"buffer_load_dwordx4 v46, s[28:31], s62 offen lds\n"
"v_mfma_f32_16x16x32_bf16 v[172:175], a[92:95], a[60:63], v[172:175]\n"
"ds_read_b128 a[156:159], v30 offset:2048\n"
"v_accvgpr_write_b32 a127, v60\n"
"v_mfma_f32_16x16x32_bf16 v[176:179], a[84:87], a[64:67], v[176:179]\n"
"ds_read_b128 a[160:163], v32 offset:0\n"
"v_mfma_f32_16x16x32_bf16 v[180:183], a[88:91], a[64:67], v[180:183]\n"
"ds_read_b128 a[164:167], v32 offset:2048\n"
"v_mfma_f32_16x16x32_bf16 v[184:187], a[92:95], a[64:67], v[184:187]\n"
"ds_read_b128 a[168:171], v34 offset:0\n"
"v_mfma_f32_16x16x32_bf16 v[188:191], a[84:87], a[68:71], v[188:191]\n"
"buffer_load_dwordx4 v47, s[28:31], s62 offen offset:1024 lds\n"
"v_mfma_f32_16x16x32_bf16 v[192:195], a[88:91], a[68:71], v[192:195]\n"
"ds_read_b128 a[172:175], v34 offset:2048\n"
"v_mfma_f32_16x16x32_bf16 v[196:199], a[92:95], a[68:71], v[196:199]\n"
"ds_read_b128 a[176:179], v36 offset:0\n"
"v_mfma_f32_16x16x32_bf16 v[200:203], a[84:87], a[72:75], v[200:203]\n"
"v_mfma_f32_16x16x32_bf16 v[204:207], a[88:91], a[72:75], v[204:207]\n"
"buffer_load_dwordx4 v48, s[28:31], s62 offen offset:2048 lds\n"
"v_mfma_f32_16x16x32_bf16 v[208:211], a[92:95], a[72:75], v[208:211]\n"
"v_mfma_f32_16x16x32_bf16 v[212:215], a[84:87], a[76:79], v[212:215]\n"
"v_mfma_f32_16x16x32_bf16 v[216:219], a[88:91], a[76:79], v[216:219]\n"
"v_mfma_f32_16x16x32_bf16 v[220:223], a[92:95], a[76:79], v[220:223]\n"
"v_mfma_f32_16x16x32_bf16 v[224:227], a[84:87], a[80:83], v[224:227]\n"
"s_add_u32 m0, s53, 16384\n"
"s_mov_b32 s60, 8\n"
"s_mul_i32 s62, s60, 786432\n"
"v_mfma_f32_16x16x32_bf16 v[228:231], a[88:91], a[80:83], v[228:231]\n"
"v_mfma_f32_16x16x32_bf16 v[232:235], a[92:95], a[80:83], v[232:235]\n"
"s_waitcnt vmcnt(11) lgkmcnt(0)\n"
"s_barrier\n"
"ds_read_b64_tr_b16 a[84:85], v38 offset:19456\n"
"v_mfma_f32_16x16x32_bf16 v[128:131], a[180:183], a[144:147], v[128:131]\n"
"buffer_load_dwordx4 v46, s[28:31], s62 offen lds\n"
"v_mfma_f32_16x16x32_bf16 v[132:135], a[184:187], a[144:147], v[132:135]\n"
"ds_read_b64_tr_b16 a[86:87], v38 offset:19968\n"
"v_mfma_f32_16x16x32_bf16 v[136:139], a[188:191], a[144:147], v[136:139]\n"
"ds_read_b64_tr_b16 a[88:89], v39 offset:19456\n"
"v_mfma_f32_16x16x32_bf16 v[140:143], a[180:183], a[148:151], v[140:143]\n"
"ds_read_b64_tr_b16 a[90:91], v39 offset:19968\n"
"v_mfma_f32_16x16x32_bf16 v[144:147], a[184:187], a[148:151], v[144:147]\n"
"s_nop 0\n"
".Lpad8_%=:\n"
"s_cbranch_execz .Lwb_%=\n"
"buffer_load_dwordx4 v47, s[28:31], s62 offen offset:1024 lds\n"
"v_mfma_f32_16x16x32_bf16 v[148:151], a[188:191], a[148:151], v[148:151]\n"
"ds_read_b64_tr_b16 a[92:93], v40 offset:19456\n"
"v_mfma_f32_16x16x32_bf16 v[152:155], a[180:183], a[152:155], v[152:155]\n"
"ds_read_b64_tr_b16 a[94:95], v40 offset:19968\n"
"v_mfma_f32_16x16x32_bf16 v[156:159], a[184:187], a[152:155], v[156:159]\n"
"ds_read_b128 a[48:51], v29 offset:0\n"
"v_mfma_f32_16x16x32_bf16 v[160:163], a[188:191], a[152:155], v[160:163]\n"
"ds_read_b128 a[52:55], v29 offset:2048\n"
"v_mfma_f32_16x16x32_bf16 v[164:167], a[180:183], a[156:159], v[164:167]\n"
"buffer_load_dwordx4 v48, s[28:31], s62 offen offset:2048 lds\n"
"v_mfma_f32_16x16x32_bf16 v[168:171], a[184:187], a[156:159], v[168:171]\n"
"s_add_u32 m0, s53, 9216\n"
"ds_read_b128 a[56:59], v31 offset:0\n"
"s_lshl_b32 s61, s60, 7\n"
"v_mfma_f32_16x16x32_bf16 v[172:175], a[188:191], a[156:159], v[172:175]\n"
"ds_read_b128 a[60:63], v31 offset:2048\n"
"v_mfma_f32_16x16x32_bf16 v[176:179], a[180:183], a[160:163], v[176:179]\n"
"ds_read_b128 a[64:67], v33 offset:0\n"
"v_mfma_f32_16x16x32_bf16 v[180:183], a[184:187], a[160:163], v[180:183]\n"
"buffer_load_dwordx4 v41, s[24:27], s61 offen lds\n"
"v_mfma_f32_16x16x32_bf16 v[184:187], a[188:191], a[160:163], v[184:187]\n"
"ds_read_b128 a[68:71], v33 offset:2048\n"
"v_mfma_f32_16x16x32_bf16 v[188:191], a[180:183], a[164:167], v[188:191]\n"
"ds_read_b128 a[72:75], v35 offset:0\n"
"v_mfma_f32_16x16x32_bf16 v[192:195], a[184:187], a[164:167], v[192:195]\n"
"ds_read_b128 a[76:79], v35 offset:2048\n"
"v_mfma_f32_16x16x32_bf16 v[196:199], a[188:191], a[164:167], v[196:199]\n"
"ds_read_b128 a[80:83], v37 offset:0\n"
"v_mfma_f32_16x16x32_bf16 v[200:203], a[180:183], a[168:171], v[200:203]\n"
"buffer_load_dwordx4 v42, s[24:27], s61 offen offset:1024 lds\n"
"v_mfma_f32_16x16x32_bf16 v[204:207], a[184:187], a[168:171], v[204:207]\n"
"v_mfma_f32_16x16x32_bf16 v[208:211], a[188:191], a[168:171], v[208:211]\n"
"v_mfma_f32_16x16x32_bf16 v[212:215], a[180:183], a[172:175], v[212:215]\n"
"v_mfma_f32_16x16x32_bf16 v[216:219], a[184:187], a[172:175], v[216:219]\n"
"v_mfma_f32_16x16x32_bf16 v[220:223], a[188:191], a[172:175], v[220:223]\n"
"buffer_load_dwordx4 v43, s[24:27], s61 offen offset:2048 lds\n"
"v_mfma_f32_16x16x32_bf16 v[224:227], a[180:183], a[176:179], v[224:227]\n"
"v_mfma_f32_16x16x32_bf16 v[228:231], a[184:187], a[176:179], v[228:231]\n"
"v_mfma_f32_16x16x32_bf16 v[232:235], a[188:191], a[176:179], v[232:235]\n"
"s_waitcnt vmcnt(9) lgkmcnt(0)\n"
"s_barrier\n"
"ds_read_b64_tr_b16 a[180:181], v38 offset:22528\n"
"v_mfma_f32_16x16x32_bf16 v[128:131], a[84:87], a[48:51], v[128:131]\n"
"buffer_load_dwordx4 v44, s[24:27], s61 offen offset:3072 lds\n"
"v_mfma_f32_16x16x32_bf16 v[132:135], a[88:91], a[48:51], v[132:135]\n"
"ds_read_b64_tr_b16 a[182:183], v38 offset:23040\n"
"v_mfma_f32_16x16x32_bf16 v[136:139], a[92:95], a[48:51], v[136:139]\n"
"s_add_u32 m0, s53, 13312\n"
"ds_read_b64_tr_b16 a[184:185], v39 offset:22528\n"
"v_mfma_f32_16x16x32_bf16 v[140:143], a[84:87], a[52:55], v[140:143]\n"
"ds_read_b64_tr_b16 a[186:187], v39 offset:23040\n"
"v_mfma_f32_16x16x32_bf16 v[144:147], a[88:91], a[52:55], v[144:147]\n"
"ds_read_b64_tr_b16 a[188:189], v40 offset:22528\n"
"v_mfma_f32_16x16x32_bf16 v[148:151], a[92:95], a[52:55], v[148:151]\n"
"s_mov_b32 exec_hi, 0\n"
"buffer_load_dwordx4 v45, s[24:27], s61 offen lds\n"
"s_mov_b32 exec_hi, -1\n"
"v_mfma_f32_16x16x32_bf16 v[152:155], a[84:87], a[56:59], v[152:155]\n"
"s_add_u32 m0, s53, 19456\n"
"ds_read_b64_tr_b16 a[190:191], v40 offset:23040\n"
"s_add_u32 s62, s62, 393216\n"
"v_mfma_f32_16x16x32_bf16 v[156:159], a[88:91], a[56:59], v[156:159]\n"
"ds_read_b128 a[144:147], v28 offset:4608\n"
"v_mfma_f32_16x16x32_bf16 v[160:163], a[92:95], a[56:59], v[160:163]\n"
"ds_read_b128 a[148:151], v28 offset:6656\n"
"v_mfma_f32_16x16x32_bf16 v[164:167], a[84:87], a[60:63], v[164:167]\n"
"buffer_load_dwordx4 v46, s[28:31], s62 offen lds\n"
"v_mfma_f32_16x16x32_bf16 v[168:171], a[88:91], a[60:63], v[168:171]\n"
"ds_read_b128 a[152:155], v30 offset:4608\n"
"v_mfma_f32_16x16x32_bf16 v[172:175], a[92:95], a[60:63], v[172:175]\n"
"ds_read_b128 a[156:159], v30 offset:6656\n"
"v_mfma_f32_16x16x32_bf16 v[176:179], a[84:87], a[64:67], v[176:179]\n"
"ds_read_b128 a[160:163], v32 offset:4608\n"
"v_mfma_f32_16x16x32_bf16 v[180:183], a[88:91], a[64:67], v[180:183]\n"
"ds_read_b128 a[164:167], v32 offset:6656\n"
"v_mfma_f32_16x16x32_bf16 v[184:187], a[92:95], a[64:67], v[184:187]\n"
"buffer_load_dwordx4 v47, s[28:31], s62 offen offset:1024 lds\n"
"v_mfma_f32_16x16x32_bf16 v[188:191], a[84:87], a[68:71], v[188:191]\n"
"ds_read_b128 a[168:171], v34 offset:4608\n"
"v_mfma_f32_16x16x32_bf16 v[192:195], a[88:91], a[68:71], v[192:195]\n"
"ds_read_b128 a[172:175], v34 offset:6656\n"
"v_mfma_f32_16x16x32_bf16 v[196:199], a[92:95], a[68:71], v[196:199]\n"
"ds_read_b128 a[176:179], v36 offset:4608\n"
"v_mfma_f32_16x16x32_bf16 v[200:203], a[84:87], a[72:75], v[200:203]\n"
"v_mfma_f32_16x16x32_bf16 v[204:207], a[88:91], a[72:75], v[204:207]\n"
"buffer_load_dwordx4 v48, s[28:31], s62 offen offset:2048 lds\n"
"v_mfma_f32_16x16x32_bf16 v[208:211], a[92:95], a[72:75], v[208:211]\n"
"v_mfma_f32_16x16x32_bf16 v[212:215], a[84:87], a[76:79], v[212:215]\n"
"v_mfma_f32_16x16x32_bf16 v[216:219], a[88:91], a[76:79], v[216:219]\n"
"v_mfma_f32_16x16x32_bf16 v[220:223], a[92:95], a[76:79], v[220:223]\n"
"v_mfma_f32_16x16x32_bf16 v[224:227], a[84:87], a[80:83], v[224:227]\n"
"s_add_u32 m0, s53, 22528\n"
"s_mov_b32 s60, 9\n"
"s_mul_i32 s62, s60, 786432\n"
"v_mfma_f32_16x16x32_bf16 v[228:231], a[88:91], a[80:83], v[228:231]\n"
"v_mfma_f32_16x16x32_bf16 v[232:235], a[92:95], a[80:83], v[232:235]\n"
"s_waitcnt vmcnt(11) lgkmcnt(0)\n"
"s_barrier\n"
"ds_read_b64_tr_b16 a[84:85], v38 offset:25600\n"
"v_mfma_f32_16x16x32_bf16 v[128:131], a[180:183], a[144:147], v[128:131]\n"
"buffer_load_dwordx4 v46, s[28:31], s62 offen lds\n"
"v_mfma_f32_16x16x32_bf16 v[132:135], a[184:187], a[144:147], v[132:135]\n"
"ds_read_b64_tr_b16 a[86:87], v38 offset:26112\n"
"v_mfma_f32_16x16x32_bf16 v[136:139], a[188:191], a[144:147], v[136:139]\n"
"ds_read_b64_tr_b16 a[88:89], v39 offset:25600\n"
"v_mfma_f32_16x16x32_bf16 v[140:143], a[180:183], a[148:151], v[140:143]\n"
"ds_read_b64_tr_b16 a[90:91], v39 offset:26112\n"
"v_mfma_f32_16x16x32_bf16 v[144:147], a[184:187], a[148:151], v[144:147]\n"
"buffer_load_dwordx4 v47, s[28:31], s62 offen offset:1024 lds\n"
"v_mfma_f32_16x16x32_bf16 v[148:151], a[188:191], a[148:151], v[148:151]\n"
"ds_read_b64_tr_b16 a[92:93], v40 offset:25600\n"
"v_mfma_f32_16x16x32_bf16 v[152:155], a[180:183], a[152:155], v[152:155]\n"
"ds_read_b64_tr_b16 a[94:95], v40 offset:26112\n"
"v_mfma_f32_16x16x32_bf16 v[156:159], a[184:187], a[152:155], v[156:159]\n"
"ds_read_b128 a[48:51], v29 offset:4608\n"
"v_mfma_f32_16x16x32_bf16 v[160:163], a[188:191], a[152:155], v[160:163]\n"
"ds_read_b128 a[52:55], v29 offset:6656\n"
"v_mfma_f32_16x16x32_bf16 v[164:167], a[180:183], a[156:159], v[164:167]\n"
"buffer_load_dwordx4 v48, s[28:31], s62 offen offset:2048 lds\n"
"v_mfma_f32_16x16x32_bf16 v[168:171], a[184:187], a[156:159], v[168:171]\n"
"s_add_u32 m0, s53, 0\n"
"ds_read_b128 a[56:59], v31 offset:4608\n"
"s_lshl_b32 s61, s60, 7\n"
"v_mfma_f32_16x16x32_bf16 v[172:175], a[188:191], a[156:159], v[172:175]\n"
"ds_read_b128 a[60:63], v31 offset:6656\n"
"v_mfma_f32_16x16x32_bf16 v[176:179], a[180:183], a[160:163], v[176:179]\n"
"ds_read_b128 a[64:67], v33 offset:4608\n"
"v_mfma_f32_16x16x32_bf16 v[180:183], a[184:187], a[160:163], v[180:183]\n"
"buffer_load_dwordx4 v41, s[24:27], s61 offen lds\n"
"v_mfma_f32_16x16x32_bf16 v[184:187], a[188:191], a[160:163], v[184:187]\n"
"ds_read_b128 a[68:71], v33 offset:6656\n"
"v_mfma_f32_16x16x32_bf16 v[188:191], a[180:183], a[164:167], v[188:191]\n"
"ds_read_b128 a[72:75], v35 offset:4608\n"
"v_mfma_f32_16x16x32_bf16 v[192:195], a[184:187], a[164:167], v[192:195]\n"
"ds_read_b128 a[76:79], v35 offset:6656\n"
"v_mfma_f32_16x16x32_bf16 v[196:199], a[188:191], a[164:167], v[196:199]\n"
"ds_read_b128 a[80:83], v37 offset:4608\n"
"v_mfma_f32_16x16x32_bf16 v[200:203], a[180:183], a[168:171], v[200:203]\n"
"buffer_load_dwordx4 v42, s[24:27], s61 offen offset:1024 lds\n"
"v_mfma_f32_16x16x32_bf16 v[204:207], a[184:187], a[168:171], v[204:207]\n"
"v_mfma_f32_16x16x32_bf16 v[208:211], a[188:191], a[168:171], v[208:211]\n"
"v_mfma_f32_16x16x32_bf16 v[212:215], a[180:183], a[172:175], v[212:215]\n"
"v_mfma_f32_16x16x32_bf16 v[216:219], a[184:187], a[172:175], v[216:219]\n"
"v_mfma_f32_16x16x32_bf16 v[220:223], a[188:191], a[172:175], v[220:223]\n"
"buffer_load_dwordx4 v43, s[24:27], s61 offen offset:2048 lds\n"
"v_mfma_f32_16x16x32_bf16 v[224:227], a[180:183], a[176:179], v[224:227]\n"
"v_mfma_f32_16x16x32_bf16 v[228:231], a[184:187], a[176:179], v[228:231]\n"
"v_mfma_f32_16x16x32_bf16 v[232:235], a[188:191], a[176:179], v[232:235]\n"
"s_waitcnt vmcnt(9) lgkmcnt(0)\n"
"s_barrier\n"
"ds_read_b64_tr_b16 a[180:181], v38 offset:16384\n"
"v_mfma_f32_16x16x32_bf16 v[128:131], a[84:87], a[48:51], v[128:131]\n"
"buffer_load_dwordx4 v44, s[24:27], s61 offen offset:3072 lds\n"
"v_mfma_f32_16x16x32_bf16 v[132:135], a[88:91], a[48:51], v[132:135]\n"
"ds_read_b64_tr_b16 a[182:183], v38 offset:16896\n"
"v_mfma_f32_16x16x32_bf16 v[136:139], a[92:95], a[48:51], v[136:139]\n"
"s_add_u32 m0, s53, 4096\n"
"ds_read_b64_tr_b16 a[184:185], v39 offset:16384\n"
"v_mfma_f32_16x16x32_bf16 v[140:143], a[84:87], a[52:55], v[140:143]\n"
"ds_read_b64_tr_b16 a[186:187], v39 offset:16896\n"
"v_mfma_f32_16x16x32_bf16 v[144:147], a[88:91], a[52:55], v[144:147]\n"
"ds_read_b64_tr_b16 a[188:189], v40 offset:16384\n"
"v_mfma_f32_16x16x32_bf16 v[148:151], a[92:95], a[52:55], v[148:151]\n"
"s_mov_b32 exec_hi, 0\n"
"buffer_load_dwordx4 v45, s[24:27], s61 offen lds\n"
"s_mov_b32 exec_hi, -1\n"
"v_mfma_f32_16x16x32_bf16 v[152:155], a[84:87], a[56:59], v[152:155]\n"
"s_add_u32 m0, s53, 25600\n"
"ds_read_b64_tr_b16 a[190:191], v40 offset:16896\n"
"s_add_u32 s62, s62, 393216\n"
"v_mfma_f32_16x16x32_bf16 v[156:159], a[88:91], a[56:59], v[156:159]\n"
"ds_read_b128 a[144:147], v28 offset:9216\n"
"v_mfma_f32_16x16x32_bf16 v[160:163], a[92:95], a[56:59], v[160:163]\n"
"ds_read_b128 a[148:151], v28 offset:11264\n"
"v_mfma_f32_16x16x32_bf16 v[164:167], a[84:87], a[60:63], v[164:167]\n"
"buffer_load_dwordx4 v46, s[28:31], s62 offen lds\n"
"v_mfma_f32_16x16x32_bf16 v[168:171], a[88:91], a[60:63], v[168:171]\n"
"ds_read_b128 a[152:155], v30 offset:9216\n"
"v_mfma_f32_16x16x32_bf16 v[172:175], a[92:95], a[60:63], v[172:175]\n"
"ds_read_b128 a[156:159], v30 offset:11264\n"
"v_mfma_f32_16x16x32_bf16 v[176:179], a[84:87], a[64:67], v[176:179]\n"
"ds_read_b128 a[160:163], v32 offset:9216\n"
"v_mfma_f32_16x16x32_bf16 v[180:183], a[88:91], a[64:67], v[180:183]\n"
"ds_read_b128 a[164:167], v32 offset:11264\n"
"v_mfma_f32_16x16x32_bf16 v[184:187], a[92:95], a[64:67], v[184:187]\n"
"buffer_load_dwordx4 v47, s[28:31], s62 offen offset:1024 lds\n"
"v_mfma_f32_16x16x32_bf16 v[188:191], a[84:87], a[68:71], v[188:191]\n"
"ds_read_b128 a[168:171], v34 offset:9216\n"
"v_mfma_f32_16x16x32_bf16 v[192:195], a[88:91], a[68:71], v[192:195]\n"
"ds_read_b128 a[172:175], v34 offset:11264\n"
"v_mfma_f32_16x16x32_bf16 v[196:199], a[92:95], a[68:71], v[196:199]\n"
"ds_read_b128 a[176:179], v36 offset:9216\n"
"v_mfma_f32_16x16x32_bf16 v[200:203], a[84:87], a[72:75], v[200:203]\n"
"v_mfma_f32_16x16x32_bf16 v[204:207], a[88:91], a[72:75], v[204:207]\n"
"buffer_load_dwordx4 v48, s[28:31], s62 offen offset:2048 lds\n"
"v_mfma_f32_16x16x32_bf16 v[208:211], a[92:95], a[72:75], v[208:211]\n"
"s_add_u32 m0, s53, 28672\n"
"s_mov_b32 s63, 0\n"
"v_mfma_f32_16x16x32_bf16 v[212:215], a[84:87], a[76:79], v[212:215]\n"
"v_mfma_f32_16x16x32_bf16 v[216:219], a[88:91], a[76:79], v[216:219]\n"
"v_mfma_f32_16x16x32_bf16 v[220:223], a[92:95], a[76:79], v[220:223]\n"
"buffer_load_dwordx4 v2, s[32:35], s63 offen nt lds\n"
"v_mfma_f32_16x16x32_bf16 v[224:227], a[84:87], a[80:83], v[224:227]\n"
"v_mfma_f32_16x16x32_bf16 v[228:231], a[88:91], a[80:83], v[228:231]\n"
"buffer_load_dwordx4 v3, s[32:35], s63 offen offset:1024 nt lds\n"
"s_nop 10\n"
"v_mfma_f32_16x16x32_bf16 v[232:235], a[92:95], a[80:83], v[232:235]\n"
"s_waitcnt vmcnt(13) lgkmcnt(0)\n"
"s_barrier\n"
"ds_read_b64_tr_b16 a[84:85], v38 offset:19456\n"
"s_nop 10\n"
"v_mfma_f32_16x16x32_bf16 v[128:131], a[180:183], a[144:147], v[128:131]\n"
"ds_read_b64_tr_b16 a[86:87], v38 offset:19968\n"
"s_nop 11\n"
"v_mfma_f32_16x16x32_bf16 v[132:135], a[184:187], a[144:147], v[132:135]\n"
"buffer_load_dwordx4 v4, s[32:35], s63 offen offset:2048 nt lds\n"
"s_nop 11\n"
"v_mfma_f32_16x16x32_bf16 v[136:139], a[188:191], a[144:147], v[136:139]\n"
"ds_read_b64_tr_b16 a[88:89], v39 offset:19456\n"
"s_nop 11\n"
"v_mfma_f32_16x16x32_bf16 v[140:143], a[180:183], a[148:151], v[140:143]\n"
"buffer_load_dwordx4 v5, s[32:35], s63 offen offset:3072 nt lds\n"
"s_add_u32 m0, s53, 32768\n"
"s_nop 10\n"
"v_mfma_f32_16x16x32_bf16 v[144:147], a[184:187], a[148:151], v[144:147]\n"
"ds_read_b64_tr_b16 a[90:91], v39 offset:19968\n"
"s_nop 11\n"
"v_mfma_f32_16x16x32_bf16 v[148:151], a[188:191], a[148:151], v[148:151]\n"
"buffer_load_dwordx4 v6, s[32:35], s63 offen nt lds\n"
"s_nop 10\n"
"v_mfma_f32_16x16x32_bf16 v[152:155], a[180:183], a[152:155], v[152:155]\n"
"ds_read_b64_tr_b16 a[92:93], v40 offset:19456\n"
"s_nop 11\n"
"v_mfma_f32_16x16x32_bf16 v[156:159], a[184:187], a[152:155], v[156:159]\n"
"buffer_load_dwordx4 v7, s[32:35], s63 offen offset:1024 nt lds\n"
"s_add_u32 m0, s53, 34816\n"
"s_mov_b32 s63, 24576\n"
"s_nop 9\n"
"v_mfma_f32_16x16x32_bf16 v[160:163], a[188:191], a[152:155], v[160:163]\n"
"ds_read_b64_tr_b16 a[94:95], v40 offset:19968\n"
"s_nop 11\n"
"v_mfma_f32_16x16x32_bf16 v[164:167], a[180:183], a[156:159], v[164:167]\n"
"buffer_load_dwordx4 v2, s[32:35], s63 offen nt lds\n"
"s_nop 11\n"
"v_mfma_f32_16x16x32_bf16 v[168:171], a[184:187], a[156:159], v[168:171]\n"
"ds_read_b128 a[48:51], v29 offset:9216\n"
"s_nop 11\n"
"v_mfma_f32_16x16x32_bf16 v[172:175], a[188:191], a[156:159], v[172:175]\n"
"ds_read_b128 a[52:55], v29 offset:11264\n"
"s_nop 11\n"
"v_mfma_f32_16x16x32_bf16 v[176:179], a[180:183], a[160:163], v[176:179]\n"
"buffer_load_dwordx4 v3, s[32:35], s63 offen offset:1024 nt lds\n"
"s_nop 10\n"
"v_mfma_f32_16x16x32_bf16 v[180:183], a[184:187], a[160:163], v[180:183]\n"
"ds_read_b128 a[56:59], v31 offset:9216\n"
"s_nop 12\n"
"v_mfma_f32_16x16x32_bf16 v[184:187], a[188:191], a[160:163], v[184:187]\n"
"ds_read_b128 a[60:63], v31 offset:11264\n"
"buffer_load_dwordx4 v4, s[32:35], s63 offen offset:2048 nt lds\n"
"s_nop 8\n"
"v_mfma_f32_16x16x32_bf16 v[188:191], a[180:183], a[164:167], v[188:191]\n"
"ds_read_b128 a[64:67], v33 offset:9216\n"
"s_nop 12\n"
"v_mfma_f32_16x16x32_bf16 v[192:195], a[184:187], a[164:167], v[192:195]\n"
"ds_read_b128 a[68:71], v33 offset:11264\n"
"buffer_load_dwordx4 v5, s[32:35], s63 offen offset:3072 nt lds\n"
"s_add_u32 m0, s53, 38912\n"
"s_nop 7\n"
"v_mfma_f32_16x16x32_bf16 v[196:199], a[188:191], a[164:167], v[196:199]\n"
"ds_read_b128 a[72:75], v35 offset:9216\n"
"s_nop 12\n"
"v_mfma_f32_16x16x32_bf16 v[200:203], a[180:183], a[168:171], v[200:203]\n"
"ds_read_b128 a[76:79], v35 offset:11264\n"
"buffer_load_dwordx4 v6, s[32:35], s63 offen nt lds\n"
"s_nop 8\n"
"v_mfma_f32_16x16x32_bf16 v[204:207], a[184:187], a[168:171], v[204:207]\n"
"ds_read_b128 a[80:83], v37 offset:9216\n"
"s_nop 12\n"
"v_mfma_f32_16x16x32_bf16 v[208:211], a[188:191], a[168:171], v[208:211]\n"
"buffer_load_dwordx4 v7, s[32:35], s63 offen offset:1024 nt lds\n"
"s_mov_b32 s63, 98304\n"
"s_nop 9\n"
"v_mfma_f32_16x16x32_bf16 v[212:215], a[180:183], a[172:175], v[212:215]\n"
"s_nop 14\n"
"v_mfma_f32_16x16x32_bf16 v[216:219], a[184:187], a[172:175], v[216:219]\n"
"s_nop 13\n"
"v_mfma_f32_16x16x32_bf16 v[220:223], a[188:191], a[172:175], v[220:223]\n"
"buffer_load_dwordx4 a[0:3], v2, s[32:35], s63 offen offset:0 nt\n"
"s_nop 10\n"
"v_mfma_f32_16x16x32_bf16 v[224:227], a[180:183], a[176:179], v[224:227]\n"
"s_nop 13\n"
"v_mfma_f32_16x16x32_bf16 v[228:231], a[184:187], a[176:179], v[228:231]\n"
"buffer_load_dwordx4 a[4:7], v3, s[32:35], s63 offen offset:1024 nt\n"
"s_nop 11\n"
"v_mfma_f32_16x16x32_bf16 v[232:235], a[188:191], a[176:179], v[232:235]\n"
"s_waitcnt vmcnt(14) lgkmcnt(0)\n"
"s_barrier\n"
"ds_read_b64_tr_b16 a[180:181], v38 offset:22528\n"
"ds_read_b64_tr_b16 a[182:183], v38 offset:23040\n"
"s_nop 8\n"
"v_mfma_f32_16x16x32_bf16 v[128:131], a[84:87], a[48:51], v[128:131]\n"
"ds_read_b64_tr_b16 a[184:185], v39 offset:22528\n"
"buffer_load_dwordx4 a[8:11], v4, s[32:35], s63 offen offset:2048 nt\n"
"s_nop 9\n"
"v_mfma_f32_16x16x32_bf16 v[132:135], a[88:91], a[48:51], v[132:135]\n"
"ds_read_b64_tr_b16 a[186:187], v39 offset:23040\n"
"s_nop 11\n"
"v_mfma_f32_16x16x32_bf16 v[136:139], a[92:95], a[48:51], v[136:139]\n"
"ds_read_b64_tr_b16 a[188:189], v40 offset:22528\n"
"ds_read_b64_tr_b16 a[190:191], v40 offset:23040\n"
"buffer_load_dwordx4 a[12:15], v5, s[32:35], s63 offen offset:3072 nt\n"
"s_nop 7\n"
"v_mfma_f32_16x16x32_bf16 v[140:143], a[84:87], a[52:55], v[140:143]\n"
"ds_read_b128 a[144:147], v28 offset:0\n"
"s_nop 11\n"
"v_mfma_f32_16x16x32_bf16 v[144:147], a[88:91], a[52:55], v[144:147]\n"
"ds_read_b128 a[148:151], v28 offset:2048\n"
"buffer_load_dwordx4 a[16:19], v6, s[32:35], s63 offen offset:0 nt\n"
"s_nop 9\n"
"v_mfma_f32_16x16x32_bf16 v[148:151], a[92:95], a[52:55], v[148:151]\n"
"ds_read_b128 a[152:155], v30 offset:0\n"
"ds_read_b128 a[156:159], v30 offset:2048\n"
"s_nop 9\n"
"v_mfma_f32_16x16x32_bf16 v[152:155], a[84:87], a[56:59], v[152:155]\n"
"ds_read_b128 a[160:163], v32 offset:0\n"
"s_nop 11\n"
"v_mfma_f32_16x16x32_bf16 v[156:159], a[88:91], a[56:59], v[156:159]\n"
"ds_read_b128 a[164:167], v32 offset:2048\n"
"buffer_load_dwordx4 a[20:23], v7, s[32:35], s63 offen offset:1024 nt\n"
"s_mov_b32 s63, 122880\n"
"s_nop 8\n"
"v_mfma_f32_16x16x32_bf16 v[160:163], a[92:95], a[56:59], v[160:163]\n"
"ds_read_b128 a[168:171], v34 offset:0\n"
"ds_read_b128 a[172:175], v34 offset:2048\n"
"s_nop 9\n"
"v_mfma_f32_16x16x32_bf16 v[164:167], a[84:87], a[60:63], v[164:167]\n"
"ds_read_b128 a[176:179], v36 offset:0\n"
"buffer_load_dwordx4 a[24:27], v2, s[32:35], s63 offen offset:0 nt\n"
"s_nop 9\n"
"v_mfma_f32_16x16x32_bf16 v[168:171], a[88:91], a[60:63], v[168:171]\n"
"ds_read_b64_tr_b16 a[228:229], v38 offset:25600\n"
"s_nop 11\n"
"v_mfma_f32_16x16x32_bf16 v[172:175], a[92:95], a[60:63], v[172:175]\n"
"ds_read_b64_tr_b16 a[230:231], v38 offset:26112\n"
"buffer_load_dwordx4 a[28:31], v3, s[32:35], s63 offen offset:1024 nt\n"
"s_nop 9\n"
"v_mfma_f32_16x16x32_bf16 v[176:179], a[84:87], a[64:67], v[176:179]\n"
"ds_read_b64_tr_b16 a[232:233], v39 offset:25600\n"
"ds_read_b64_tr_b16 a[234:235], v39 offset:26112\n"
"s_nop 9\n"
"v_mfma_f32_16x16x32_bf16 v[180:183], a[88:91], a[64:67], v[180:183]\n"
"ds_read_b64_tr_b16 a[236:237], v40 offset:25600\n"
"buffer_load_dwordx4 a[32:35], v4, s[32:35], s63 offen offset:2048 nt\n"
"s_nop 9\n"
"v_mfma_f32_16x16x32_bf16 v[184:187], a[92:95], a[64:67], v[184:187]\n"
"ds_read_b64_tr_b16 a[238:239], v40 offset:26112\n"
"s_nop 11\n"
"v_mfma_f32_16x16x32_bf16 v[188:191], a[84:87], a[68:71], v[188:191]\n"
"ds_read_b128 a[192:195], v29 offset:0\n"
"ds_read_b128 a[196:199], v29 offset:2048\n"
"buffer_load_dwordx4 a[36:39], v5, s[32:35], s63 offen offset:3072 nt\n"
"s_nop 7\n"
"v_mfma_f32_16x16x32_bf16 v[192:195], a[88:91], a[68:71], v[192:195]\n"
"ds_read_b128 a[200:203], v31 offset:0\n"
"s_nop 11\n"
"v_mfma_f32_16x16x32_bf16 v[196:199], a[92:95], a[68:71], v[196:199]\n"
"ds_read_b128 a[204:207], v31 offset:2048\n"
"s_nop 11\n"
"v_mfma_f32_16x16x32_bf16 v[200:203], a[84:87], a[72:75], v[200:203]\n"
"ds_read_b128 a[208:211], v33 offset:0\n"
"ds_read_b128 a[212:215], v33 offset:2048\n"
"buffer_load_dwordx4 a[40:43], v6, s[32:35], s63 offen offset:0 nt\n"
"s_nop 7\n"
"v_mfma_f32_16x16x32_bf16 v[204:207], a[88:91], a[72:75], v[204:207]\n"
"ds_read_b128 a[216:219], v35 offset:0\n"
"s_nop 11\n"
"v_mfma_f32_16x16x32_bf16 v[208:211], a[92:95], a[72:75], v[208:211]\n"
"ds_read_b128 a[220:223], v35 offset:2048\n"
"buffer_load_dwordx4 a[44:47], v7, s[32:35], s63 offen offset:1024 nt\n"
"s_mov_b32 s63, 147456\n"
"s_nop 8\n"
"v_mfma_f32_16x16x32_bf16 v[212:215], a[84:87], a[76:79], v[212:215]\n"
"ds_read_b128 a[224:227], v37 offset:0\n"
"s_nop 11\n"
"v_mfma_f32_16x16x32_bf16 v[216:219], a[88:91], a[76:79], v[216:219]\n"
"buffer_load_dwordx4 a[96:99], v2, s[32:35], s63 offen offset:0 nt\n"
"s_nop 11\n"
"v_mfma_f32_16x16x32_bf16 v[220:223], a[92:95], a[76:79], v[220:223]\n"
"s_nop 13\n"
"v_mfma_f32_16x16x32_bf16 v[224:227], a[84:87], a[80:83], v[224:227]\n"
"buffer_load_dwordx4 a[100:103], v3, s[32:35], s63 offen offset:1024 nt\n"
"s_nop 10\n"
"v_mfma_f32_16x16x32_bf16 v[228:231], a[88:91], a[80:83], v[228:231]\n"
"s_nop 13\n"
"v_mfma_f32_16x16x32_bf16 v[232:235], a[92:95], a[80:83], v[232:235]\n"
"s_waitcnt vmcnt(20) lgkmcnt(0)\n"
"s_barrier\n"
"buffer_load_dwordx4 a[104:107], v4, s[32:35], s63 offen offset:2048 nt\n"
"s_nop 10\n"
"v_mfma_f32_16x16x32_bf16 v[224:227], a[180:183], a[176:179], v[224:227]\n"
"ds_read_b128 a[48:51], v20 offset:28672\n"
"v_readfirstlane_b32 s58, v124\n"
"v_readfirstlane_b32 s59, v125\n"
"s_nop 9\n"
"v_mfma_f32_16x16x32_bf16 v[224:227], a[228:231], a[224:227], v[224:227]\n"
"ds_read_b128 a[52:55], v21 offset:28672\n"
"buffer_load_dwordx4 a[108:111], v5, s[32:35], s63 offen offset:3072 nt\n"
"s_nop 9\n"
"v_mfma_f32_16x16x32_bf16 v[228:231], a[184:187], a[176:179], v[228:231]\n"
"ds_read_b128 a[56:59], v20 offset:28800\n"
"s_nop 11\n"
"v_mfma_f32_16x16x32_bf16 v[228:231], a[232:235], a[224:227], v[228:231]\n"
"ds_read_b128 a[60:63], v21 offset:28800\n"
"s_nop 12\n"
"v_mfma_f32_16x16x32_bf16 v[232:235], a[188:191], a[176:179], v[232:235]\n"
"ds_read_b128 a[64:67], v20 offset:28928\n"
"buffer_load_dwordx4 a[128:131], v6, s[32:35], s63 offen offset:0 nt\n"
"v_mul_f32 v224, s64, v224\n"
"v_mul_f32 v225, s64, v225\n"
"v_mul_f32 v226, s64, v226\n"
"v_mul_f32 v227, s64, v227\n"
"v_cvt_pk_bf16_f32 v64, v224, v225\n"
"v_cvt_pk_bf16_f32 v65, v226, v227\n"
"s_nop 2\n"
"v_mfma_f32_16x16x32_bf16 v[232:235], a[236:239], a[224:227], v[232:235]\n"
"ds_write_b64 v55, v[64:65]\n"
"s_nop 9\n"
"v_mfma_f32_16x16x32_bf16 v[128:131], a[180:183], a[144:147], v[128:131]\n"
"ds_read_b128 a[68:71], v21 offset:28928\n"
"buffer_load_dwordx4 a[132:135], v7, s[32:35], s63 offen offset:1024 nt\n"
"v_mul_f32 v228, s64, v228\n"
"v_mul_f32 v229, s64, v229\n"
"v_mul_f32 v230, s64, v230\n"
"v_mul_f32 v231, s64, v231\n"
"v_cvt_pk_bf16_f32 v66, v228, v229\n"
"v_cvt_pk_bf16_f32 v67, v230, v231\n"
"s_nop 3\n"
"v_mfma_f32_16x16x32_bf16 v[128:131], a[228:231], a[192:195], v[128:131]\n"
"s_mov_b32 s63, 172032\n"
"ds_write_b64 v56, v[66:67]\n"
"s_nop 9\n"
"v_mfma_f32_16x16x32_bf16 v[132:135], a[184:187], a[144:147], v[132:135]\n"
"ds_read_b128 a[72:75], v12 offset:28672\n"
"buffer_load_dwordx4 a[136:139], v2, s[32:35], s63 offen offset:0 nt\n"
"v_mul_f32 v232, s64, v232\n"
"v_mul_f32 v233, s64, v233\n"
"v_mul_f32 v234, s64, v234\n"
"v_mul_f32 v235, s64, v235\n"
"v_cvt_pk_bf16_f32 v68, v232, v233\n"
"v_cvt_pk_bf16_f32 v69, v234, v235\n"
"s_nop 3\n"
"v_mfma_f32_16x16x32_bf16 v[132:135], a[232:235], a[192:195], v[132:135]\n"
"ds_write_b64 v57, v[68:69]\n"
"s_nop 9\n"
"v_mfma_f32_16x16x32_bf16 v[136:139], a[188:191], a[144:147], v[136:139]\n"
"ds_read_b128 a[76:79], v13 offset:28672\n"
"buffer_load_dwordx4 a[140:143], v3, s[32:35], s63 offen offset:1024 nt\n"
"v_mul_f32 v128, s64, v128\n"
"v_mul_f32 v129, s64, v129\n"
"v_mul_f32 v130, s64, v130\n"
"v_mul_f32 v131, s64, v131\n"
"v_cvt_pk_bf16_f32 v70, v128, v129\n"
"v_cvt_pk_bf16_f32 v71, v130, v131\n"
"s_nop 3\n"
"v_mfma_f32_16x16x32_bf16 v[136:139], a[236:239], a[192:195], v[136:139]\n"
"ds_write_b64 v49, v[70:71] offset:0\n"
"s_nop 9\n"
"v_mfma_f32_16x16x32_bf16 v[140:143], a[180:183], a[148:151], v[140:143]\n"
"ds_read_b128 a[80:83], v12 offset:28800\n"
"buffer_load_dwordx4 a[244:247], v4, s[32:35], s63 offen offset:2048 nt\n"
"v_mul_f32 v132, s64, v132\n"
"v_mul_f32 v133, s64, v133\n"
"v_mul_f32 v134, s64, v134\n"
"v_mul_f32 v135, s64, v135\n"
"v_cvt_pk_bf16_f32 v72, v132, v133\n"
"v_cvt_pk_bf16_f32 v73, v134, v135\n"
"s_nop 2\n"
"v_mfma_f32_16x16x32_bf16 v[140:143], a[228:231], a[196:199], v[140:143]\n"
"ds_write_b64 v50, v[72:73] offset:0\n"
"s_nop 9\n"
"v_mfma_f32_16x16x32_bf16 v[144:147], a[184:187], a[148:151], v[144:147]\n"
"ds_read_b128 a[84:87], v13 offset:28800\n"
"v_mul_f32 v136, s64, v136\n"
"v_mul_f32 v137, s64, v137\n"
"v_mul_f32 v138, s64, v138\n"
"v_mul_f32 v139, s64, v139\n"
"v_cvt_pk_bf16_f32 v74, v136, v137\n"
"v_cvt_pk_bf16_f32 v75, v138, v139\n"
"s_nop 6\n"
"v_mfma_f32_16x16x32_bf16 v[144:147], a[232:235], a[196:199], v[144:147]\n"
"buffer_load_dwordx4 a[248:251], v5, s[32:35], s63 offen offset:3072 nt\n"
"s_nop 10\n"
"v_mfma_f32_16x16x32_bf16 v[148:151], a[188:191], a[148:151], v[148:151]\n"
"ds_read_b128 a[88:91], v12 offset:28928\n"
"ds_write_b64 v51, v[74:75] offset:0\n"
"v_mul_f32 v140, s64, v140\n"
"v_mul_f32 v141, s64, v141\n"
"v_mul_f32 v142, s64, v142\n"
"v_mul_f32 v143, s64, v143\n"
"v_cvt_pk_bf16_f32 v76, v140, v141\n"
"v_cvt_pk_bf16_f32 v77, v142, v143\n"
"s_nop 1\n"
"v_mfma_f32_16x16x32_bf16 v[148:151], a[236:239], a[196:199], v[148:151]\n"
"buffer_load_dwordx4 a[252:255], v6, s[32:35], s63 offen offset:0 nt\n"
"s_nop 10\n"
"v_mfma_f32_16x16x32_bf16 v[152:155], a[180:183], a[152:155], v[152:155]\n"
"ds_read_b128 a[92:95], v13 offset:28928\n"
"ds_write_b64 v49, v[76:77] offset:6144\n"
"v_mul_f32 v144, s64, v144\n"
"v_mul_f32 v145, s64, v145\n"
"v_mul_f32 v146, s64, v146\n"
"v_mul_f32 v147, s64, v147\n"
"v_cvt_pk_bf16_f32 v78, v144, v145\n"
"v_cvt_pk_bf16_f32 v79, v146, v147\n"
"s_nop 1\n"
"v_mfma_f32_16x16x32_bf16 v[152:155], a[228:231], a[200:203], v[152:155]\n"
"buffer_load_dwordx4 v[252:255], v7, s[32:35], s63 offen offset:1024 nt\n"
"s_add_u32 m0, s53, 16384\n"
"s_mov_b32 s63, 49152\n"
"s_nop 9\n"
"v_mfma_f32_16x16x32_bf16 v[156:159], a[184:187], a[152:155], v[156:159]\n"
"ds_write_b64 v50, v[78:79] offset:6144\n"
"v_mul_f32 v148, s64, v148\n"
"v_mul_f32 v149, s64, v149\n"
"v_mul_f32 v150, s64, v150\n"
"v_mul_f32 v151, s64, v151\n"
"v_cvt_pk_bf16_f32 v64, v148, v149\n"
"v_cvt_pk_bf16_f32 v65, v150, v151\n"
"s_nop 3\n"
"v_mfma_f32_16x16x32_bf16 v[156:159], a[232:235], a[200:203], v[156:159]\n"
"buffer_load_dwordx4 v2, s[32:35], s63 offen nt lds\n"
"s_nop 10\n"
"v_mfma_f32_16x16x32_bf16 v[160:163], a[188:191], a[152:155], v[160:163]\n"
"ds_write_b64 v51, v[64:65] offset:6144\n"
"v_mul_f32 v152, s64, v152\n"
"v_mul_f32 v153, s64, v153\n"
"v_mul_f32 v154, s64, v154\n"
"v_mul_f32 v155, s64, v155\n"
"v_cvt_pk_bf16_f32 v66, v152, v153\n"
"v_cvt_pk_bf16_f32 v67, v154, v155\n"
"s_nop 3\n"
"v_mfma_f32_16x16x32_bf16 v[160:163], a[236:239], a[200:203], v[160:163]\n"
"buffer_load_dwordx4 v3, s[32:35], s63 offen offset:1024 nt lds\n"
"s_nop 10\n"
"v_mfma_f32_16x16x32_bf16 v[164:167], a[180:183], a[156:159], v[164:167]\n"
"ds_write_b64 v49, v[66:67] offset:40960\n"
"v_mul_f32 v156, s64, v156\n"
"v_mul_f32 v157, s64, v157\n"
"v_mul_f32 v158, s64, v158\n"
"v_mul_f32 v159, s64, v159\n"
"v_cvt_pk_bf16_f32 v68, v156, v157\n"
"v_cvt_pk_bf16_f32 v69, v158, v159\n"
"s_nop 3\n"
"v_mfma_f32_16x16x32_bf16 v[164:167], a[228:231], a[204:207], v[164:167]\n"
"ds_write_b64 v50, v[68:69] offset:40960\n"
"s_nop 9\n"
"v_mfma_f32_16x16x32_bf16 v[168:171], a[184:187], a[156:159], v[168:171]\n"
"buffer_load_dwordx4 v4, s[32:35], s63 offen offset:2048 nt lds\n"
"v_mul_f32 v160, s64, v160\n"
"v_mul_f32 v161, s64, v161\n"
"v_mul_f32 v162, s64, v162\n"
"v_mul_f32 v163, s64, v163\n"
"v_cvt_pk_bf16_f32 v70, v160, v161\n"
"v_cvt_pk_bf16_f32 v71, v162, v163\n"
"s_nop 5\n"
"v_mfma_f32_16x16x32_bf16 v[168:171], a[232:235], a[204:207], v[168:171]\n"
"ds_write_b64 v51, v[70:71] offset:40960\n"
"s_nop 9\n"
"v_mfma_f32_16x16x32_bf16 v[172:175], a[188:191], a[156:159], v[172:175]\n"
"buffer_load_dwordx4 v5, s[32:35], s63 offen offset:3072 nt lds\n"
"v_mul_f32 v164, s64, v164\n"
"v_mul_f32 v165, s64, v165\n"
"v_mul_f32 v166, s64, v166\n"
"v_mul_f32 v167, s64, v167\n"
"v_cvt_pk_bf16_f32 v72, v164, v165\n"
"v_cvt_pk_bf16_f32 v73, v166, v167\n"
"s_nop 5\n"
"v_mfma_f32_16x16x32_bf16 v[172:175], a[236:239], a[204:207], v[172:175]\n"
"s_add_u32 m0, s53, 20480\n"
"ds_write_b64 v49, v[72:73] offset:47104\n"
"s_nop 9\n"
"v_mfma_f32_16x16x32_bf16 v[176:179], a[180:183], a[160:163], v[176:179]\n"
"s_waitcnt lgkmcnt(15)\n"
"s_barrier\n"
"buffer_load_dwordx4 v6, s[32:35], s63 offen nt lds\n"
"v_mul_f32 v168, s64, v168\n"
"v_mul_f32 v169, s64, v169\n"
"v_mul_f32 v170, s64, v170\n"
"v_mul_f32 v171, s64, v171\n"
"v_cvt_pk_bf16_f32 v74, v168, v169\n"
"v_cvt_pk_bf16_f32 v75, v170, v171\n"
"s_nop 4\n"
"v_mfma_f32_16x16x32_bf16 v[176:179], a[228:231], a[208:211], v[176:179]\n"
"ds_write_b64 v50, v[74:75] offset:47104\n"
"s_nop 9\n"
".Lpad9_%=:\n"
"s_cbranch_execz .Lwb_%=\n"
"v_mfma_f32_16x16x32_bf16 v[180:183], a[184:187], a[160:163], v[180:183]\n"
"ds_read_b128 a[192:195], v1 offset:12288\n"
"buffer_load_dwordx4 v7, s[32:35], s63 offen offset:1024 nt lds\n"
"v_mul_f32 v172, s64, v172\n"
"v_mul_f32 v173, s64, v173\n"
"v_mul_f32 v174, s64, v174\n"
"v_mul_f32 v175, s64, v175\n"
"v_cvt_pk_bf16_f32 v76, v172, v173\n"
"v_cvt_pk_bf16_f32 v77, v174, v175\n"
"s_nop 2\n"
"v_mfma_f32_16x16x32_bf16 v[180:183], a[232:235], a[208:211], v[180:183]\n"
"s_add_u32 m0, s53, 22528\n"
"ds_write_b64 v51, v[76:77] offset:47104\n"
"s_mov_b32 s63, 73728\n"
"s_nop 8\n"
"v_mfma_f32_16x16x32_bf16 v[184:187], a[188:191], a[160:163], v[184:187]\n"
"ds_read_b128 a[196:199], v1 offset:13312\n"
".Lwalk2_%=:\n"
"s_mov_b64 exec, 0\n"
"s_mov_b32 s98, .Lpad10_%=-.Lentry_%=\n"
"s_cmp_eq_u32 s52, 1\n"
"s_cselect_b32 s98, .Lpad11_%=-.Lentry_%=, s98\n"
"s_cmp_eq_u32 s52, 2\n"
"s_cselect_b32 s98, .Lpad12_%=-.Lentry_%=, s98\n"
"s_cmp_eq_u32 s52, 3\n"
"s_cselect_b32 s98, .Lpad13_%=-.Lentry_%=, s98\n"
"s_getpc_b64 s[96:97]\n"
".Lwh20_%=:\n"
"s_sub_u32 s96, s96, .Lwh20_%=-.Lentry_%=\n"
"s_subb_u32 s97, s97, 0\n"
"s_add_u32 s96, s96, s98\n"
"s_addc_u32 s97, s97, 0\n"
"s_swappc_b64 vcc, s[96:97]\n"
"s_mov_b32 s98, .Lpad14_%=-.Lentry_%=\n"
"s_cmp_eq_u32 s52, 1\n"
"s_cselect_b32 s98, .Lpad15_%=-.Lentry_%=, s98\n"
"s_cmp_eq_u32 s52, 2\n"
"s_cselect_b32 s98, .Lpad16_%=-.Lentry_%=, s98\n"
"s_cmp_eq_u32 s52, 3\n"
"s_cselect_b32 s98, .Lpad16_%=-.Lentry_%=, s98\n"
"s_getpc_b64 s[96:97]\n"
".Lwh21_%=:\n"
"s_sub_u32 s96, s96, .Lwh21_%=-.Lentry_%=\n"
"s_subb_u32 s97, s97, 0\n"
"s_add_u32 s96, s96, s98\n"
"s_addc_u32 s97, s97, 0\n"
"s_swappc_b64 vcc, s[96:97]\n"
"s_mov_b64 exec, -1\n"
"v_mul_f32 v176, s64, v176\n"
"v_mul_f32 v177, s64, v177\n"
"v_mul_f32 v178, s64, v178\n"
"v_mul_f32 v179, s64, v179\n"
"v_cvt_pk_bf16_f32 v78, v176, v177\n"
"v_cvt_pk_bf16_f32 v79, v178, v179\n"
"s_nop 6\n"
"v_mfma_f32_16x16x32_bf16 v[184:187], a[236:239], a[208:211], v[184:187]\n"
"buffer_load_dwordx4 v2, s[32:35], s63 offen nt lds\n"
"s_nop 10\n"
"v_mfma_f32_16x16x32_bf16 v[188:191], a[180:183], a[164:167], v[188:191]\n"
"ds_read_b128 a[200:203], v1 offset:53248\n"
"ds_write_b64 v52, v[78:79] offset:0\n"
"v_mul_f32 v180, s64, v180\n"
"v_mul_f32 v181, s64, v181\n"
"v_mul_f32 v182, s64, v182\n"
"v_mul_f32 v183, s64, v183\n"
"v_cvt_pk_bf16_f32 v64, v180, v181\n"
"v_cvt_pk_bf16_f32 v65, v182, v183\n"
"s_nop 1\n"
"v_mfma_f32_16x16x32_bf16 v[188:191], a[228:231], a[212:215], v[188:191]\n"
"buffer_load_dwordx4 v3, s[32:35], s63 offen offset:1024 nt lds\n"
"s_nop 10\n"
"v_mfma_f32_16x16x32_bf16 v[192:195], a[184:187], a[164:167], v[192:195]\n"
"ds_read_b128 a[204:207], v1 offset:54272\n"
"ds_write_b64 v53, v[64:65] offset:0\n"
"v_mul_f32 v184, s64, v184\n"
"v_mul_f32 v185, s64, v185\n"
"v_mul_f32 v186, s64, v186\n"
"v_mul_f32 v187, s64, v187\n"
"v_cvt_pk_bf16_f32 v66, v184, v185\n"
"v_cvt_pk_bf16_f32 v67, v186, v187\n"
"s_nop 1\n"
"v_mfma_f32_16x16x32_bf16 v[192:195], a[232:235], a[212:215], v[192:195]\n"
"buffer_load_dwordx4 v4, s[32:35], s63 offen offset:2048 nt lds\n"
"s_nop 10\n"
"v_mfma_f32_16x16x32_bf16 v[196:199], a[188:191], a[164:167], v[196:199]\n"
"ds_read_b128 a[208:211], v25 offset:12288\n"
"ds_write_b64 v54, v[66:67] offset:0\n"
"v_mul_f32 v188, s64, v188\n"
"v_mul_f32 v189, s64, v189\n"
"v_mul_f32 v190, s64, v190\n"
"v_mul_f32 v191, s64, v191\n"
"v_cvt_pk_bf16_f32 v68, v188, v189\n"
"v_cvt_pk_bf16_f32 v69, v190, v191\n"
"s_nop 1\n"
"v_mfma_f32_16x16x32_bf16 v[196:199], a[236:239], a[212:215], v[196:199]\n"
"buffer_load_dwordx4 v5, s[32:35], s63 offen offset:3072 nt lds\n"
"s_add_u32 m0, s53, 26624\n"
"s_nop 10\n"
"v_mfma_f32_16x16x32_bf16 v[200:203], a[180:183], a[168:171], v[200:203]\n"
"ds_read_b128 a[212:215], v25 offset:13312\n"
"ds_write_b64 v52, v[68:69] offset:6144\n"
"v_mul_f32 v192, s64, v192\n"
"v_mul_f32 v193, s64, v193\n"
"v_mul_f32 v194, s64, v194\n"
"v_mul_f32 v195, s64, v195\n"
"v_cvt_pk_bf16_f32 v70, v192, v193\n"
"v_cvt_pk_bf16_f32 v71, v194, v195\n"
"s_nop 1\n"
"v_mfma_f32_16x16x32_bf16 v[200:203], a[228:231], a[216:219], v[200:203]\n"
"buffer_load_dwordx4 v6, s[32:35], s63 offen nt lds\n"
"s_nop 10\n"
"v_mfma_f32_16x16x32_bf16 v[204:207], a[184:187], a[168:171], v[204:207]\n"
"ds_write_b64 v53, v[70:71] offset:6144\n"
"v_mul_f32 v196, s64, v196\n"
"v_mul_f32 v197, s64, v197\n"
"v_mul_f32 v198, s64, v198\n"
"v_mul_f32 v199, s64, v199\n"
"v_cvt_pk_bf16_f32 v72, v196, v197\n"
"v_cvt_pk_bf16_f32 v73, v198, v199\n"
"s_nop 3\n"
"v_mfma_f32_16x16x32_bf16 v[204:207], a[232:235], a[216:219], v[204:207]\n"
"ds_write_b64 v54, v[72:73] offset:6144\n"
"s_nop 9\n"
"v_mfma_f32_16x16x32_bf16 v[208:211], a[188:191], a[168:171], v[208:211]\n"
"buffer_load_dwordx4 v7, s[32:35], s63 offen offset:1024 nt lds\n"
"v_mul_f32 v200, s64, v200\n"
"v_mul_f32 v201, s64, v201\n"
"v_mul_f32 v202, s64, v202\n"
"v_mul_f32 v203, s64, v203\n"
"v_cvt_pk_bf16_f32 v74, v200, v201\n"
"v_cvt_pk_bf16_f32 v75, v202, v203\n"
"s_nop 5\n"
"v_mfma_f32_16x16x32_bf16 v[208:211], a[236:239], a[216:219], v[208:211]\n"
"ds_write_b64 v52, v[74:75] offset:40960\n"
"s_nop 9\n"
"v_mfma_f32_16x16x32_bf16 v[212:215], a[180:183], a[172:175], v[212:215]\n"
"v_mul_f32 v204, s64, v204\n"
"v_mul_f32 v205, s64, v205\n"
"v_mul_f32 v206, s64, v206\n"
"v_mul_f32 v207, s64, v207\n"
"v_cvt_pk_bf16_f32 v76, v204, v205\n"
"v_cvt_pk_bf16_f32 v77, v206, v207\n"
"s_nop 6\n"
"v_mfma_f32_16x16x32_bf16 v[212:215], a[228:231], a[220:223], v[212:215]\n"
"ds_write_b64 v53, v[76:77] offset:40960\n"
"s_nop 9\n"
"v_mfma_f32_16x16x32_bf16 v[216:219], a[184:187], a[172:175], v[216:219]\n"
"v_mul_f32 v208, s64, v208\n"
"v_mul_f32 v209, s64, v209\n"
"v_mul_f32 v210, s64, v210\n"
"v_mul_f32 v211, s64, v211\n"
"v_cvt_pk_bf16_f32 v78, v208, v209\n"
"v_cvt_pk_bf16_f32 v79, v210, v211\n"
"s_nop 7\n"
"v_mfma_f32_16x16x32_bf16 v[216:219], a[232:235], a[220:223], v[216:219]\n"
"ds_write_b64 v54, v[78:79] offset:40960\n"
"s_nop 9\n"
"v_mfma_f32_16x16x32_bf16 v[220:223], a[188:191], a[172:175], v[220:223]\n"
"v_mul_f32 v212, s64, v212\n"
"v_mul_f32 v213, s64, v213\n"
"v_mul_f32 v214, s64, v214\n"
"v_mul_f32 v215, s64, v215\n"
"v_cvt_pk_bf16_f32 v64, v212, v213\n"
"v_cvt_pk_bf16_f32 v65, v214, v215\n"
"v_mfma_f32_16x16x32_bf16 v[220:223], a[236:239], a[220:223], v[220:223]\n"
"s_waitcnt lgkmcnt(3)\n"
"ds_write_b64 v52, v[64:65] offset:47104\n"
"v_mfma_f32_16x16x32_bf16 v[80:83], a[48:51], a[192:195], 0\n"
"v_mul_f32 v216, s64, v216\n"
"v_mul_f32 v217, s64, v217\n"
"v_mul_f32 v218, s64, v218\n"
"v_mul_f32 v219, s64, v219\n"
"v_cvt_pk_bf16_f32 v66, v216, v217\n"
"v_cvt_pk_bf16_f32 v67, v218, v219\n"
"v_mfma_f32_16x16x32_bf16 v[80:83], a[52:55], a[196:199], v[80:83]\n"
"ds_write_b64 v53, v[66:67] offset:47104\n"
"v_mfma_f32_16x16x32_bf16 v[80:83], a[56:59], a[200:203], v[80:83]\n"
"v_mul_f32 v220, s64, v220\n"
"v_mul_f32 v221, s64, v221\n"
"v_mul_f32 v222, s64, v222\n"
"v_mul_f32 v223, s64, v223\n"
"v_cvt_pk_bf16_f32 v68, v220, v221\n"
"v_mfma_f32_16x16x32_bf16 v[80:83], a[60:63], a[204:207], v[80:83]\n"
"v_cvt_pk_bf16_f32 v69, v222, v223\n"
"v_mfma_f32_16x16x32_bf16 v[80:83], a[64:67], a[208:211], v[80:83]\n"
"ds_write_b64 v54, v[68:69] offset:47104\n"
"v_mfma_f32_16x16x32_bf16 v[80:83], a[68:71], a[212:215], v[80:83]\n"
"s_waitcnt vmcnt(12)\n"
"s_waitcnt lgkmcnt(0)\n"
".Ljoin_%=:\n"
"s_add_u32 s58, s58, 1\n"
"s_barrier\n"
"ds_read_b128 a[144:147], v26 offset:0\n"
"ds_read_b128 a[148:151], v26 offset:1024\n"
"v_mfma_f32_16x16x32_bf16 v[40:43], a[72:75], a[192:195], 0\n"
"ds_read_b128 a[152:155], v26 offset:2048\n"
"v_mfma_f32_16x16x32_bf16 v[40:43], a[76:79], a[196:199], v[40:43]\n"
"ds_read_b128 a[156:159], v26 offset:3072\n"
"v_mov_b32 v44, 0x3f803f80\n"
"v_mfma_f32_16x16x32_bf16 v[40:43], a[80:83], a[200:203], v[40:43]\n"
"ds_read_b128 a[160:163], v26 offset:4096\n"
"v_mov_b32 v45, 0x3f803f80\n"
"v_mfma_f32_16x16x32_bf16 v[40:43], a[84:87], a[204:207], v[40:43]\n"
"ds_read_b128 a[164:167], v26 offset:5120\n"
"ds_read_b128 a[168:171], v26 offset:6144\n"
"v_mfma_f32_16x16x32_bf16 v[40:43], a[88:91], a[208:211], v[40:43]\n"
"ds_read_b128 a[172:175], v26 offset:7168\n"
"v_mov_b32 v46, 0x3f803f80\n"
"v_mfma_f32_16x16x32_bf16 v[40:43], a[92:95], a[212:215], v[40:43]\n"
"s_waitcnt lgkmcnt(7)\n"
"ds_read_b128 a[176:179], v26 offset:8192\n"
"v_mov_b32 v47, 0x3f803f80\n"
"v_mfma_f32_16x16x32_bf16 v[48:51], a[72:75], a[144:147], 0\n"
"s_waitcnt lgkmcnt(7)\n"
"ds_read_b128 a[180:183], v26 offset:9216\n"
"ds_read_b128 a[184:187], v26 offset:10240\n"
"v_mfma_f32_16x16x32_bf16 v[48:51], a[76:79], a[148:151], v[48:51]\n"
"s_waitcnt lgkmcnt(8)\n"
"ds_read_b128 a[188:191], v26 offset:11264\n"
"v_mfma_f32_16x16x32_bf16 v[48:51], a[80:83], a[152:155], v[48:51]\n"
"ds_read_b128 a[216:219], v14 offset:28672\n"
"s_cmp_lg_u32 s65, 0\n"
"s_cbranch_scc1 .Lmx_%=\n"
".Lg2_%=:\n"
"s_waitcnt lgkmcnt(9)\n"
"v_mfma_f32_16x16x32_bf16 v[48:51], a[84:87], a[156:159], v[48:51]\n"
"s_waitcnt lgkmcnt(8)\n"
"ds_read_b128 a[220:223], v15 offset:28672\n"
"ds_read_b128 a[224:227], v14 offset:28800\n"
"v_max3_f32 v27, v80, v81, v82\n"
"v_max3_f32 v27, v27, v83, v24\n"
"v_mov_b32 v126, v27\n"
"s_nop 1\n"
"v_permlane16_swap_b32 v27, v126\n"
"v_max_f32 v27, v27, v126\n"
"v_mov_b32 v126, v27\n"
"s_nop 1\n"
"v_permlane32_swap_b32 v27, v126\n"
"v_mfma_f32_16x16x32_bf16 v[48:51], a[88:91], a[160:163], v[48:51]\n"
"s_waitcnt lgkmcnt(8)\n"
"ds_read_b128 a[228:231], v15 offset:28800\n"
"v_max_f32 v27, v27, v126\n"
"v_mfma_f32_16x16x32_bf16 v[48:51], a[92:95], a[164:167], v[48:51]\n"
"ds_read_b128 a[232:235], v14 offset:28928\n"
"v_sub_f32 v36, 0, v27\n"
"v_mfma_f32_16x16x32_bf16 v[52:55], a[72:75], a[168:171], 0\n"
"s_waitcnt lgkmcnt(8)\n"
"ds_read_b128 a[236:239], v15 offset:28928\n"
"v_sub_f32 v37, 0, v27\n"
"v_mfma_f32_16x16x32_bf16 v[52:55], a[76:79], a[172:175], v[52:55]\n"
"ds_read_b128 a[48:51], v16 offset:28672\n"
"v_sub_f32 v38, 0, v27\n"
"v_mfma_f32_16x16x32_bf16 v[52:55], a[80:83], a[176:179], v[52:55]\n"
"s_waitcnt lgkmcnt(7)\n"
"ds_read_b128 a[52:55], v17 offset:28672\n"
"v_sub_f32 v39, 0, v27\n"
"v_mfma_f32_16x16x32_bf16 v[52:55], a[84:87], a[180:183], v[52:55]\n"
"v_max3_f32 v27, v48, v49, v50\n"
"v_max3_f32 v27, v27, v51, v22\n"
"v_mov_b32 v126, v27\n"
"s_nop 1\n"
"v_permlane16_swap_b32 v27, v126\n"
"v_max_f32 v27, v27, v126\n"
"v_mov_b32 v126, v27\n"
"s_nop 1\n"
"v_permlane32_swap_b32 v27, v126\n"
"v_mfma_f32_16x16x32_bf16 v[52:55], a[88:91], a[184:187], v[52:55]\n"
"ds_read_b128 a[56:59], v16 offset:28800\n"
"v_max_f32 v27, v27, v126\n"
"v_mfma_f32_16x16x32_bf16 v[52:55], a[92:95], a[188:191], v[52:55]\n"
"s_waitcnt lgkmcnt(5)\n"
"ds_read_b128 a[60:63], v17 offset:28800\n"
"v_sub_f32 v28, 0, v27\n"
"v_mfma_f32_16x16x32_bf16 v[56:59], a[216:219], a[144:147], 0\n"
"ds_read_b128 a[64:67], v16 offset:28928\n"
"v_sub_f32 v29, 0, v27\n"
"v_mfma_f32_16x16x32_bf16 v[56:59], a[220:223], a[148:151], v[56:59]\n"
"ds_read_b128 a[68:71], v17 offset:28928\n"
"v_sub_f32 v30, 0, v27\n"
"v_mfma_f32_16x16x32_bf16 v[56:59], a[224:227], a[152:155], v[56:59]\n"
"ds_read_b128 a[72:75], v18 offset:28672\n"
"v_sub_f32 v31, 0, v27\n"
"v_mfma_f32_16x16x32_bf16 v[56:59], a[228:231], a[156:159], v[56:59]\n"
"s_waitcnt lgkmcnt(5)\n"
"v_max3_f32 v27, v52, v53, v54\n"
"v_max3_f32 v27, v27, v55, v23\n"
"v_mov_b32 v126, v27\n"
"s_nop 1\n"
"v_permlane16_swap_b32 v27, v126\n"
"v_max_f32 v27, v27, v126\n"
"v_mov_b32 v126, v27\n"
"s_nop 1\n"
"v_permlane32_swap_b32 v27, v126\n"
"v_max_f32 v27, v27, v126\n"
"v_sub_f32 v32, 0, v27\n"
"v_sub_f32 v33, 0, v27\n"
"v_sub_f32 v34, 0, v27\n"
"v_sub_f32 v35, 0, v27\n"
"v_add_f32 v40, v40, v36\n"
"v_add_f32 v41, v41, v36\n"
"v_add_f32 v42, v42, v36\n"
"v_add_f32 v43, v43, v36\n"
"v_exp_f32 v40, v40\n"
"v_exp_f32 v41, v41\n"
"v_exp_f32 v42, v42\n"
"v_exp_f32 v43, v43\n"
"v_cvt_pk_bf16_f32 v60, v40, v41\n"
"v_cvt_pk_bf16_f32 v61, v42, v43\n"
"v_add_f32 v48, v48, v28\n"
"v_add_f32 v49, v49, v28\n"
"v_add_f32 v50, v50, v28\n"
"v_add_f32 v51, v51, v28\n"
"v_exp_f32 v48, v48\n"
"v_exp_f32 v49, v49\n"
"v_exp_f32 v50, v50\n"
"v_mfma_f32_16x16x32_bf16 v[56:59], a[232:235], a[160:163], v[56:59]\n"
"v_exp_f32 v51, v51\n"
"v_mfma_f32_16x16x32_bf16 v[56:59], a[236:239], a[164:167], v[56:59]\n"
"ds_read_b128 a[76:79], v19 offset:28672\n"
"v_cvt_pk_bf16_f32 v128, v48, v49\n"
"v_mfma_f32_16x16x32_bf16 v[64:67], a[216:219], a[168:171], 0\n"
"ds_read_b128 a[80:83], v18 offset:28800\n"
"v_cvt_pk_bf16_f32 v129, v50, v51\n"
"v_mfma_f32_16x16x32_bf16 v[64:67], a[220:223], a[172:175], v[64:67]\n"
"ds_read_b128 a[84:87], v19 offset:28800\n"
"v_add_f32 v52, v52, v32\n"
"v_mfma_f32_16x16x32_bf16 v[64:67], a[224:227], a[176:179], v[64:67]\n"
"ds_read_b128 a[88:91], v18 offset:28928\n"
"v_add_f32 v53, v53, v32\n"
"v_mfma_f32_16x16x32_bf16 v[64:67], a[228:231], a[180:183], v[64:67]\n"
"s_mov_b32 s63, 0\n"
"ds_read_b128 a[92:95], v19 offset:28928\n"
"v_add_f32 v54, v54, v32\n"
"v_mfma_f32_16x16x32_bf16 v[64:67], a[232:235], a[184:187], v[64:67]\n"
"s_add_u32 m0, s53, 12288\n"
"v_add_f32 v55, v55, v32\n"
"v_accvgpr_read_b32 v2, a122\n"
"v_mfma_f32_16x16x32_bf16 v[64:67], a[236:239], a[188:191], v[64:67]\n"
"v_exp_f32 v52, v52\n"
"v_mfma_f32_16x16x32_bf16 v[68:71], a[48:51], a[144:147], v[28:31]\n"
"v_exp_f32 v53, v53\n"
"v_mfma_f32_16x16x32_bf16 v[68:71], a[52:55], a[148:151], v[68:71]\n"
"s_waitcnt lgkmcnt(3)\n"
"v_exp_f32 v54, v54\n"
"v_mfma_f32_16x16x32_bf16 v[68:71], a[56:59], a[152:155], v[68:71]\n"
"v_exp_f32 v55, v55\n"
"v_mfma_f32_16x16x32_bf16 v[68:71], a[60:63], a[156:159], v[68:71]\n"
"v_cvt_pk_bf16_f32 v132, v52, v53\n"
"v_accvgpr_read_b32 v3, a123\n"
"v_mfma_f32_16x16x32_bf16 v[68:71], a[64:67], a[160:163], v[68:71]\n"
"s_waitcnt lgkmcnt(0)\n"
"s_barrier\n"
"v_cvt_pk_bf16_f32 v133, v54, v55\n"
"v_mfma_f32_16x16x32_bf16 v[68:71], a[68:71], a[164:167], v[68:71]\n"
"v_add_f32 v56, v56, v28\n"
"v_accvgpr_read_b32 v4, a124\n"
"v_mfma_f32_16x16x32_bf16 v[72:75], a[48:51], a[168:171], v[32:35]\n"
"s_waitcnt vmcnt(4)\n"
"buffer_load_dwordx4 v8, s[36:39], s63 offen nt lds\n"
"v_add_f32 v57, v57, v28\n"
"v_mfma_f32_16x16x32_bf16 v[72:75], a[52:55], a[172:175], v[72:75]\n"
"v_accvgpr_read_b32 v5, a125\n"
"v_add_f32 v58, v58, v28\n"
"v_mfma_f32_16x16x32_bf16 v[72:75], a[56:59], a[176:179], v[72:75]\n"
"v_accvgpr_read_b32 v6, a126\n"
"v_accvgpr_read_b32 v7, a127\n"
"v_add_f32 v59, v59, v28\n"
"v_mfma_f32_16x16x32_bf16 v[72:75], a[60:63], a[180:183], v[72:75]\n"
"v_exp_f32 v56, v56\n"
"v_mfma_f32_16x16x32_bf16 v[72:75], a[64:67], a[184:187], v[72:75]\n"
"v_exp_f32 v57, v57\n"
"v_mfma_f32_16x16x32_bf16 v[72:75], a[68:71], a[188:191], v[72:75]\n"
"ds_read_b128 a[216:219], v12 offset:34816\n"
"v_exp_f32 v58, v58\n"
"v_mfma_f32_16x16x32_bf16 v[80:83], a[72:75], a[144:147], v[28:31]\n"
"ds_read_b128 a[220:223], v13 offset:34816\n"
"v_exp_f32 v59, v59\n"
"v_mfma_f32_16x16x32_bf16 v[80:83], a[76:79], a[148:151], v[80:83]\n"
"ds_read_b128 a[224:227], v12 offset:34944\n"
"v_cvt_pk_bf16_f32 v136, v56, v57\n"
"v_mfma_f32_16x16x32_bf16 v[80:83], a[80:83], a[152:155], v[80:83]\n"
"ds_read_b128 a[228:231], v13 offset:34944\n"
"ds_write_b64 v26, a[0:1] offset:28672\n"
"v_cvt_pk_bf16_f32 v137, v58, v59\n"
"v_mfma_f32_16x16x32_bf16 v[80:83], a[84:87], a[156:159], v[80:83]\n"
"ds_read_b128 a[232:235], v12 offset:35072\n"
"v_add_f32 v64, v64, v32\n"
"v_mfma_f32_16x16x32_bf16 v[80:83], a[88:91], a[160:163], v[80:83]\n"
"ds_read_b128 a[236:239], v13 offset:35072\n"
"v_add_f32 v65, v65, v32\n"
"v_mfma_f32_16x16x32_bf16 v[80:83], a[92:95], a[164:167], v[80:83]\n"
"s_waitcnt vmcnt(4)\n"
"buffer_load_dwordx4 v9, s[36:39], s63 offen offset:1024 nt lds\n"
"ds_write_b64 v26, a[2:3] offset:28680\n"
"v_add_f32 v66, v66, v32\n"
"v_mfma_f32_16x16x32_bf16 v[40:43], a[72:75], a[168:171], v[32:35]\n"
"ds_read_b128 a[48:51], v14 offset:34816\n"
"v_add_f32 v67, v67, v32\n"
"v_mfma_f32_16x16x32_bf16 v[40:43], a[76:79], a[172:175], v[40:43]\n"
"v_exp_f32 v64, v64\n"
"v_mfma_f32_16x16x32_bf16 v[40:43], a[80:83], a[176:179], v[40:43]\n"
"v_exp_f32 v65, v65\n"
"v_mfma_f32_16x16x32_bf16 v[40:43], a[84:87], a[180:183], v[40:43]\n"
"ds_write_b64 v26, a[4:5] offset:29696\n"
"v_exp_f32 v66, v66\n"
"v_mfma_f32_16x16x32_bf16 v[40:43], a[88:91], a[184:187], v[40:43]\n"
"v_exp_f32 v67, v67\n"
"v_mfma_f32_16x16x32_bf16 v[40:43], a[92:95], a[188:191], v[40:43]\n"
"s_waitcnt lgkmcnt(3)\n"
"ds_read_b128 a[52:55], v15 offset:34816\n"
"v_cvt_pk_bf16_f32 v140, v64, v65\n"
"v_mfma_f32_16x16x32_bf16 v[48:51], a[216:219], a[144:147], v[28:31]\n"
"ds_write_b64 v26, a[6:7] offset:29704\n"
"v_cvt_pk_bf16_f32 v141, v66, v67\n"
"v_mfma_f32_16x16x32_bf16 v[48:51], a[220:223], a[148:151], v[48:51]\n"
"v_exp_f32 v68, v68\n"
"v_mfma_f32_16x16x32_bf16 v[48:51], a[224:227], a[152:155], v[48:51]\n"
"v_exp_f32 v69, v69\n"
"v_mfma_f32_16x16x32_bf16 v[48:51], a[228:231], a[156:159], v[48:51]\n"
"v_exp_f32 v70, v70\n"
"v_mfma_f32_16x16x32_bf16 v[48:51], a[232:235], a[160:163], v[48:51]\n"
"s_waitcnt vmcnt(4)\n"
"buffer_load_dwordx4 v10, s[36:39], s63 offen offset:2048 nt lds\n"
"ds_write_b64 v26, a[8:9] offset:30720\n"
"v_exp_f32 v71, v71\n"
"v_mfma_f32_16x16x32_bf16 v[48:51], a[236:239], a[164:167], v[48:51]\n"
"ds_read_b128 a[56:59], v14 offset:34944\n"
"v_cvt_pk_bf16_f32 v144, v68, v69\n"
"v_mfma_f32_16x16x32_bf16 v[52:55], a[216:219], a[168:171], v[32:35]\n"
"ds_read_b128 a[60:63], v15 offset:34944\n"
"v_cvt_pk_bf16_f32 v145, v70, v71\n"
"v_mfma_f32_16x16x32_bf16 v[52:55], a[220:223], a[172:175], v[52:55]\n"
"v_exp_f32 v72, v72\n"
"v_mfma_f32_16x16x32_bf16 v[52:55], a[224:227], a[176:179], v[52:55]\n"
"ds_write_b64 v26, a[10:11] offset:30728\n"
"v_exp_f32 v73, v73\n"
"v_mfma_f32_16x16x32_bf16 v[52:55], a[228:231], a[180:183], v[52:55]\n"
"ds_read_b128 a[64:67], v14 offset:35072\n"
"v_exp_f32 v74, v74\n"
"v_mfma_f32_16x16x32_bf16 v[52:55], a[232:235], a[184:187], v[52:55]\n"
"ds_read_b128 a[68:71], v15 offset:35072\n"
"v_exp_f32 v75, v75\n"
"v_mfma_f32_16x16x32_bf16 v[52:55], a[236:239], a[188:191], v[52:55]\n"
"ds_write_b64 v26, a[12:13] offset:31744\n"
"v_cvt_pk_bf16_f32 v148, v72, v73\n"
"v_mfma_f32_16x16x32_bf16 v[56:59], a[216:219], a[192:195], v[36:39]\n"
"ds_read_b128 a[72:75], v16 offset:34816\n"
"v_cvt_pk_bf16_f32 v149, v74, v75\n"
"v_mfma_f32_16x16x32_bf16 v[56:59], a[220:223], a[196:199], v[56:59]\n"
"v_exp_f32 v80, v80\n"
"v_mfma_f32_16x16x32_bf16 v[56:59], a[224:227], a[200:203], v[56:59]\n"
"v_exp_f32 v81, v81\n"
"v_mfma_f32_16x16x32_bf16 v[56:59], a[228:231], a[204:207], v[56:59]\n"
"s_waitcnt vmcnt(4)\n"
"buffer_load_dwordx4 v11, s[36:39], s63 offen offset:3072 nt lds\n"
"ds_write_b64 v26, a[14:15] offset:31752\n"
"v_exp_f32 v82, v82\n"
"v_mfma_f32_16x16x32_bf16 v[56:59], a[232:235], a[208:211], v[56:59]\n"
"v_exp_f32 v83, v83\n"
"v_mfma_f32_16x16x32_bf16 v[56:59], a[236:239], a[212:215], v[56:59]\n"
"s_waitcnt lgkmcnt(3)\n"
"ds_read_b128 a[76:79], v17 offset:34816\n"
"v_cvt_pk_bf16_f32 v152, v80, v81\n"
"v_mfma_f32_16x16x32_bf16 v[64:67], a[48:51], a[144:147], v[28:31]\n"
"ds_read_b128 a[80:83], v16 offset:34944\n"
"ds_write_b64 v26, a[16:17] offset:32768\n"
"v_cvt_pk_bf16_f32 v153, v82, v83\n"
"v_mfma_f32_16x16x32_bf16 v[64:67], a[52:55], a[148:151], v[64:67]\n"
"ds_read_b128 a[84:87], v17 offset:34944\n"
"v_exp_f32 v40, v40\n"
"v_mfma_f32_16x16x32_bf16 v[64:67], a[56:59], a[152:155], v[64:67]\n"
"ds_read_b128 a[88:91], v16 offset:35072\n"
"v_exp_f32 v41, v41\n"
"v_mfma_f32_16x16x32_bf16 v[64:67], a[60:63], a[156:159], v[64:67]\n"
"ds_read_b128 a[92:95], v17 offset:35072\n"
"v_exp_f32 v42, v42\n"
"v_mfma_f32_16x16x32_bf16 v[64:67], a[64:67], a[160:163], v[64:67]\n"
"ds_read_b128 a[216:219], v18 offset:34816\n"
"ds_write_b64 v26, a[18:19] offset:32776\n"
"v_exp_f32 v43, v43\n"
"v_mfma_f32_16x16x32_bf16 v[64:67], a[68:71], a[164:167], v[64:67]\n"
"ds_read_b128 a[220:223], v19 offset:34816\n"
"v_cvt_pk_bf16_f32 v156, v40, v41\n"
"v_mfma_f32_16x16x32_bf16 v[68:71], a[48:51], a[168:171], v[32:35]\n"
"ds_read_b128 a[224:227], v18 offset:34944\n"
"v_cvt_pk_bf16_f32 v157, v42, v43\n"
"v_min_u32 v125, 19, v0\n"
"v_lshlrev_b32 v125, 7, v125\n"
"s_add_u32 s13, s12, 163840\n"
"v_mfma_f32_16x16x32_bf16 v[68:71], a[52:55], a[172:175], v[68:71]\n"
"ds_read_b128 a[228:231], v19 offset:34944\n"
"v_exp_f32 v48, v48\n"
"v_mfma_f32_16x16x32_bf16 v[68:71], a[56:59], a[176:179], v[68:71]\n"
"ds_read_b128 a[232:235], v18 offset:35072\n"
"ds_write_b64 v26, a[20:21] offset:33792\n"
"buffer_load_dword v125, v125, s[40:43], s13 offen\n"
"v_exp_f32 v49, v49\n"
"v_mfma_f32_16x16x32_bf16 v[68:71], a[60:63], a[180:183], v[68:71]\n"
"ds_read_b128 a[236:239], v19 offset:35072\n"
"ds_write_b64 v26, a[22:23] offset:33800\n"
"v_exp_f32 v50, v50\n"
"v_mfma_f32_16x16x32_bf16 v[68:71], a[64:67], a[184:187], v[68:71]\n"
"v_exp_f32 v51, v51\n"
"v_mfma_f32_16x16x32_bf16 v[68:71], a[68:71], a[188:191], v[68:71]\n"
"s_waitcnt lgkmcnt(7)\n"
"v_cvt_pk_bf16_f32 v130, v48, v49\n"
"v_cvt_pk_bf16_f32 v131, v50, v51\n"
"v_mfma_f32_16x16x32_bf16 v[72:75], a[72:75], a[144:147], v[28:31]\n"
"v_exp_f32 v52, v52\n"
"v_mfma_f32_16x16x32_bf16 v[72:75], a[76:79], a[148:151], v[72:75]\n"
"v_exp_f32 v53, v53\n"
"v_mfma_f32_16x16x32_bf16 v[72:75], a[80:83], a[152:155], v[72:75]\n"
"v_exp_f32 v54, v54\n"
"v_mfma_f32_16x16x32_bf16 v[72:75], a[84:87], a[156:159], v[72:75]\n"
"v_exp_f32 v55, v55\n"
"v_mfma_f32_16x16x32_bf16 v[72:75], a[88:91], a[160:163], v[72:75]\n"
"s_waitcnt lgkmcnt(0)\n"
"s_barrier\n"
"v_cvt_pk_bf16_f32 v134, v52, v53\n"
"v_mfma_f32_16x16x32_bf16 v[72:75], a[92:95], a[164:167], v[72:75]\n"
"ds_read_b128 a[48:51], v12 offset:16384\n"
"v_cvt_pk_bf16_f32 v135, v54, v55\n"
"v_mfma_f32_16x16x32_bf16 v[80:83], a[72:75], a[168:171], v[32:35]\n"
"v_exp_f32 v56, v56\n"
"v_mfma_f32_16x16x32_bf16 v[80:83], a[76:79], a[172:175], v[80:83]\n"
"v_exp_f32 v57, v57\n"
"v_mfma_f32_16x16x32_bf16 v[80:83], a[80:83], a[176:179], v[80:83]\n"
"v_exp_f32 v58, v58\n"
"v_mfma_f32_16x16x32_bf16 v[80:83], a[84:87], a[180:183], v[80:83]\n"
"v_exp_f32 v59, v59\n"
"v_mfma_f32_16x16x32_bf16 v[80:83], a[88:91], a[184:187], v[80:83]\n"
"ds_read_b128 a[52:55], v13 offset:16384\n"
"v_cvt_pk_bf16_f32 v62, v56, v57\n"
"v_mfma_f32_16x16x32_bf16 v[80:83], a[92:95], a[188:191], v[80:83]\n"
"s_mov_b32 s63, 49152\n"
"ds_read_b128 a[56:59], v12 offset:16512\n"
"v_cvt_pk_bf16_f32 v63, v58, v59\n"
"v_mfma_f32_16x16x32_bf16 v[84:87], v[44:47], v[128:131], 0\n"
"s_add_u32 m0, s53, 0\n"
"v_exp_f32 v64, v64\n"
"v_mfma_f32_16x16x32_bf16 v[88:91], v[44:47], v[132:135], 0\n"
"v_exp_f32 v65, v65\n"
"v_mfma_f32_16x16x32_bf16 v[92:95], v[44:47], v[60:63], 0\n"
"ds_write_b64 v26, a[24:25] offset:34816\n"
"v_mfma_f32_16x16x32_bf16 v[40:43], a[216:219], a[144:147], v[28:31]\n"
"v_exp_f32 v66, v66\n"
"v_mfma_f32_16x16x32_bf16 v[40:43], a[220:223], a[148:151], v[40:43]\n"
"v_exp_f32 v67, v67\n"
"v_mfma_f32_16x16x32_bf16 v[40:43], a[224:227], a[152:155], v[40:43]\n"
"ds_write_b64 v26, a[26:27] offset:34824\n"
"v_mfma_f32_16x16x32_bf16 v[40:43], a[228:231], a[156:159], v[40:43]\n"
"ds_read_b128 a[60:63], v13 offset:16512\n"
"v_cvt_pk_bf16_f32 v138, v64, v65\n"
"v_mfma_f32_16x16x32_bf16 v[40:43], a[232:235], a[160:163], v[40:43]\n"
"ds_read_b128 a[64:67], v12 offset:16640\n"
"v_cvt_pk_bf16_f32 v139, v66, v67\n"
"v_mfma_f32_16x16x32_bf16 v[40:43], a[236:239], a[164:167], v[40:43]\n"
"s_waitcnt vmcnt(5)\n"
"buffer_load_dwordx4 v8, s[36:39], s63 offen nt lds\n"
"v_mfma_f32_16x16x32_bf16 v[48:51], a[216:219], a[168:171], v[32:35]\n"
"ds_read_b128 a[68:71], v13 offset:16640\n"
"ds_write_b64 v26, a[28:29] offset:35840\n"
"v_exp_f32 v68, v68\n"
"v_mfma_f32_16x16x32_bf16 v[48:51], a[220:223], a[172:175], v[48:51]\n"
"v_exp_f32 v69, v69\n"
"v_mfma_f32_16x16x32_bf16 v[48:51], a[224:227], a[176:179], v[48:51]\n"
"v_exp_f32 v70, v70\n"
"v_mfma_f32_16x16x32_bf16 v[48:51], a[228:231], a[180:183], v[48:51]\n"
"ds_write_b64 v26, a[30:31] offset:35848\n"
"v_exp_f32 v71, v71\n"
"v_mfma_f32_16x16x32_bf16 v[48:51], a[232:235], a[184:187], v[48:51]\n"
"ds_read_b128 a[72:75], v14 offset:16384\n"
"v_cvt_pk_bf16_f32 v142, v68, v69\n"
"v_mfma_f32_16x16x32_bf16 v[48:51], a[236:239], a[188:191], v[48:51]\n"
"ds_read_b128 a[76:79], v15 offset:16384\n"
"v_cvt_pk_bf16_f32 v143, v70, v71\n"
"v_mfma_f32_16x16x32_bf16 v[84:87], v[44:47], v[136:139], v[84:87]\n"
"v_exp_f32 v72, v72\n"
"v_mfma_f32_16x16x32_bf16 v[88:91], v[44:47], v[140:143], v[88:91]\n"
"s_waitcnt lgkmcnt(3)\n"
"ds_write_b64 v26, a[32:33] offset:36864\n"
"v_mfma_f32_16x16x32_bf16 v[52:55], a[48:51], a[144:147], v[28:31]\n"
"v_exp_f32 v73, v73\n"
"v_mfma_f32_16x16x32_bf16 v[52:55], a[52:55], a[148:151], v[52:55]\n"
"v_exp_f32 v74, v74\n"
"v_mfma_f32_16x16x32_bf16 v[52:55], a[56:59], a[152:155], v[52:55]\n"
"s_waitcnt vmcnt(5)\n"
"buffer_load_dwordx4 v9, s[36:39], s63 offen offset:1024 nt lds\n"
"v_mfma_f32_16x16x32_bf16 v[52:55], a[60:63], a[156:159], v[52:55]\n"
"ds_write_b64 v26, a[34:35] offset:36872\n"
"v_mfma_f32_16x16x32_bf16 v[52:55], a[64:67], a[160:163], v[52:55]\n"
"v_exp_f32 v75, v75\n"
"v_mfma_f32_16x16x32_bf16 v[52:55], a[68:71], a[164:167], v[52:55]\n"
"ds_read_b128 a[80:83], v14 offset:16512\n"
"v_cvt_pk_bf16_f32 v146, v72, v73\n"
"v_mfma_f32_16x16x32_bf16 v[56:59], a[48:51], a[168:171], v[32:35]\n"
"ds_write_b64 v26, a[36:37] offset:37888\n"
"v_mfma_f32_16x16x32_bf16 v[56:59], a[52:55], a[172:175], v[56:59]\n"
"ds_read_b128 a[84:87], v15 offset:16512\n"
"v_cvt_pk_bf16_f32 v147, v74, v75\n"
"v_mfma_f32_16x16x32_bf16 v[56:59], a[56:59], a[176:179], v[56:59]\n"
"v_exp_f32 v80, v80\n"
"v_mfma_f32_16x16x32_bf16 v[56:59], a[60:63], a[180:183], v[56:59]\n"
"v_exp_f32 v81, v81\n"
"v_mfma_f32_16x16x32_bf16 v[56:59], a[64:67], a[184:187], v[56:59]\n"
"ds_write_b64 v26, a[38:39] offset:37896\n"
"v_mfma_f32_16x16x32_bf16 v[56:59], a[68:71], a[188:191], v[56:59]\n"
"ds_read_b128 a[88:91], v14 offset:16640\n"
"v_mfma_f32_16x16x32_bf16 v[64:67], a[48:51], a[192:195], v[36:39]\n"
"ds_read_b128 a[92:95], v15 offset:16640\n"
"v_mfma_f32_16x16x32_bf16 v[64:67], a[52:55], a[196:199], v[64:67]\n"
"s_waitcnt vmcnt(5)\n"
"buffer_load_dwordx4 v10, s[36:39], s63 offen offset:2048 nt lds\n"
"ds_write_b64 v26, a[40:41] offset:38912\n"
"v_exp_f32 v82, v82\n"
"v_mfma_f32_16x16x32_bf16 v[64:67], a[56:59], a[200:203], v[64:67]\n"
"v_exp_f32 v83, v83\n"
"v_mfma_f32_16x16x32_bf16 v[64:67], a[60:63], a[204:207], v[64:67]\n"
"ds_read_b128 a[216:219], v16 offset:16384\n"
"v_cvt_pk_bf16_f32 v150, v80, v81\n"
"v_mfma_f32_16x16x32_bf16 v[64:67], a[64:67], a[208:211], v[64:67]\n"
"ds_read_b128 a[220:223], v17 offset:16384\n"
"v_cvt_pk_bf16_f32 v151, v82, v83\n"
"v_mfma_f32_16x16x32_bf16 v[64:67], a[68:71], a[212:215], v[64:67]\n"
"ds_write_b64 v26, a[42:43] offset:38920\n"
"v_exp_f32 v40, v40\n"
"v_mfma_f32_16x16x32_bf16 v[84:87], v[44:47], v[144:147], v[84:87]\n"
"v_exp_f32 v41, v41\n"
"v_mfma_f32_16x16x32_bf16 v[88:91], v[44:47], v[148:151], v[88:91]\n"
"s_waitcnt lgkmcnt(4)\n"
"v_exp_f32 v42, v42\n"
"v_mfma_f32_16x16x32_bf16 v[68:71], a[72:75], a[144:147], v[28:31]\n"
"v_exp_f32 v43, v43\n"
"v_mfma_f32_16x16x32_bf16 v[68:71], a[76:79], a[148:151], v[68:71]\n"
"ds_write_b64 v26, a[44:45] offset:39936\n"
"v_cvt_pk_bf16_f32 v154, v40, v41\n"
"v_mfma_f32_16x16x32_bf16 v[68:71], a[80:83], a[152:155], v[68:71]\n"
"ds_read_b128 a[224:227], v16 offset:16512\n"
"v_cvt_pk_bf16_f32 v155, v42, v43\n"
"v_mfma_f32_16x16x32_bf16 v[68:71], a[84:87], a[156:159], v[68:71]\n"
"ds_read_b128 a[228:231], v17 offset:16512\n"
"v_exp_f32 v48, v48\n"
"v_mfma_f32_16x16x32_bf16 v[68:71], a[88:91], a[160:163], v[68:71]\n"
".Lpad10_%=:\n"
"s_cbranch_execz .Lwb_%=\n"
"s_waitcnt vmcnt(5)\n"
"buffer_load_dwordx4 v11, s[36:39], s63 offen offset:3072 nt lds\n"
"ds_read_b128 a[232:235], v16 offset:16640\n"
"ds_write_b64 v26, a[46:47] offset:39944\n"
"v_exp_f32 v49, v49\n"
"v_mfma_f32_16x16x32_bf16 v[68:71], a[92:95], a[164:167], v[68:71]\n"
"ds_read_b128 a[236:239], v17 offset:16640\n"
"v_exp_f32 v50, v50\n"
"v_mfma_f32_16x16x32_bf16 v[72:75], a[72:75], a[168:171], v[32:35]\n"
"s_add_u32 s62, s10, 163840\n"
"ds_read_b128 a[48:51], v18 offset:16384\n"
"v_exp_f32 v51, v51\n"
"v_mfma_f32_16x16x32_bf16 v[72:75], a[76:79], a[172:175], v[72:75]\n"
"ds_read_b128 a[52:55], v19 offset:16384\n"
"v_cvt_pk_bf16_f32 v158, v48, v49\n"
"v_mfma_f32_16x16x32_bf16 v[72:75], a[80:83], a[176:179], v[72:75]\n"
"ds_read_b128 a[56:59], v18 offset:16512\n"
"buffer_load_dwordx4 a[0:3], v2, s[40:43], s62 offen offset:0\n"
"v_cvt_pk_bf16_f32 v159, v50, v51\n"
"v_mfma_f32_16x16x32_bf16 v[72:75], a[84:87], a[180:183], v[72:75]\n"
"ds_read_b128 a[60:63], v19 offset:16512\n"
"v_exp_f32 v52, v52\n"
"v_mfma_f32_16x16x32_bf16 v[72:75], a[88:91], a[184:187], v[72:75]\n"
"ds_read_b128 a[64:67], v18 offset:16640\n"
"v_exp_f32 v53, v53\n"
"v_mfma_f32_16x16x32_bf16 v[72:75], a[92:95], a[188:191], v[72:75]\n"
"ds_read_b128 a[68:71], v19 offset:16640\n"
"v_exp_f32 v54, v54\n"
"v_mfma_f32_16x16x32_bf16 v[84:87], v[44:47], v[152:155], v[84:87]\n"
"v_exp_f32 v55, v55\n"
"v_mfma_f32_16x16x32_bf16 v[88:91], v[44:47], v[156:159], v[88:91]\n"
"s_waitcnt lgkmcnt(5)\n"
"buffer_load_dwordx4 a[4:7], v3, s[40:43], s62 offen offset:1024\n"
"v_cvt_pk_bf16_f32 v160, v52, v53\n"
"v_mfma_f32_16x16x32_bf16 v[80:83], a[216:219], a[144:147], v[28:31]\n"
"v_cvt_pk_bf16_f32 v161, v54, v55\n"
"v_min_u32 v124, 19, v0\n"
"v_mfma_f32_16x16x32_bf16 v[80:83], a[220:223], a[148:151], v[80:83]\n"
"v_exp_f32 v56, v56\n"
"v_mfma_f32_16x16x32_bf16 v[80:83], a[224:227], a[152:155], v[80:83]\n"
"v_lshlrev_b32 v124, 7, v124\n"
"s_add_u32 s13, s12, 0\n"
"v_exp_f32 v57, v57\n"
"v_mfma_f32_16x16x32_bf16 v[80:83], a[228:231], a[156:159], v[80:83]\n"
"v_exp_f32 v58, v58\n"
"v_mfma_f32_16x16x32_bf16 v[80:83], a[232:235], a[160:163], v[80:83]\n"
"s_waitcnt lgkmcnt(0)\n"
"s_barrier\n"
"buffer_load_dword v124, v124, s[40:43], s13 offen\n"
"v_exp_f32 v59, v59\n"
"v_mfma_f32_16x16x32_bf16 v[80:83], a[236:239], a[164:167], v[80:83]\n"
"ds_read_b128 a[72:75], v12 offset:22528\n"
"v_cvt_pk_bf16_f32 v164, v56, v57\n"
"v_mfma_f32_16x16x32_bf16 v[40:43], a[216:219], a[168:171], v[32:35]\n"
"ds_read_b128 a[76:79], v13 offset:22528\n"
"v_cvt_pk_bf16_f32 v165, v58, v59\n"
"v_mfma_f32_16x16x32_bf16 v[40:43], a[220:223], a[172:175], v[40:43]\n"
"v_exp_f32 v64, v64\n"
"v_mfma_f32_16x16x32_bf16 v[40:43], a[224:227], a[176:179], v[40:43]\n"
"v_exp_f32 v65, v65\n"
"v_mfma_f32_16x16x32_bf16 v[40:43], a[228:231], a[180:183], v[40:43]\n"
"v_exp_f32 v66, v66\n"
"v_mfma_f32_16x16x32_bf16 v[40:43], a[232:235], a[184:187], v[40:43]\n"
"v_exp_f32 v67, v67\n"
"v_mfma_f32_16x16x32_bf16 v[40:43], a[236:239], a[188:191], v[40:43]\n"
"ds_read_b128 a[80:83], v12 offset:22656\n"
"v_cvt_pk_bf16_f32 v112, v64, v65\n"
"v_mfma_f32_16x16x32_bf16 v[48:51], a[48:51], a[144:147], v[28:31]\n"
"ds_read_b128 a[84:87], v13 offset:22656\n"
"v_cvt_pk_bf16_f32 v113, v66, v67\n"
"v_mfma_f32_16x16x32_bf16 v[48:51], a[52:55], a[148:151], v[48:51]\n"
"v_exp_f32 v68, v68\n"
"v_mfma_f32_16x16x32_bf16 v[48:51], a[56:59], a[152:155], v[48:51]\n"
"ds_write_b64 v26, a[96:97] offset:16384\n"
"v_exp_f32 v69, v69\n"
"v_mfma_f32_16x16x32_bf16 v[48:51], a[60:63], a[156:159], v[48:51]\n"
"ds_read_b128 a[88:91], v12 offset:22784\n"
"v_exp_f32 v70, v70\n"
"v_mfma_f32_16x16x32_bf16 v[48:51], a[64:67], a[160:163], v[48:51]\n"
"ds_read_b128 a[92:95], v13 offset:22784\n"
"v_exp_f32 v71, v71\n"
"v_mfma_f32_16x16x32_bf16 v[48:51], a[68:71], a[164:167], v[48:51]\n"
"ds_write_b64 v26, a[98:99] offset:16392\n"
"v_cvt_pk_bf16_f32 v168, v68, v69\n"
"v_mfma_f32_16x16x32_bf16 v[52:55], a[48:51], a[168:171], v[32:35]\n"
"s_mov_b32 s63, 98304\n"
"s_add_u32 m0, s53, 8192\n"
"v_cvt_pk_bf16_f32 v169, v70, v71\n"
"v_mfma_f32_16x16x32_bf16 v[52:55], a[52:55], a[172:175], v[52:55]\n"
"v_exp_f32 v72, v72\n"
"v_mfma_f32_16x16x32_bf16 v[52:55], a[56:59], a[176:179], v[52:55]\n"
"v_exp_f32 v73, v73\n"
"v_mfma_f32_16x16x32_bf16 v[52:55], a[60:63], a[180:183], v[52:55]\n"
"ds_write_b64 v26, a[100:101] offset:17408\n"
"v_exp_f32 v74, v74\n"
"v_mfma_f32_16x16x32_bf16 v[52:55], a[64:67], a[184:187], v[52:55]\n"
"v_exp_f32 v75, v75\n"
"v_mfma_f32_16x16x32_bf16 v[52:55], a[68:71], a[188:191], v[52:55]\n"
"s_waitcnt lgkmcnt(2)\n"
"ds_read_b128 a[216:219], v14 offset:22528\n"
"v_cvt_pk_bf16_f32 v172, v72, v73\n"
"v_mfma_f32_16x16x32_bf16 v[56:59], a[72:75], a[144:147], v[28:31]\n"
"ds_write_b64 v26, a[102:103] offset:17416\n"
"v_cvt_pk_bf16_f32 v173, v74, v75\n"
"v_mfma_f32_16x16x32_bf16 v[56:59], a[76:79], a[148:151], v[56:59]\n"
"v_exp_f32 v80, v80\n"
"v_mfma_f32_16x16x32_bf16 v[56:59], a[80:83], a[152:155], v[56:59]\n"
"s_waitcnt vmcnt(8)\n"
"buffer_load_dwordx4 v8, s[36:39], s63 offen nt lds\n"
"v_exp_f32 v81, v81\n"
"v_mfma_f32_16x16x32_bf16 v[56:59], a[84:87], a[156:159], v[56:59]\n"
"v_exp_f32 v82, v82\n"
"v_mfma_f32_16x16x32_bf16 v[56:59], a[88:91], a[160:163], v[56:59]\n"
"ds_write_b64 v26, a[104:105] offset:18432\n"
"v_exp_f32 v83, v83\n"
"v_mfma_f32_16x16x32_bf16 v[56:59], a[92:95], a[164:167], v[56:59]\n"
"ds_read_b128 a[220:223], v15 offset:22528\n"
"v_cvt_pk_bf16_f32 v176, v80, v81\n"
"v_mfma_f32_16x16x32_bf16 v[64:67], a[72:75], a[168:171], v[32:35]\n"
"buffer_load_dwordx4 a[8:11], v4, s[40:43], s62 offen offset:2048\n"
"v_cvt_pk_bf16_f32 v177, v82, v83\n"
"v_mfma_f32_16x16x32_bf16 v[64:67], a[76:79], a[172:175], v[64:67]\n"
"ds_read_b128 a[224:227], v14 offset:22656\n"
"v_exp_f32 v40, v40\n"
"v_mfma_f32_16x16x32_bf16 v[64:67], a[80:83], a[176:179], v[64:67]\n"
"ds_read_b128 a[228:231], v15 offset:22656\n"
"ds_write_b64 v26, a[106:107] offset:18440\n"
"v_exp_f32 v41, v41\n"
"v_mfma_f32_16x16x32_bf16 v[64:67], a[84:87], a[180:183], v[64:67]\n"
"ds_read_b128 a[232:235], v14 offset:22784\n"
"v_exp_f32 v42, v42\n"
"v_mfma_f32_16x16x32_bf16 v[64:67], a[88:91], a[184:187], v[64:67]\n"
"ds_read_b128 a[236:239], v15 offset:22784\n"
"v_exp_f32 v43, v43\n"
"v_mfma_f32_16x16x32_bf16 v[64:67], a[92:95], a[188:191], v[64:67]\n"
"ds_write_b64 v26, a[108:109] offset:19456\n"
"v_cvt_pk_bf16_f32 v180, v40, v41\n"
"v_mfma_f32_16x16x32_bf16 v[68:71], a[72:75], a[192:195], v[36:39]\n"
"ds_read_b128 a[48:51], v16 offset:22528\n"
"v_cvt_pk_bf16_f32 v181, v42, v43\n"
"v_mfma_f32_16x16x32_bf16 v[68:71], a[76:79], a[196:199], v[68:71]\n"
"s_waitcnt vmcnt(8)\n"
"buffer_load_dwordx4 v9, s[36:39], s63 offen offset:1024 nt lds\n"
"v_exp_f32 v48, v48\n"
"v_mfma_f32_16x16x32_bf16 v[68:71], a[80:83], a[200:203], v[68:71]\n"
"v_exp_f32 v49, v49\n"
"v_mfma_f32_16x16x32_bf16 v[68:71], a[84:87], a[204:207], v[68:71]\n"
"ds_write_b64 v26, a[110:111] offset:19464\n"
"v_exp_f32 v50, v50\n"
"v_mfma_f32_16x16x32_bf16 v[68:71], a[88:91], a[208:211], v[68:71]\n"
"v_exp_f32 v51, v51\n"
"v_mfma_f32_16x16x32_bf16 v[68:71], a[92:95], a[212:215], v[68:71]\n"
"s_waitcnt lgkmcnt(3)\n"
"ds_read_b128 a[52:55], v17 offset:22528\n"
"v_cvt_pk_bf16_f32 v184, v48, v49\n"
"v_mfma_f32_16x16x32_bf16 v[72:75], a[216:219], a[144:147], v[28:31]\n"
"ds_read_b128 a[56:59], v16 offset:22656\n"
"ds_write_b64 v26, a[128:129] offset:20480\n"
"v_cvt_pk_bf16_f32 v185, v50, v51\n"
"v_mfma_f32_16x16x32_bf16 v[72:75], a[220:223], a[148:151], v[72:75]\n"
"ds_read_b128 a[60:63], v17 offset:22656\n"
"v_exp_f32 v52, v52\n"
"v_mfma_f32_16x16x32_bf16 v[72:75], a[224:227], a[152:155], v[72:75]\n"
"ds_read_b128 a[64:67], v16 offset:22784\n"
"v_exp_f32 v53, v53\n"
"v_mfma_f32_16x16x32_bf16 v[72:75], a[228:231], a[156:159], v[72:75]\n"
"ds_read_b128 a[68:71], v17 offset:22784\n"
"v_exp_f32 v54, v54\n"
"v_mfma_f32_16x16x32_bf16 v[72:75], a[232:235], a[160:163], v[72:75]\n"
"ds_read_b128 a[72:75], v18 offset:22528\n"
"ds_write_b64 v26, a[130:131] offset:20488\n"
"v_exp_f32 v55, v55\n"
"v_mfma_f32_16x16x32_bf16 v[72:75], a[236:239], a[164:167], v[72:75]\n"
"ds_read_b128 a[76:79], v19 offset:22528\n"
"v_cvt_pk_bf16_f32 v188, v52, v53\n"
"v_mfma_f32_16x16x32_bf16 v[80:83], a[216:219], a[168:171], v[32:35]\n"
"s_waitcnt vmcnt(8)\n"
"buffer_load_dwordx4 v10, s[36:39], s63 offen offset:2048 nt lds\n"
"ds_read_b128 a[80:83], v18 offset:22656\n"
"v_cvt_pk_bf16_f32 v189, v54, v55\n"
"v_mfma_f32_16x16x32_bf16 v[80:83], a[220:223], a[172:175], v[80:83]\n"
"ds_read_b128 a[84:87], v19 offset:22656\n"
"v_exp_f32 v56, v56\n"
"v_mfma_f32_16x16x32_bf16 v[80:83], a[224:227], a[176:179], v[80:83]\n"
"ds_read_b128 a[88:91], v18 offset:22784\n"
"ds_write_b64 v26, a[132:133] offset:21504\n"
"v_exp_f32 v57, v57\n"
"v_mfma_f32_16x16x32_bf16 v[80:83], a[228:231], a[180:183], v[80:83]\n"
"ds_read_b128 a[92:95], v19 offset:22784\n"
"ds_write_b64 v26, a[134:135] offset:21512\n"
"v_exp_f32 v58, v58\n"
"v_mfma_f32_16x16x32_bf16 v[80:83], a[232:235], a[184:187], v[80:83]\n"
"buffer_load_dwordx4 a[12:15], v5, s[40:43], s62 offen offset:3072\n"
"v_exp_f32 v59, v59\n"
"v_mfma_f32_16x16x32_bf16 v[80:83], a[236:239], a[188:191], v[80:83]\n"
"s_waitcnt lgkmcnt(7)\n"
"v_cvt_pk_bf16_f32 v162, v56, v57\n"
"v_cvt_pk_bf16_f32 v163, v58, v59\n"
"v_mfma_f32_16x16x32_bf16 v[40:43], a[48:51], a[144:147], v[28:31]\n"
"v_exp_f32 v64, v64\n"
"v_mfma_f32_16x16x32_bf16 v[40:43], a[52:55], a[148:151], v[40:43]\n"
"v_exp_f32 v65, v65\n"
"v_mfma_f32_16x16x32_bf16 v[40:43], a[56:59], a[152:155], v[40:43]\n"
"v_exp_f32 v66, v66\n"
"v_mfma_f32_16x16x32_bf16 v[40:43], a[60:63], a[156:159], v[40:43]\n"
"v_exp_f32 v67, v67\n"
"v_mfma_f32_16x16x32_bf16 v[40:43], a[64:67], a[160:163], v[40:43]\n"
"s_waitcnt lgkmcnt(0)\n"
"s_barrier\n"
"v_cvt_pk_bf16_f32 v166, v64, v65\n"
"v_mfma_f32_16x16x32_bf16 v[40:43], a[68:71], a[164:167], v[40:43]\n"
"s_waitcnt vmcnt(9)\n"
"buffer_load_dwordx4 v11, s[36:39], s63 offen offset:3072 nt lds\n"
"v_mfma_f32_16x16x32_bf16 v[48:51], a[48:51], a[168:171], v[32:35]\n"
"v_cvt_pk_bf16_f32 v167, v66, v67\n"
"v_min_u32 v125, 19, v0\n"
"v_mfma_f32_16x16x32_bf16 v[48:51], a[52:55], a[172:175], v[48:51]\n"
"v_exp_f32 v68, v68\n"
"v_mfma_f32_16x16x32_bf16 v[48:51], a[56:59], a[176:179], v[48:51]\n"
"v_exp_f32 v69, v69\n"
"v_mfma_f32_16x16x32_bf16 v[48:51], a[60:63], a[180:183], v[48:51]\n"
"v_exp_f32 v70, v70\n"
"v_mfma_f32_16x16x32_bf16 v[48:51], a[64:67], a[184:187], v[48:51]\n"
"v_exp_f32 v71, v71\n"
"v_mfma_f32_16x16x32_bf16 v[48:51], a[68:71], a[188:191], v[48:51]\n"
"v_cvt_pk_bf16_f32 v114, v68, v69\n"
"v_lshlrev_b32 v125, 7, v125\n"
"v_mfma_f32_16x16x32_bf16 v[84:87], v[44:47], v[160:163], v[84:87]\n"
"s_add_u32 s13, s12, 81920\n"
"ds_read_b128 a[216:219], v12 offset:28672\n"
"v_cvt_pk_bf16_f32 v115, v70, v71\n"
"v_mfma_f32_16x16x32_bf16 v[88:91], v[44:47], v[164:167], v[88:91]\n"
"v_exp_f32 v72, v72\n"
"v_mfma_f32_16x16x32_bf16 v[92:95], v[44:47], v[112:115], v[92:95]\n"
"ds_write_b64 v26, a[136:137] offset:22528\n"
"v_mfma_f32_16x16x32_bf16 v[52:55], a[72:75], a[144:147], v[28:31]\n"
"v_exp_f32 v73, v73\n"
"v_mfma_f32_16x16x32_bf16 v[52:55], a[76:79], a[148:151], v[52:55]\n"
"buffer_load_dword v125, v125, s[40:43], s13 offen\n"
"v_mfma_f32_16x16x32_bf16 v[52:55], a[80:83], a[152:155], v[52:55]\n"
"ds_read_b128 a[220:223], v13 offset:28672\n"
"ds_write_b64 v26, a[138:139] offset:22536\n"
"v_exp_f32 v74, v74\n"
"v_mfma_f32_16x16x32_bf16 v[52:55], a[84:87], a[156:159], v[52:55]\n"
"ds_read_b128 a[224:227], v12 offset:28800\n"
"v_exp_f32 v75, v75\n"
"v_mfma_f32_16x16x32_bf16 v[52:55], a[88:91], a[160:163], v[52:55]\n"
"ds_read_b128 a[228:231], v13 offset:28800\n"
"v_cvt_pk_bf16_f32 v170, v72, v73\n"
"v_mfma_f32_16x16x32_bf16 v[52:55], a[92:95], a[164:167], v[52:55]\n"
"ds_read_b128 a[232:235], v12 offset:28928\n"
"buffer_load_dwordx4 a[16:19], v6, s[40:43], s62 offen offset:0\n"
"v_cvt_pk_bf16_f32 v171, v74, v75\n"
"v_mfma_f32_16x16x32_bf16 v[56:59], a[72:75], a[168:171], v[32:35]\n"
"ds_read_b128 a[236:239], v13 offset:28928\n"
"ds_write_b64 v26, a[140:141] offset:23552\n"
"v_exp_f32 v80, v80\n"
"v_mfma_f32_16x16x32_bf16 v[56:59], a[76:79], a[172:175], v[56:59]\n"
"v_exp_f32 v81, v81\n"
"v_mfma_f32_16x16x32_bf16 v[56:59], a[80:83], a[176:179], v[56:59]\n"
"v_exp_f32 v82, v82\n"
"v_mfma_f32_16x16x32_bf16 v[56:59], a[84:87], a[180:183], v[56:59]\n"
"ds_write_b64 v26, a[142:143] offset:23560\n"
"v_exp_f32 v83, v83\n"
"v_mfma_f32_16x16x32_bf16 v[56:59], a[88:91], a[184:187], v[56:59]\n"
"ds_read_b128 a[48:51], v14 offset:28672\n"
"v_cvt_pk_bf16_f32 v174, v80, v81\n"
"v_mfma_f32_16x16x32_bf16 v[56:59], a[92:95], a[188:191], v[56:59]\n"
"ds_read_b128 a[52:55], v15 offset:28672\n"
"v_cvt_pk_bf16_f32 v175, v82, v83\n"
"v_mfma_f32_16x16x32_bf16 v[84:87], v[44:47], v[168:171], v[84:87]\n"
"v_exp_f32 v40, v40\n"
"v_mfma_f32_16x16x32_bf16 v[88:91], v[44:47], v[172:175], v[88:91]\n"
"s_waitcnt lgkmcnt(3)\n"
"ds_write_b64 v26, a[244:245] offset:24576\n"
"v_mfma_f32_16x16x32_bf16 v[64:67], a[216:219], a[144:147], v[28:31]\n"
"v_exp_f32 v41, v41\n"
"v_mfma_f32_16x16x32_bf16 v[64:67], a[220:223], a[148:151], v[64:67]\n"
"v_exp_f32 v42, v42\n"
"v_mfma_f32_16x16x32_bf16 v[64:67], a[224:227], a[152:155], v[64:67]\n"
"v_exp_f32 v43, v43\n"
"v_mfma_f32_16x16x32_bf16 v[64:67], a[228:231], a[156:159], v[64:67]\n"
"ds_write_b64 v26, a[246:247] offset:24584\n"
"v_mfma_f32_16x16x32_bf16 v[64:67], a[232:235], a[160:163], v[64:67]\n"
"s_mov_b32 s63, 147456\n"
"ds_read_b128 a[56:59], v14 offset:28800\n"
"v_cvt_pk_bf16_f32 v178, v40, v41\n"
"v_mfma_f32_16x16x32_bf16 v[64:67], a[236:239], a[164:167], v[64:67]\n"
"s_add_u32 m0, s53, 4096\n"
"ds_read_b128 a[60:63], v15 offset:28800\n"
"v_cvt_pk_bf16_f32 v179, v42, v43\n"
"v_mfma_f32_16x16x32_bf16 v[68:71], a[216:219], a[168:171], v[32:35]\n"
"ds_write_b64 v26, a[248:249] offset:25600\n"
"v_mfma_f32_16x16x32_bf16 v[68:71], a[220:223], a[172:175], v[68:71]\n"
"v_exp_f32 v48, v48\n"
"v_mfma_f32_16x16x32_bf16 v[68:71], a[224:227], a[176:179], v[68:71]\n"
"v_exp_f32 v49, v49\n"
"v_mfma_f32_16x16x32_bf16 v[68:71], a[228:231], a[180:183], v[68:71]\n"
"v_exp_f32 v50, v50\n"
"v_mfma_f32_16x16x32_bf16 v[68:71], a[232:235], a[184:187], v[68:71]\n"
"ds_write_b64 v26, a[250:251] offset:25608\n"
"v_mfma_f32_16x16x32_bf16 v[68:71], a[236:239], a[188:191], v[68:71]\n"
"ds_read_b128 a[64:67], v14 offset:28928\n"
"v_mfma_f32_16x16x32_bf16 v[72:75], a[216:219], a[192:195], v[36:39]\n"
"s_waitcnt vmcnt(11)\n"
"buffer_load_dwordx4 v8, s[36:39], s63 offen nt lds\n"
"ds_read_b128 a[68:71], v15 offset:28928\n"
"v_mfma_f32_16x16x32_bf16 v[72:75], a[220:223], a[196:199], v[72:75]\n"
"ds_write_b64 v26, a[252:253] offset:26624\n"
"v_mfma_f32_16x16x32_bf16 v[72:75], a[224:227], a[200:203], v[72:75]\n"
"v_exp_f32 v51, v51\n"
"v_mfma_f32_16x16x32_bf16 v[72:75], a[228:231], a[204:207], v[72:75]\n"
"ds_read_b128 a[72:75], v16 offset:28672\n"
"v_cvt_pk_bf16_f32 v182, v48, v49\n"
"v_mfma_f32_16x16x32_bf16 v[72:75], a[232:235], a[208:211], v[72:75]\n"
"ds_read_b128 a[76:79], v17 offset:28672\n"
"v_cvt_pk_bf16_f32 v183, v50, v51\n"
"v_mfma_f32_16x16x32_bf16 v[72:75], a[236:239], a[212:215], v[72:75]\n"
"ds_write_b64 v26, a[254:255] offset:26632\n"
"v_exp_f32 v52, v52\n"
"v_mfma_f32_16x16x32_bf16 v[84:87], v[44:47], v[176:179], v[84:87]\n"
"v_exp_f32 v53, v53\n"
"v_mfma_f32_16x16x32_bf16 v[88:91], v[44:47], v[180:183], v[88:91]\n"
"s_waitcnt lgkmcnt(4)\n"
"v_exp_f32 v54, v54\n"
"v_mfma_f32_16x16x32_bf16 v[80:83], a[48:51], a[144:147], v[28:31]\n"
"v_exp_f32 v55, v55\n"
"v_mfma_f32_16x16x32_bf16 v[80:83], a[52:55], a[148:151], v[80:83]\n"
"ds_write_b64 v26, v[252:253] offset:27648\n"
"v_cvt_pk_bf16_f32 v186, v52, v53\n"
"v_mfma_f32_16x16x32_bf16 v[80:83], a[56:59], a[152:155], v[80:83]\n"
"ds_read_b128 a[80:83], v16 offset:28800\n"
"v_cvt_pk_bf16_f32 v187, v54, v55\n"
"v_mfma_f32_16x16x32_bf16 v[80:83], a[60:63], a[156:159], v[80:83]\n"
"s_waitcnt vmcnt(8)\n"
"buffer_load_dwordx4 v9, s[36:39], s63 offen offset:1024 nt lds\n"
"ds_read_b128 a[84:87], v17 offset:28800\n"
"v_exp_f32 v56, v56\n"
"v_mfma_f32_16x16x32_bf16 v[80:83], a[64:67], a[160:163], v[80:83]\n"
"ds_read_b128 a[88:91], v16 offset:28928\n"
"ds_write_b64 v26, v[254:255] offset:27656\n"
"v_exp_f32 v57, v57\n"
"v_mfma_f32_16x16x32_bf16 v[80:83], a[68:71], a[164:167], v[80:83]\n"
"s_add_u32 s62, s11, 163840\n"
"ds_read_b128 a[92:95], v17 offset:28928\n"
"v_exp_f32 v58, v58\n"
"v_mfma_f32_16x16x32_bf16 v[40:43], a[48:51], a[168:171], v[32:35]\n"
"ds_read_b128 a[216:219], v18 offset:28672\n"
"v_exp_f32 v59, v59\n"
"v_mfma_f32_16x16x32_bf16 v[40:43], a[52:55], a[172:175], v[40:43]\n"
"ds_read_b128 a[220:223], v19 offset:28672\n"
"buffer_load_dwordx4 a[20:23], v2, s[40:43], s62 offen offset:0\n"
"v_cvt_pk_bf16_f32 v190, v56, v57\n"
"v_mfma_f32_16x16x32_bf16 v[40:43], a[56:59], a[176:179], v[40:43]\n"
"ds_read_b128 a[224:227], v18 offset:28800\n"
"v_cvt_pk_bf16_f32 v191, v58, v59\n"
"v_mfma_f32_16x16x32_bf16 v[40:43], a[60:63], a[180:183], v[40:43]\n"
"ds_read_b128 a[228:231], v19 offset:28800\n"
"v_exp_f32 v64, v64\n"
"v_mfma_f32_16x16x32_bf16 v[40:43], a[64:67], a[184:187], v[40:43]\n"
"ds_read_b128 a[232:235], v18 offset:28928\n"
"v_exp_f32 v65, v65\n"
"v_mfma_f32_16x16x32_bf16 v[40:43], a[68:71], a[188:191], v[40:43]\n"
"ds_read_b128 a[236:239], v19 offset:28928\n"
"v_exp_f32 v66, v66\n"
"v_mfma_f32_16x16x32_bf16 v[84:87], v[44:47], v[184:187], v[84:87]\n"
"v_exp_f32 v67, v67\n"
"v_mfma_f32_16x16x32_bf16 v[88:91], v[44:47], v[188:191], v[88:91]\n"
"s_waitcnt lgkmcnt(5)\n"
"v_cvt_pk_bf16_f32 v192, v64, v65\n"
"v_cvt_pk_bf16_f32 v193, v66, v67\n"
"v_mfma_f32_16x16x32_bf16 v[48:51], a[72:75], a[144:147], v[28:31]\n"
"s_waitcnt vmcnt(8)\n"
"buffer_load_dwordx4 v10, s[36:39], s63 offen offset:2048 nt lds\n"
"v_mfma_f32_16x16x32_bf16 v[48:51], a[76:79], a[148:151], v[48:51]\n"
"v_exp_f32 v68, v68\n"
"v_mfma_f32_16x16x32_bf16 v[48:51], a[80:83], a[152:155], v[48:51]\n"
"v_exp_f32 v69, v69\n"
"v_mfma_f32_16x16x32_bf16 v[48:51], a[84:87], a[156:159], v[48:51]\n"
"v_exp_f32 v70, v70\n"
"v_mfma_f32_16x16x32_bf16 v[48:51], a[88:91], a[160:163], v[48:51]\n"
"s_waitcnt lgkmcnt(0)\n"
"s_barrier\n"
"v_exp_f32 v71, v71\n"
"v_mfma_f32_16x16x32_bf16 v[48:51], a[92:95], a[164:167], v[48:51]\n"
"ds_read_b128 a[48:51], v12 offset:34816\n"
"v_cvt_pk_bf16_f32 v196, v68, v69\n"
"v_mfma_f32_16x16x32_bf16 v[52:55], a[72:75], a[168:171], v[32:35]\n"
"ds_read_b128 a[52:55], v13 offset:34816\n"
"v_cvt_pk_bf16_f32 v197, v70, v71\n"
"v_mfma_f32_16x16x32_bf16 v[52:55], a[76:79], a[172:175], v[52:55]\n"
"v_exp_f32 v72, v72\n"
"v_mfma_f32_16x16x32_bf16 v[52:55], a[80:83], a[176:179], v[52:55]\n"
"v_exp_f32 v73, v73\n"
"v_mfma_f32_16x16x32_bf16 v[52:55], a[84:87], a[180:183], v[52:55]\n"
"v_exp_f32 v74, v74\n"
"v_mfma_f32_16x16x32_bf16 v[52:55], a[88:91], a[184:187], v[52:55]\n"
"v_exp_f32 v75, v75\n"
"v_mfma_f32_16x16x32_bf16 v[52:55], a[92:95], a[188:191], v[52:55]\n"
"s_waitcnt vmcnt(8)\n"
"buffer_load_dwordx4 v11, s[36:39], s63 offen offset:3072 nt lds\n"
"v_cvt_pk_bf16_f32 v116, v72, v73\n"
"v_mfma_f32_16x16x32_bf16 v[56:59], a[216:219], a[144:147], v[28:31]\n"
"ds_read_b128 a[56:59], v12 offset:34944\n"
"v_cvt_pk_bf16_f32 v117, v74, v75\n"
"v_mfma_f32_16x16x32_bf16 v[56:59], a[220:223], a[148:151], v[56:59]\n"
"v_exp_f32 v80, v80\n"
"v_mfma_f32_16x16x32_bf16 v[56:59], a[224:227], a[152:155], v[56:59]\n"
"ds_read_b128 a[60:63], v13 offset:34944\n"
"v_exp_f32 v81, v81\n"
"v_mfma_f32_16x16x32_bf16 v[56:59], a[228:231], a[156:159], v[56:59]\n"
"ds_read_b128 a[64:67], v12 offset:35072\n"
"buffer_load_dwordx4 a[24:27], v3, s[40:43], s62 offen offset:1024\n"
"v_exp_f32 v82, v82\n"
"v_mfma_f32_16x16x32_bf16 v[56:59], a[232:235], a[160:163], v[56:59]\n"
"ds_read_b128 a[68:71], v13 offset:35072\n"
"v_exp_f32 v83, v83\n"
"v_mfma_f32_16x16x32_bf16 v[56:59], a[236:239], a[164:167], v[56:59]\n"
"s_mov_b32 s63, 128\n"
"s_add_u32 m0, s53, 28672\n"
"v_cvt_pk_bf16_f32 v200, v80, v81\n"
"v_mfma_f32_16x16x32_bf16 v[64:67], a[216:219], a[168:171], v[32:35]\n"
"ds_read_b128 a[72:75], v14 offset:34816\n"
"v_cvt_pk_bf16_f32 v201, v82, v83\n"
"v_mfma_f32_16x16x32_bf16 v[64:67], a[220:223], a[172:175], v[64:67]\n"
"v_exp_f32 v40, v40\n"
"v_mfma_f32_16x16x32_bf16 v[64:67], a[224:227], a[176:179], v[64:67]\n"
"v_exp_f32 v41, v41\n"
"v_mfma_f32_16x16x32_bf16 v[64:67], a[228:231], a[180:183], v[64:67]\n"
"v_exp_f32 v42, v42\n"
"v_mfma_f32_16x16x32_bf16 v[64:67], a[232:235], a[184:187], v[64:67]\n"
"s_waitcnt vmcnt(8)\n"
"buffer_load_dwordx4 v8, s[36:39], s63 offen nt lds\n"
"v_exp_f32 v43, v43\n"
"v_mfma_f32_16x16x32_bf16 v[64:67], a[236:239], a[188:191], v[64:67]\n"
"s_waitcnt lgkmcnt(1)\n"
"ds_read_b128 a[76:79], v15 offset:34816\n"
"v_cvt_pk_bf16_f32 v204, v40, v41\n"
"v_mfma_f32_16x16x32_bf16 v[68:71], a[48:51], a[144:147], v[28:31]\n"
"ds_read_b128 a[80:83], v14 offset:34944\n"
"v_cvt_pk_bf16_f32 v205, v42, v43\n"
"v_mfma_f32_16x16x32_bf16 v[68:71], a[52:55], a[148:151], v[68:71]\n"
"v_exp_f32 v48, v48\n"
"v_mfma_f32_16x16x32_bf16 v[68:71], a[56:59], a[152:155], v[68:71]\n"
"v_exp_f32 v49, v49\n"
"v_mfma_f32_16x16x32_bf16 v[68:71], a[60:63], a[156:159], v[68:71]\n"
"v_exp_f32 v50, v50\n"
"v_mfma_f32_16x16x32_bf16 v[68:71], a[64:67], a[160:163], v[68:71]\n"
"v_exp_f32 v51, v51\n"
"v_mfma_f32_16x16x32_bf16 v[68:71], a[68:71], a[164:167], v[68:71]\n"
"ds_read_b128 a[84:87], v15 offset:34944\n"
"v_cvt_pk_bf16_f32 v208, v48, v49\n"
"v_mfma_f32_16x16x32_bf16 v[72:75], a[48:51], a[168:171], v[32:35]\n"
"ds_read_b128 a[88:91], v14 offset:35072\n"
"v_cvt_pk_bf16_f32 v209, v50, v51\n"
"v_mfma_f32_16x16x32_bf16 v[72:75], a[52:55], a[172:175], v[72:75]\n"
"v_exp_f32 v52, v52\n"
"v_mfma_f32_16x16x32_bf16 v[72:75], a[56:59], a[176:179], v[72:75]\n"
"v_exp_f32 v53, v53\n"
"v_mfma_f32_16x16x32_bf16 v[72:75], a[60:63], a[180:183], v[72:75]\n"
"v_exp_f32 v54, v54\n"
"v_mfma_f32_16x16x32_bf16 v[72:75], a[64:67], a[184:187], v[72:75]\n"
"ds_read_b128 a[92:95], v15 offset:35072\n"
"v_exp_f32 v55, v55\n"
"v_mfma_f32_16x16x32_bf16 v[72:75], a[68:71], a[188:191], v[72:75]\n"
"ds_read_b128 a[216:219], v16 offset:34816\n"
"v_cvt_pk_bf16_f32 v212, v52, v53\n"
"v_mfma_f32_16x16x32_bf16 v[80:83], a[48:51], a[192:195], v[36:39]\n"
"ds_read_b128 a[220:223], v17 offset:34816\n"
"v_cvt_pk_bf16_f32 v213, v54, v55\n"
"v_mfma_f32_16x16x32_bf16 v[80:83], a[52:55], a[196:199], v[80:83]\n"
"s_waitcnt vmcnt(6)\n"
"buffer_load_dwordx4 v9, s[36:39], s63 offen offset:1024 nt lds\n"
"v_exp_f32 v56, v56\n"
"v_mfma_f32_16x16x32_bf16 v[80:83], a[56:59], a[200:203], v[80:83]\n"
"v_exp_f32 v57, v57\n"
"v_mfma_f32_16x16x32_bf16 v[80:83], a[60:63], a[204:207], v[80:83]\n"
"v_exp_f32 v58, v58\n"
"v_mfma_f32_16x16x32_bf16 v[80:83], a[64:67], a[208:211], v[80:83]\n"
"v_exp_f32 v59, v59\n"
"v_mfma_f32_16x16x32_bf16 v[80:83], a[68:71], a[212:215], v[80:83]\n"
"s_waitcnt lgkmcnt(2)\n"
"buffer_load_dwordx4 a[28:31], v4, s[40:43], s62 offen offset:2048\n"
"v_cvt_pk_bf16_f32 v216, v56, v57\n"
"v_mfma_f32_16x16x32_bf16 v[40:43], a[72:75], a[144:147], v[28:31]\n"
"ds_read_b128 a[224:227], v16 offset:34944\n"
"v_cvt_pk_bf16_f32 v217, v58, v59\n"
"v_mfma_f32_16x16x32_bf16 v[40:43], a[76:79], a[148:151], v[40:43]\n"
"ds_read_b128 a[228:231], v17 offset:34944\n"
"v_exp_f32 v64, v64\n"
"v_mfma_f32_16x16x32_bf16 v[40:43], a[80:83], a[152:155], v[40:43]\n"
"ds_read_b128 a[232:235], v16 offset:35072\n"
"v_exp_f32 v65, v65\n"
"v_mfma_f32_16x16x32_bf16 v[40:43], a[84:87], a[156:159], v[40:43]\n"
"ds_read_b128 a[236:239], v17 offset:35072\n"
"v_exp_f32 v66, v66\n"
"v_mfma_f32_16x16x32_bf16 v[40:43], a[88:91], a[160:163], v[40:43]\n"
"ds_read_b128 a[48:51], v18 offset:34816\n"
"v_exp_f32 v67, v67\n"
"v_mfma_f32_16x16x32_bf16 v[40:43], a[92:95], a[164:167], v[40:43]\n"
"ds_read_b128 a[52:55], v19 offset:34816\n"
"v_cvt_pk_bf16_f32 v220, v64, v65\n"
"v_mfma_f32_16x16x32_bf16 v[48:51], a[72:75], a[168:171], v[32:35]\n"
"ds_read_b128 a[56:59], v18 offset:34944\n"
"v_cvt_pk_bf16_f32 v221, v66, v67\n"
"v_mfma_f32_16x16x32_bf16 v[48:51], a[76:79], a[172:175], v[48:51]\n"
"ds_read_b128 a[60:63], v19 offset:34944\n"
"v_exp_f32 v68, v68\n"
"v_mfma_f32_16x16x32_bf16 v[48:51], a[80:83], a[176:179], v[48:51]\n"
"ds_read_b128 a[64:67], v18 offset:35072\n"
"v_exp_f32 v69, v69\n"
"v_mfma_f32_16x16x32_bf16 v[48:51], a[84:87], a[180:183], v[48:51]\n"
"s_waitcnt vmcnt(7)\n"
"buffer_load_dwordx4 v10, s[36:39], s63 offen offset:2048 nt lds\n"
"ds_read_b128 a[68:71], v19 offset:35072\n"
"v_exp_f32 v70, v70\n"
"v_mfma_f32_16x16x32_bf16 v[48:51], a[88:91], a[184:187], v[48:51]\n"
"v_exp_f32 v71, v71\n"
"v_mfma_f32_16x16x32_bf16 v[48:51], a[92:95], a[188:191], v[48:51]\n"
"s_waitcnt lgkmcnt(5)\n"
"v_cvt_pk_bf16_f32 v194, v68, v69\n"
"v_cvt_pk_bf16_f32 v195, v70, v71\n"
"v_mfma_f32_16x16x32_bf16 v[52:55], a[216:219], a[144:147], v[28:31]\n"
"v_exp_f32 v72, v72\n"
"v_mfma_f32_16x16x32_bf16 v[52:55], a[220:223], a[148:151], v[52:55]\n"
"v_exp_f32 v73, v73\n"
"v_mfma_f32_16x16x32_bf16 v[52:55], a[224:227], a[152:155], v[52:55]\n"
"v_exp_f32 v74, v74\n"
"v_mfma_f32_16x16x32_bf16 v[52:55], a[228:231], a[156:159], v[52:55]\n"
"v_exp_f32 v75, v75\n"
"v_mfma_f32_16x16x32_bf16 v[52:55], a[232:235], a[160:163], v[52:55]\n"
"s_waitcnt lgkmcnt(0)\n"
"s_barrier\n"
"v_cvt_pk_bf16_f32 v198, v72, v73\n"
"v_mfma_f32_16x16x32_bf16 v[52:55], a[236:239], a[164:167], v[52:55]\n"
"ds_read_b128 a[72:75], v12 offset:16384\n"
"v_cvt_pk_bf16_f32 v199, v74, v75\n"
"v_mfma_f32_16x16x32_bf16 v[56:59], a[216:219], a[168:171], v[32:35]\n"
"v_exp_f32 v80, v80\n"
"v_mfma_f32_16x16x32_bf16 v[56:59], a[220:223], a[172:175], v[56:59]\n"
"v_exp_f32 v81, v81\n"
"v_mfma_f32_16x16x32_bf16 v[56:59], a[224:227], a[176:179], v[56:59]\n"
"v_exp_f32 v82, v82\n"
"v_mfma_f32_16x16x32_bf16 v[56:59], a[228:231], a[180:183], v[56:59]\n"
"v_exp_f32 v83, v83\n"
"v_mfma_f32_16x16x32_bf16 v[56:59], a[232:235], a[184:187], v[56:59]\n"
"ds_read_b128 a[76:79], v13 offset:16384\n"
"v_cvt_pk_bf16_f32 v118, v80, v81\n"
"v_mfma_f32_16x16x32_bf16 v[56:59], a[236:239], a[188:191], v[56:59]\n"
"ds_read_b128 a[80:83], v12 offset:16512\n"
"v_cvt_pk_bf16_f32 v119, v82, v83\n"
"v_mfma_f32_16x16x32_bf16 v[84:87], v[44:47], v[192:195], v[84:87]\n"
"s_waitcnt vmcnt(6)\n"
"buffer_load_dwordx4 v11, s[36:39], s63 offen offset:3072 nt lds\n"
"v_mfma_f32_16x16x32_bf16 v[88:91], v[44:47], v[196:199], v[88:91]\n"
"v_exp_f32 v40, v40\n"
"v_mfma_f32_16x16x32_bf16 v[92:95], v[44:47], v[116:119], v[92:95]\n"
"v_exp_f32 v41, v41\n"
"v_mfma_f32_16x16x32_bf16 v[64:67], a[48:51], a[144:147], v[28:31]\n"
"v_exp_f32 v42, v42\n"
"v_mfma_f32_16x16x32_bf16 v[64:67], a[52:55], a[148:151], v[64:67]\n"
"buffer_load_dwordx4 a[32:35], v5, s[40:43], s62 offen offset:3072\n"
"v_mfma_f32_16x16x32_bf16 v[64:67], a[56:59], a[152:155], v[64:67]\n"
"v_exp_f32 v43, v43\n"
"v_mfma_f32_16x16x32_bf16 v[64:67], a[60:63], a[156:159], v[64:67]\n"
"s_mov_b32 s63, 49280\n"
"ds_read_b128 a[84:87], v13 offset:16512\n"
"v_cvt_pk_bf16_f32 v202, v40, v41\n"
"v_mfma_f32_16x16x32_bf16 v[64:67], a[64:67], a[160:163], v[64:67]\n"
"s_add_u32 m0, s53, 32768\n"
"ds_read_b128 a[88:91], v12 offset:16640\n"
"v_cvt_pk_bf16_f32 v203, v42, v43\n"
"v_mfma_f32_16x16x32_bf16 v[64:67], a[68:71], a[164:167], v[64:67]\n"
"v_exp_f32 v48, v48\n"
"v_mfma_f32_16x16x32_bf16 v[68:71], a[48:51], a[168:171], v[32:35]\n"
"ds_read_b128 a[92:95], v13 offset:16640\n"
"v_mfma_f32_16x16x32_bf16 v[68:71], a[52:55], a[172:175], v[68:71]\n"
"v_exp_f32 v49, v49\n"
"v_mfma_f32_16x16x32_bf16 v[68:71], a[56:59], a[176:179], v[68:71]\n"
"v_exp_f32 v50, v50\n"
"v_mfma_f32_16x16x32_bf16 v[68:71], a[60:63], a[180:183], v[68:71]\n"
"v_exp_f32 v51, v51\n"
"v_mfma_f32_16x16x32_bf16 v[68:71], a[64:67], a[184:187], v[68:71]\n"
"ds_read_b128 a[216:219], v14 offset:16384\n"
"v_cvt_pk_bf16_f32 v206, v48, v49\n"
"v_mfma_f32_16x16x32_bf16 v[68:71], a[68:71], a[188:191], v[68:71]\n"
"ds_read_b128 a[220:223], v15 offset:16384\n"
"v_cvt_pk_bf16_f32 v207, v50, v51\n"
"v_mfma_f32_16x16x32_bf16 v[84:87], v[44:47], v[200:203], v[84:87]\n"
"s_waitcnt vmcnt(7)\n"
"buffer_load_dwordx4 v8, s[36:39], s63 offen nt lds\n"
"v_mfma_f32_16x16x32_bf16 v[88:91], v[44:47], v[204:207], v[88:91]\n"
"s_waitcnt lgkmcnt(2)\n"
"v_exp_f32 v52, v52\n"
"v_mfma_f32_16x16x32_bf16 v[72:75], a[72:75], a[144:147], v[28:31]\n"
"v_exp_f32 v53, v53\n"
"v_mfma_f32_16x16x32_bf16 v[72:75], a[76:79], a[148:151], v[72:75]\n"
".Lpad11_%=:\n"
"s_cbranch_execz .Lwb_%=\n"
"v_exp_f32 v54, v54\n"
"v_mfma_f32_16x16x32_bf16 v[72:75], a[80:83], a[152:155], v[72:75]\n"
"v_exp_f32 v55, v55\n"
"v_mfma_f32_16x16x32_bf16 v[72:75], a[84:87], a[156:159], v[72:75]\n"
"ds_read_b128 a[224:227], v14 offset:16512\n"
"v_cvt_pk_bf16_f32 v210, v52, v53\n"
"v_mfma_f32_16x16x32_bf16 v[72:75], a[88:91], a[160:163], v[72:75]\n"
"ds_read_b128 a[228:231], v15 offset:16512\n"
"v_cvt_pk_bf16_f32 v211, v54, v55\n"
"v_mfma_f32_16x16x32_bf16 v[72:75], a[92:95], a[164:167], v[72:75]\n"
"v_exp_f32 v56, v56\n"
"v_mfma_f32_16x16x32_bf16 v[80:83], a[72:75], a[168:171], v[32:35]\n"
"v_exp_f32 v57, v57\n"
"v_mfma_f32_16x16x32_bf16 v[80:83], a[76:79], a[172:175], v[80:83]\n"
"v_exp_f32 v58, v58\n"
"v_mfma_f32_16x16x32_bf16 v[80:83], a[80:83], a[176:179], v[80:83]\n"
"v_exp_f32 v59, v59\n"
"v_mfma_f32_16x16x32_bf16 v[80:83], a[84:87], a[180:183], v[80:83]\n"
"ds_read_b128 a[232:235], v14 offset:16640\n"
"v_cvt_pk_bf16_f32 v214, v56, v57\n"
"v_mfma_f32_16x16x32_bf16 v[80:83], a[88:91], a[184:187], v[80:83]\n"
"ds_read_b128 a[236:239], v15 offset:16640\n"
"v_cvt_pk_bf16_f32 v215, v58, v59\n"
"v_mfma_f32_16x16x32_bf16 v[80:83], a[92:95], a[188:191], v[80:83]\n"
"v_exp_f32 v64, v64\n"
"v_mfma_f32_16x16x32_bf16 v[40:43], a[72:75], a[192:195], v[36:39]\n"
"v_exp_f32 v65, v65\n"
"v_mfma_f32_16x16x32_bf16 v[40:43], a[76:79], a[196:199], v[40:43]\n"
"s_waitcnt vmcnt(6)\n"
"buffer_load_dwordx4 v9, s[36:39], s63 offen offset:1024 nt lds\n"
"v_mfma_f32_16x16x32_bf16 v[40:43], a[80:83], a[200:203], v[40:43]\n"
"v_exp_f32 v66, v66\n"
"v_mfma_f32_16x16x32_bf16 v[40:43], a[84:87], a[204:207], v[40:43]\n"
"v_exp_f32 v67, v67\n"
"v_mfma_f32_16x16x32_bf16 v[40:43], a[88:91], a[208:211], v[40:43]\n"
"ds_read_b128 a[48:51], v16 offset:16384\n"
"v_cvt_pk_bf16_f32 v218, v64, v65\n"
"v_mfma_f32_16x16x32_bf16 v[40:43], a[92:95], a[212:215], v[40:43]\n"
"buffer_load_dwordx4 a[36:39], v6, s[40:43], s62 offen offset:0\n"
"v_mfma_f32_16x16x32_bf16 v[84:87], v[44:47], v[208:211], v[84:87]\n"
"ds_read_b128 a[52:55], v17 offset:16384\n"
"v_cvt_pk_bf16_f32 v219, v66, v67\n"
"v_mfma_f32_16x16x32_bf16 v[88:91], v[44:47], v[212:215], v[88:91]\n"
"s_waitcnt lgkmcnt(2)\n"
"v_exp_f32 v68, v68\n"
"v_mfma_f32_16x16x32_bf16 v[48:51], a[216:219], a[144:147], v[28:31]\n"
"v_exp_f32 v69, v69\n"
"v_mfma_f32_16x16x32_bf16 v[48:51], a[220:223], a[148:151], v[48:51]\n"
"v_exp_f32 v70, v70\n"
"v_mfma_f32_16x16x32_bf16 v[48:51], a[224:227], a[152:155], v[48:51]\n"
"ds_read_b128 a[56:59], v16 offset:16512\n"
"v_mfma_f32_16x16x32_bf16 v[48:51], a[228:231], a[156:159], v[48:51]\n"
"ds_read_b128 a[60:63], v17 offset:16512\n"
"v_mfma_f32_16x16x32_bf16 v[48:51], a[232:235], a[160:163], v[48:51]\n"
"ds_read_b128 a[64:67], v16 offset:16640\n"
"v_mfma_f32_16x16x32_bf16 v[48:51], a[236:239], a[164:167], v[48:51]\n"
"ds_read_b128 a[68:71], v17 offset:16640\n"
"v_mfma_f32_16x16x32_bf16 v[52:55], a[216:219], a[168:171], v[32:35]\n"
"ds_read_b128 a[72:75], v18 offset:16384\n"
"v_exp_f32 v71, v71\n"
"v_mfma_f32_16x16x32_bf16 v[52:55], a[220:223], a[172:175], v[52:55]\n"
"ds_read_b128 a[76:79], v19 offset:16384\n"
"v_cvt_pk_bf16_f32 v222, v68, v69\n"
"v_mfma_f32_16x16x32_bf16 v[52:55], a[224:227], a[176:179], v[52:55]\n"
"s_waitcnt vmcnt(7)\n"
"buffer_load_dwordx4 v10, s[36:39], s63 offen offset:2048 nt lds\n"
"ds_read_b128 a[80:83], v18 offset:16512\n"
"v_cvt_pk_bf16_f32 v223, v70, v71\n"
"v_mfma_f32_16x16x32_bf16 v[52:55], a[228:231], a[180:183], v[52:55]\n"
"ds_read_b128 a[84:87], v19 offset:16512\n"
"v_exp_f32 v72, v72\n"
"v_mfma_f32_16x16x32_bf16 v[52:55], a[232:235], a[184:187], v[52:55]\n"
"ds_read_b128 a[88:91], v18 offset:16640\n"
"v_exp_f32 v73, v73\n"
"v_mfma_f32_16x16x32_bf16 v[52:55], a[236:239], a[188:191], v[52:55]\n"
"ds_read_b128 a[92:95], v19 offset:16640\n"
"v_exp_f32 v74, v74\n"
"v_mfma_f32_16x16x32_bf16 v[84:87], v[44:47], v[216:219], v[84:87]\n"
"v_exp_f32 v75, v75\n"
"v_mfma_f32_16x16x32_bf16 v[88:91], v[44:47], v[220:223], v[88:91]\n"
"s_waitcnt lgkmcnt(5)\n"
"v_cvt_pk_bf16_f32 v224, v72, v73\n"
"v_cvt_pk_bf16_f32 v225, v74, v75\n"
"v_mfma_f32_16x16x32_bf16 v[56:59], a[48:51], a[144:147], v[28:31]\n"
"v_exp_f32 v80, v80\n"
"v_mfma_f32_16x16x32_bf16 v[56:59], a[52:55], a[148:151], v[56:59]\n"
"v_exp_f32 v81, v81\n"
"v_mfma_f32_16x16x32_bf16 v[56:59], a[56:59], a[152:155], v[56:59]\n"
"v_exp_f32 v82, v82\n"
"v_mfma_f32_16x16x32_bf16 v[56:59], a[60:63], a[156:159], v[56:59]\n"
"v_exp_f32 v83, v83\n"
"v_mfma_f32_16x16x32_bf16 v[56:59], a[64:67], a[160:163], v[56:59]\n"
"s_waitcnt lgkmcnt(0)\n"
"s_barrier\n"
"v_cvt_pk_bf16_f32 v228, v80, v81\n"
"v_mfma_f32_16x16x32_bf16 v[56:59], a[68:71], a[164:167], v[56:59]\n"
"ds_read_b128 a[216:219], v12 offset:22528\n"
"v_cvt_pk_bf16_f32 v229, v82, v83\n"
"v_mfma_f32_16x16x32_bf16 v[64:67], a[48:51], a[168:171], v[32:35]\n"
"v_exp_f32 v40, v40\n"
"v_mfma_f32_16x16x32_bf16 v[64:67], a[52:55], a[172:175], v[64:67]\n"
"v_exp_f32 v41, v41\n"
"v_mfma_f32_16x16x32_bf16 v[64:67], a[56:59], a[176:179], v[64:67]\n"
"s_waitcnt vmcnt(6)\n"
"buffer_load_dwordx4 v11, s[36:39], s63 offen offset:3072 nt lds\n"
"v_mfma_f32_16x16x32_bf16 v[64:67], a[60:63], a[180:183], v[64:67]\n"
"v_exp_f32 v42, v42\n"
"v_mfma_f32_16x16x32_bf16 v[64:67], a[64:67], a[184:187], v[64:67]\n"
"v_exp_f32 v43, v43\n"
"v_mfma_f32_16x16x32_bf16 v[64:67], a[68:71], a[188:191], v[64:67]\n"
"ds_read_b128 a[220:223], v13 offset:22528\n"
"v_cvt_pk_bf16_f32 v120, v40, v41\n"
"v_mfma_f32_16x16x32_bf16 v[68:71], a[72:75], a[144:147], v[28:31]\n"
"ds_read_b128 a[224:227], v12 offset:22656\n"
"v_cvt_pk_bf16_f32 v121, v42, v43\n"
"v_mfma_f32_16x16x32_bf16 v[68:71], a[76:79], a[148:151], v[68:71]\n"
"v_exp_f32 v48, v48\n"
"v_mfma_f32_16x16x32_bf16 v[68:71], a[80:83], a[152:155], v[68:71]\n"
"ds_read_b128 a[228:231], v13 offset:22656\n"
"v_exp_f32 v49, v49\n"
"v_mfma_f32_16x16x32_bf16 v[68:71], a[84:87], a[156:159], v[68:71]\n"
"ds_read_b128 a[232:235], v12 offset:22784\n"
"v_exp_f32 v50, v50\n"
"v_mfma_f32_16x16x32_bf16 v[68:71], a[88:91], a[160:163], v[68:71]\n"
"ds_read_b128 a[236:239], v13 offset:22784\n"
"v_exp_f32 v51, v51\n"
"v_mfma_f32_16x16x32_bf16 v[68:71], a[92:95], a[164:167], v[68:71]\n"
"s_mov_b32 s63, 98432\n"
"s_add_u32 m0, s53, 36864\n"
"v_cvt_pk_bf16_f32 v232, v48, v49\n"
"v_mfma_f32_16x16x32_bf16 v[72:75], a[72:75], a[168:171], v[32:35]\n"
"ds_read_b128 a[48:51], v14 offset:22528\n"
"v_cvt_pk_bf16_f32 v233, v50, v51\n"
"v_mfma_f32_16x16x32_bf16 v[72:75], a[76:79], a[172:175], v[72:75]\n"
"v_exp_f32 v52, v52\n"
"v_mfma_f32_16x16x32_bf16 v[72:75], a[80:83], a[176:179], v[72:75]\n"
"v_exp_f32 v53, v53\n"
"v_mfma_f32_16x16x32_bf16 v[72:75], a[84:87], a[180:183], v[72:75]\n"
"v_exp_f32 v54, v54\n"
"v_mfma_f32_16x16x32_bf16 v[72:75], a[88:91], a[184:187], v[72:75]\n"
"v_exp_f32 v55, v55\n"
"v_mfma_f32_16x16x32_bf16 v[72:75], a[92:95], a[188:191], v[72:75]\n"
"s_waitcnt lgkmcnt(1)\n"
"s_waitcnt vmcnt(6)\n"
"buffer_load_dwordx4 v8, s[36:39], s63 offen nt lds\n"
"v_cvt_pk_bf16_f32 v236, v52, v53\n"
"v_mfma_f32_16x16x32_bf16 v[80:83], a[216:219], a[144:147], v[28:31]\n"
"ds_read_b128 a[52:55], v15 offset:22528\n"
"v_cvt_pk_bf16_f32 v237, v54, v55\n"
"v_mfma_f32_16x16x32_bf16 v[80:83], a[220:223], a[148:151], v[80:83]\n"
"v_exp_f32 v56, v56\n"
"v_mfma_f32_16x16x32_bf16 v[80:83], a[224:227], a[152:155], v[80:83]\n"
"v_exp_f32 v57, v57\n"
"v_mfma_f32_16x16x32_bf16 v[80:83], a[228:231], a[156:159], v[80:83]\n"
"v_exp_f32 v58, v58\n"
"v_mfma_f32_16x16x32_bf16 v[80:83], a[232:235], a[160:163], v[80:83]\n"
"v_exp_f32 v59, v59\n"
"v_mfma_f32_16x16x32_bf16 v[80:83], a[236:239], a[164:167], v[80:83]\n"
"ds_read_b128 a[56:59], v14 offset:22656\n"
"v_cvt_pk_bf16_f32 v240, v56, v57\n"
"v_mfma_f32_16x16x32_bf16 v[40:43], a[216:219], a[168:171], v[32:35]\n"
"ds_read_b128 a[60:63], v15 offset:22656\n"
"v_cvt_pk_bf16_f32 v241, v58, v59\n"
"v_mfma_f32_16x16x32_bf16 v[40:43], a[220:223], a[172:175], v[40:43]\n"
"v_exp_f32 v64, v64\n"
"v_mfma_f32_16x16x32_bf16 v[40:43], a[224:227], a[176:179], v[40:43]\n"
"v_exp_f32 v65, v65\n"
"v_mfma_f32_16x16x32_bf16 v[40:43], a[228:231], a[180:183], v[40:43]\n"
"ds_read_b128 a[64:67], v14 offset:22784\n"
"v_exp_f32 v66, v66\n"
"v_mfma_f32_16x16x32_bf16 v[40:43], a[232:235], a[184:187], v[40:43]\n"
"ds_read_b128 a[68:71], v15 offset:22784\n"
"v_exp_f32 v67, v67\n"
"v_mfma_f32_16x16x32_bf16 v[40:43], a[236:239], a[188:191], v[40:43]\n"
"ds_read_b128 a[72:75], v16 offset:22528\n"
"v_cvt_pk_bf16_f32 v244, v64, v65\n"
"v_mfma_f32_16x16x32_bf16 v[48:51], a[216:219], a[192:195], v[36:39]\n"
"ds_read_b128 a[76:79], v17 offset:22528\n"
"v_cvt_pk_bf16_f32 v245, v66, v67\n"
"v_mfma_f32_16x16x32_bf16 v[48:51], a[220:223], a[196:199], v[48:51]\n"
"v_exp_f32 v68, v68\n"
"v_mfma_f32_16x16x32_bf16 v[48:51], a[224:227], a[200:203], v[48:51]\n"
"s_waitcnt vmcnt(5)\n"
"buffer_load_dwordx4 v9, s[36:39], s63 offen offset:1024 nt lds\n"
"v_exp_f32 v69, v69\n"
"v_mfma_f32_16x16x32_bf16 v[48:51], a[228:231], a[204:207], v[48:51]\n"
"v_exp_f32 v70, v70\n"
"v_mfma_f32_16x16x32_bf16 v[48:51], a[232:235], a[208:211], v[48:51]\n"
"v_exp_f32 v71, v71\n"
"v_mfma_f32_16x16x32_bf16 v[48:51], a[236:239], a[212:215], v[48:51]\n"
"s_waitcnt lgkmcnt(2)\n"
"ds_read_b128 a[80:83], v16 offset:22656\n"
"v_cvt_pk_bf16_f32 v248, v68, v69\n"
"v_mfma_f32_16x16x32_bf16 v[52:55], a[48:51], a[144:147], v[28:31]\n"
"ds_read_b128 a[84:87], v17 offset:22656\n"
"v_cvt_pk_bf16_f32 v249, v70, v71\n"
"v_mfma_f32_16x16x32_bf16 v[52:55], a[52:55], a[148:151], v[52:55]\n"
"v_exp_f32 v72, v72\n"
"v_mfma_f32_16x16x32_bf16 v[52:55], a[56:59], a[152:155], v[52:55]\n"
"v_exp_f32 v73, v73\n"
"v_mfma_f32_16x16x32_bf16 v[52:55], a[60:63], a[156:159], v[52:55]\n"
"ds_read_b128 a[88:91], v16 offset:22784\n"
"v_exp_f32 v74, v74\n"
"v_mfma_f32_16x16x32_bf16 v[52:55], a[64:67], a[160:163], v[52:55]\n"
"ds_read_b128 a[92:95], v17 offset:22784\n"
"v_exp_f32 v75, v75\n"
"v_mfma_f32_16x16x32_bf16 v[52:55], a[68:71], a[164:167], v[52:55]\n"
"ds_read_b128 a[216:219], v18 offset:22528\n"
"v_cvt_pk_bf16_f32 v252, v72, v73\n"
"v_mfma_f32_16x16x32_bf16 v[56:59], a[48:51], a[168:171], v[32:35]\n"
"ds_read_b128 a[220:223], v19 offset:22528\n"
"v_cvt_pk_bf16_f32 v253, v74, v75\n"
"v_mfma_f32_16x16x32_bf16 v[56:59], a[52:55], a[172:175], v[56:59]\n"
"v_exp_f32 v80, v80\n"
"v_mfma_f32_16x16x32_bf16 v[56:59], a[56:59], a[176:179], v[56:59]\n"
"v_exp_f32 v81, v81\n"
"v_mfma_f32_16x16x32_bf16 v[56:59], a[60:63], a[180:183], v[56:59]\n"
"v_exp_f32 v82, v82\n"
"v_mfma_f32_16x16x32_bf16 v[56:59], a[64:67], a[184:187], v[56:59]\n"
"v_exp_f32 v83, v83\n"
"v_mfma_f32_16x16x32_bf16 v[56:59], a[68:71], a[188:191], v[56:59]\n"
"s_waitcnt lgkmcnt(2)\n"
"s_waitcnt vmcnt(5)\n"
"buffer_load_dwordx4 v10, s[36:39], s63 offen offset:2048 nt lds\n"
"v_cvt_pk_bf16_f32 v226, v80, v81\n"
"v_mfma_f32_16x16x32_bf16 v[64:67], a[72:75], a[144:147], v[28:31]\n"
"ds_read_b128 a[224:227], v18 offset:22656\n"
"v_cvt_pk_bf16_f32 v227, v82, v83\n"
"v_mfma_f32_16x16x32_bf16 v[64:67], a[76:79], a[148:151], v[64:67]\n"
"v_exp_f32 v40, v40\n"
"v_mfma_f32_16x16x32_bf16 v[64:67], a[80:83], a[152:155], v[64:67]\n"
"ds_read_b128 a[228:231], v19 offset:22656\n"
"v_exp_f32 v41, v41\n"
"v_mfma_f32_16x16x32_bf16 v[64:67], a[84:87], a[156:159], v[64:67]\n"
"ds_read_b128 a[232:235], v18 offset:22784\n"
"v_exp_f32 v42, v42\n"
"v_mfma_f32_16x16x32_bf16 v[64:67], a[88:91], a[160:163], v[64:67]\n"
"ds_read_b128 a[236:239], v19 offset:22784\n"
"v_exp_f32 v43, v43\n"
"v_mfma_f32_16x16x32_bf16 v[64:67], a[92:95], a[164:167], v[64:67]\n"
"v_cvt_pk_bf16_f32 v230, v40, v41\n"
"v_cvt_pk_bf16_f32 v231, v42, v43\n"
"v_mfma_f32_16x16x32_bf16 v[68:71], a[72:75], a[168:171], v[32:35]\n"
"v_exp_f32 v48, v48\n"
"v_mfma_f32_16x16x32_bf16 v[68:71], a[76:79], a[172:175], v[68:71]\n"
"v_exp_f32 v49, v49\n"
"v_mfma_f32_16x16x32_bf16 v[68:71], a[80:83], a[176:179], v[68:71]\n"
"v_exp_f32 v50, v50\n"
"v_mfma_f32_16x16x32_bf16 v[68:71], a[84:87], a[180:183], v[68:71]\n"
"v_exp_f32 v51, v51\n"
"v_mfma_f32_16x16x32_bf16 v[68:71], a[88:91], a[184:187], v[68:71]\n"
"v_cvt_pk_bf16_f32 v122, v48, v49\n"
"v_cvt_pk_bf16_f32 v123, v50, v51\n"
"v_mfma_f32_16x16x32_bf16 v[68:71], a[92:95], a[188:191], v[68:71]\n"
"s_waitcnt lgkmcnt(0)\n"
"s_barrier\n"
"ds_read_b64_tr_b16 a[244:245], v96 offset:12288\n"
"v_mfma_f32_16x16x32_bf16 v[84:87], v[44:47], v[224:227], v[84:87]\n"
"v_exp_f32 v52, v52\n"
"v_mfma_f32_16x16x32_bf16 v[88:91], v[44:47], v[228:231], v[88:91]\n"
"v_exp_f32 v53, v53\n"
"v_mfma_f32_16x16x32_bf16 v[92:95], v[44:47], v[120:123], v[92:95]\n"
"s_waitcnt vmcnt(4)\n"
"buffer_load_dwordx4 v11, s[36:39], s63 offen offset:3072 nt lds\n"
"v_mfma_f32_16x16x32_bf16 v[72:75], a[216:219], a[144:147], v[28:31]\n"
"v_exp_f32 v54, v54\n"
"v_mfma_f32_16x16x32_bf16 v[72:75], a[220:223], a[148:151], v[72:75]\n"
"v_exp_f32 v55, v55\n"
"v_mfma_f32_16x16x32_bf16 v[72:75], a[224:227], a[152:155], v[72:75]\n"
"ds_read_b64_tr_b16 a[246:247], v96 offset:14336\n"
"v_cvt_pk_bf16_f32 v234, v52, v53\n"
"v_mfma_f32_16x16x32_bf16 v[72:75], a[228:231], a[156:159], v[72:75]\n"
"ds_read_b64_tr_b16 a[48:49], v97 offset:12288\n"
"v_cvt_pk_bf16_f32 v235, v54, v55\n"
"v_mfma_f32_16x16x32_bf16 v[72:75], a[232:235], a[160:163], v[72:75]\n"
"s_mov_b32 s63, 147584\n"
"v_exp_f32 v56, v56\n"
"v_mfma_f32_16x16x32_bf16 v[72:75], a[236:239], a[164:167], v[72:75]\n"
"ds_read_b64_tr_b16 a[50:51], v97 offset:14336\n"
"v_exp_f32 v57, v57\n"
"v_mfma_f32_16x16x32_bf16 v[80:83], a[216:219], a[168:171], v[32:35]\n"
"ds_read_b64_tr_b16 a[52:53], v98 offset:12288\n"
"v_exp_f32 v58, v58\n"
"v_mfma_f32_16x16x32_bf16 v[80:83], a[220:223], a[172:175], v[80:83]\n"
"ds_read_b64_tr_b16 a[54:55], v98 offset:14336\n"
"v_exp_f32 v59, v59\n"
"v_mfma_f32_16x16x32_bf16 v[80:83], a[224:227], a[176:179], v[80:83]\n"
"s_add_u32 m0, s53, 24576\n"
"ds_read_b64_tr_b16 a[56:57], v99 offset:12288\n"
"v_cvt_pk_bf16_f32 v238, v56, v57\n"
"v_mfma_f32_16x16x32_bf16 v[80:83], a[228:231], a[180:183], v[80:83]\n"
"ds_read_b64_tr_b16 a[58:59], v99 offset:14336\n"
"v_cvt_pk_bf16_f32 v239, v58, v59\n"
"v_mfma_f32_16x16x32_bf16 v[80:83], a[232:235], a[184:187], v[80:83]\n"
"v_exp_f32 v64, v64\n"
"v_mfma_f32_16x16x32_bf16 v[80:83], a[236:239], a[188:191], v[80:83]\n"
"ds_read_b64_tr_b16 a[60:61], v100 offset:12288\n"
"v_exp_f32 v65, v65\n"
"v_mfma_f32_16x16x32_bf16 v[84:87], v[44:47], v[232:235], v[84:87]\n"
"ds_read_b64_tr_b16 a[62:63], v100 offset:14336\n"
"v_exp_f32 v66, v66\n"
"v_mfma_f32_16x16x32_bf16 v[88:91], v[44:47], v[236:239], v[88:91]\n"
"s_waitcnt lgkmcnt(5)\n"
"ds_read_b64_tr_b16 a[64:65], v101 offset:12288\n"
"s_waitcnt vmcnt(4)\n"
"buffer_load_dwordx4 v8, s[36:39], s63 offen nt lds\n"
"v_exp_f32 v67, v67\n"
"v_mfma_f32_16x16x32_bf16 v[28:31], a[244:247], v[128:131], 0\n"
"ds_read_b64_tr_b16 a[66:67], v101 offset:14336\n"
"v_cvt_pk_bf16_f32 v242, v64, v65\n"
"v_mfma_f32_16x16x32_bf16 v[28:31], a[48:51], v[136:139], v[28:31]\n"
"s_waitcnt lgkmcnt(5)\n"
"ds_read_b64_tr_b16 a[68:69], v102 offset:12288\n"
"v_cvt_pk_bf16_f32 v243, v66, v67\n"
"v_mfma_f32_16x16x32_bf16 v[28:31], a[52:55], v[144:147], v[28:31]\n"
"s_waitcnt lgkmcnt(5)\n"
"ds_read_b64_tr_b16 a[70:71], v102 offset:14336\n"
"v_exp_f32 v68, v68\n"
"v_mfma_f32_16x16x32_bf16 v[28:31], a[56:59], v[152:155], v[28:31]\n"
"ds_read_b64_tr_b16 a[72:73], v103 offset:12288\n"
"v_exp_f32 v69, v69\n"
"v_mfma_f32_16x16x32_bf16 v[32:35], a[244:247], v[132:135], 0\n"
"ds_read_b64_tr_b16 a[74:75], v103 offset:14336\n"
"v_exp_f32 v70, v70\n"
"v_mfma_f32_16x16x32_bf16 v[32:35], a[48:51], v[140:143], v[32:35]\n"
"v_exp_f32 v71, v71\n"
"v_mfma_f32_16x16x32_bf16 v[32:35], a[52:55], v[148:151], v[32:35]\n"
"ds_read_b64_tr_b16 a[76:77], v104 offset:12288\n"
"v_cvt_pk_bf16_f32 v246, v68, v69\n"
"v_mfma_f32_16x16x32_bf16 v[32:35], a[56:59], v[156:159], v[32:35]\n"
"ds_read_b64_tr_b16 a[78:79], v104 offset:14336\n"
"v_cvt_pk_bf16_f32 v247, v70, v71\n"
"v_mfma_f32_16x16x32_bf16 a[96:99], a[244:247], v[60:63], 0\n"
"s_waitcnt lgkmcnt(5)\n"
"v_exp_f32 v72, v72\n"
"v_mfma_f32_16x16x32_bf16 v[36:39], a[60:63], v[128:131], 0\n"
"v_exp_f32 v73, v73\n"
"v_mfma_f32_16x16x32_bf16 v[36:39], a[64:67], v[136:139], v[36:39]\n"
"s_waitcnt lgkmcnt(3)\n"
"v_exp_f32 v74, v74\n"
"v_mfma_f32_16x16x32_bf16 v[36:39], a[68:71], v[144:147], v[36:39]\n"
"s_waitcnt lgkmcnt(2)\n"
"v_exp_f32 v75, v75\n"
"v_mfma_f32_16x16x32_bf16 v[36:39], a[72:75], v[152:155], v[36:39]\n"
"ds_read_b64_tr_b16 a[80:81], v105 offset:12288\n"
"v_cvt_pk_bf16_f32 v250, v72, v73\n"
"v_mfma_f32_16x16x32_bf16 v[40:43], a[60:63], v[132:135], 0\n"
"ds_read_b64_tr_b16 a[82:83], v105 offset:14336\n"
"v_cvt_pk_bf16_f32 v251, v74, v75\n"
"v_mfma_f32_16x16x32_bf16 v[40:43], a[64:67], v[140:143], v[40:43]\n"
"s_waitcnt vmcnt(4)\n"
"buffer_load_dwordx4 v9, s[36:39], s63 offen offset:1024 nt lds\n"
"ds_read_b64_tr_b16 a[84:85], v106 offset:12288\n"
"v_exp_f32 v80, v80\n"
"v_mfma_f32_16x16x32_bf16 v[40:43], a[68:71], v[148:151], v[40:43]\n"
"ds_read_b64_tr_b16 a[86:87], v106 offset:14336\n"
"v_exp_f32 v81, v81\n"
"v_mfma_f32_16x16x32_bf16 v[40:43], a[72:75], v[156:159], v[40:43]\n"
"ds_read_b64_tr_b16 a[88:89], v107 offset:12288\n"
"v_exp_f32 v82, v82\n"
"v_mfma_f32_16x16x32_bf16 a[100:103], a[60:63], v[60:63], 0\n"
"ds_read_b64_tr_b16 a[90:91], v107 offset:14336\n"
"v_exp_f32 v83, v83\n"
"v_mfma_f32_16x16x32_bf16 v[84:87], v[44:47], v[240:243], v[84:87]\n"
"ds_read_b64_tr_b16 a[92:93], v108 offset:12288\n"
"v_cvt_pk_bf16_f32 v254, v80, v81\n"
"v_mfma_f32_16x16x32_bf16 v[88:91], v[44:47], v[244:247], v[88:91]\n"
"ds_read_b64_tr_b16 a[94:95], v108 offset:14336\n"
"v_cvt_pk_bf16_f32 v255, v82, v83\n"
"v_mfma_f32_16x16x32_bf16 v[84:87], v[44:47], v[248:251], v[84:87]\n"
"ds_read_b64_tr_b16 a[216:217], v109 offset:12288\n"
"v_accvgpr_write_b32 a212, v120\n"
"v_mfma_f32_16x16x32_bf16 v[88:91], v[44:47], v[252:255], v[88:91]\n"
"s_waitcnt lgkmcnt(6)\n"
"ds_read_b64_tr_b16 a[218:219], v109 offset:14336\n"
"v_accvgpr_write_b32 a213, v121\n"
"v_mfma_f32_16x16x32_bf16 v[44:47], a[76:79], v[128:131], 0\n"
"ds_read_b64_tr_b16 a[220:221], v110 offset:12288\n"
"v_accvgpr_write_b32 a214, v122\n"
"v_mfma_f32_16x16x32_bf16 v[44:47], a[80:83], v[136:139], v[44:47]\n"
"s_waitcnt lgkmcnt(6)\n"
"ds_read_b64_tr_b16 a[222:223], v110 offset:14336\n"
"v_accvgpr_write_b32 a215, v123\n"
"v_mfma_f32_16x16x32_bf16 v[44:47], a[84:87], v[144:147], v[44:47]\n"
"s_waitcnt lgkmcnt(6)\n"
"ds_read_b64_tr_b16 a[224:225], v111 offset:12288\n"
"v_mfma_f32_16x16x32_bf16 v[44:47], a[88:91], v[152:155], v[44:47]\n"
"ds_read_b64_tr_b16 a[226:227], v111 offset:14336\n"
"v_add_f32 v126, v84, v88\n"
"v_mfma_f32_16x16x32_bf16 v[48:51], a[76:79], v[132:135], 0\n"
"ds_read_b64_tr_b16 a[40:41], v96 offset:0\n"
"v_add_f32 v93, v126, v92\n"
"v_mfma_f32_16x16x32_bf16 v[48:51], a[80:83], v[140:143], v[48:51]\n"
"ds_read_b64_tr_b16 a[42:43], v96 offset:2048\n"
"v_mfma_f32_16x16x32_bf16 v[48:51], a[84:87], v[148:151], v[48:51]\n"
"ds_read_b64_tr_b16 a[44:45], v97 offset:0\n"
"v_mfma_f32_16x16x32_bf16 v[48:51], a[88:91], v[156:159], v[48:51]\n"
"s_waitcnt vmcnt(4)\n"
"buffer_load_dwordx4 v10, s[36:39], s63 offen offset:2048 nt lds\n"
"ds_read_b64_tr_b16 a[46:47], v97 offset:2048\n"
"v_mfma_f32_16x16x32_bf16 a[104:107], a[76:79], v[60:63], 0\n"
"s_waitcnt lgkmcnt(6)\n"
"ds_read_b64_tr_b16 a[128:129], v98 offset:0\n"
"v_mfma_f32_16x16x32_bf16 v[52:55], a[92:95], v[128:131], 0\n"
"ds_read_b64_tr_b16 a[130:131], v98 offset:2048\n"
"v_mfma_f32_16x16x32_bf16 v[52:55], a[216:219], v[136:139], v[52:55]\n"
"ds_read_b64_tr_b16 a[132:133], v99 offset:0\n"
"v_mfma_f32_16x16x32_bf16 v[52:55], a[220:223], v[144:147], v[52:55]\n"
"s_waitcnt lgkmcnt(6)\n"
"ds_read_b64_tr_b16 a[134:135], v99 offset:2048\n"
"v_mfma_f32_16x16x32_bf16 v[52:55], a[224:227], v[152:155], v[52:55]\n"
"ds_read_b64_tr_b16 a[136:137], v100 offset:0\n"
"v_mfma_f32_16x16x32_bf16 v[56:59], a[92:95], v[132:135], 0\n"
"ds_read_b64_tr_b16 a[138:139], v100 offset:2048\n"
"v_mfma_f32_16x16x32_bf16 v[56:59], a[216:219], v[140:143], v[56:59]\n"
"ds_read_b64_tr_b16 a[140:141], v101 offset:0\n"
"v_mfma_f32_16x16x32_bf16 v[56:59], a[220:223], v[148:151], v[56:59]\n"
"ds_read_b64_tr_b16 a[142:143], v101 offset:2048\n"
"v_mfma_f32_16x16x32_bf16 v[56:59], a[224:227], v[156:159], v[56:59]\n"
"ds_read_b64_tr_b16 a[192:193], v102 offset:0\n"
"v_mfma_f32_16x16x32_bf16 a[108:111], a[92:95], v[60:63], 0\n"
"s_waitcnt lgkmcnt(6)\n"
"ds_read_b64_tr_b16 a[194:195], v102 offset:2048\n"
"v_mfma_f32_16x16x32_bf16 v[28:31], a[40:43], v[160:163], v[28:31]\n"
"ds_read_b64_tr_b16 a[196:197], v103 offset:0\n"
"v_mfma_f32_16x16x32_bf16 v[28:31], a[44:47], v[168:171], v[28:31]\n"
"ds_read_b64_tr_b16 a[198:199], v103 offset:2048\n"
"v_mfma_f32_16x16x32_bf16 v[28:31], a[128:131], v[176:179], v[28:31]\n"
"s_waitcnt lgkmcnt(6)\n"
"ds_read_b64_tr_b16 a[144:145], v104 offset:0\n"
"v_mfma_f32_16x16x32_bf16 v[28:31], a[132:135], v[184:187], v[28:31]\n"
"ds_read_b64_tr_b16 a[146:147], v104 offset:2048\n"
"v_mfma_f32_16x16x32_bf16 v[32:35], a[40:43], v[164:167], v[32:35]\n"
"s_waitcnt vmcnt(4)\n"
"buffer_load_dwordx4 v11, s[36:39], s63 offen offset:3072 nt lds\n"
"ds_read_b64_tr_b16 a[148:149], v105 offset:0\n"
"v_mfma_f32_16x16x32_bf16 v[32:35], a[44:47], v[172:175], v[32:35]\n"
"ds_read_b64_tr_b16 a[150:151], v105 offset:2048\n"
"v_mfma_f32_16x16x32_bf16 v[32:35], a[128:131], v[180:183], v[32:35]\n"
"ds_read_b64_tr_b16 a[152:153], v106 offset:0\n"
"v_mfma_f32_16x16x32_bf16 v[32:35], a[132:135], v[188:191], v[32:35]\n"
"ds_read_b64_tr_b16 a[154:155], v106 offset:2048\n"
"v_mfma_f32_16x16x32_bf16 a[96:99], a[40:43], v[112:115], a[96:99]\n"
"ds_read_b64_tr_b16 a[156:157], v107 offset:0\n"
"v_mfma_f32_16x16x32_bf16 v[36:39], a[136:139], v[160:163], v[36:39]\n"
"s_waitcnt lgkmcnt(6)\n"
"ds_read_b64_tr_b16 a[158:159], v107 offset:2048\n"
"v_mfma_f32_16x16x32_bf16 v[36:39], a[140:143], v[168:171], v[36:39]\n"
"ds_read_b64_tr_b16 a[160:161], v108 offset:0\n"
"ds_write_b64 v26, v[92:93] offset:20480\n"
"v_mfma_f32_16x16x32_bf16 v[36:39], a[192:195], v[176:179], v[36:39]\n"
"s_mov_b32 s63, 256\n"
"ds_read_b64_tr_b16 a[162:163], v108 offset:2048\n"
"s_add_u32 m0, s53, 12288\n"
"v_mfma_f32_16x16x32_bf16 v[36:39], a[196:199], v[184:187], v[36:39]\n"
"ds_read_b64_tr_b16 a[164:165], v109 offset:0\n"
"v_mfma_f32_16x16x32_bf16 v[40:43], a[136:139], v[164:167], v[40:43]\n"
"ds_read_b64_tr_b16 a[166:167], v109 offset:2048\n"
"v_mfma_f32_16x16x32_bf16 v[40:43], a[140:143], v[172:175], v[40:43]\n"
"ds_read_b64_tr_b16 a[168:169], v110 offset:0\n"
"v_mfma_f32_16x16x32_bf16 v[40:43], a[192:195], v[180:183], v[40:43]\n"
"ds_read_b64_tr_b16 a[170:171], v110 offset:2048\n"
"v_mfma_f32_16x16x32_bf16 v[40:43], a[196:199], v[188:191], v[40:43]\n"
"ds_read_b64_tr_b16 a[172:173], v111 offset:0\n"
"v_mfma_f32_16x16x32_bf16 a[100:103], a[136:139], v[112:115], a[100:103]\n"
"s_waitcnt lgkmcnt(6)\n"
"ds_read_b64_tr_b16 a[174:175], v111 offset:2048\n"
"v_mfma_f32_16x16x32_bf16 v[44:47], a[144:147], v[160:163], v[44:47]\n"
"s_barrier\n"
"ds_read_b64_tr_b16 a[228:229], v96 offset:8192\n"
"v_mfma_f32_16x16x32_bf16 v[44:47], a[148:151], v[168:171], v[44:47]\n"
"s_waitcnt vmcnt(4)\n"
"buffer_load_dwordx4 v8, s[36:39], s63 offen nt lds\n"
"v_mfma_f32_16x16x32_bf16 v[44:47], a[152:155], v[176:179], v[44:47]\n"
"ds_read_b64_tr_b16 a[230:231], v96 offset:10240\n"
"v_mfma_f32_16x16x32_bf16 v[44:47], a[156:159], v[184:187], v[44:47]\n"
"ds_read_b64_tr_b16 a[232:233], v97 offset:8192\n"
"v_mfma_f32_16x16x32_bf16 v[48:51], a[144:147], v[164:167], v[48:51]\n"
"ds_read_b64_tr_b16 a[234:235], v97 offset:10240\n"
"v_mfma_f32_16x16x32_bf16 v[48:51], a[148:151], v[172:175], v[48:51]\n"
"ds_read_b64 v[12:13], v1 offset:20480\n"
"v_mfma_f32_16x16x32_bf16 v[48:51], a[152:155], v[180:183], v[48:51]\n"
"ds_read_b64_tr_b16 a[236:237], v98 offset:8192\n"
"v_mfma_f32_16x16x32_bf16 v[48:51], a[156:159], v[188:191], v[48:51]\n"
"ds_read_b64 v[14:15], v1 offset:61440\n"
"v_mfma_f32_16x16x32_bf16 a[104:107], a[144:147], v[112:115], a[104:107]\n"
"s_waitcnt lgkmcnt(6)\n"
"ds_read_b64_tr_b16 a[238:239], v98 offset:10240\n"
"v_mfma_f32_16x16x32_bf16 v[52:55], a[160:163], v[160:163], v[52:55]\n"
"ds_read_b64 v[16:17], v25 offset:20480\n"
"ds_read_b64 v[18:19], v25 offset:61440\n"
"v_mfma_f32_16x16x32_bf16 v[52:55], a[164:167], v[168:171], v[52:55]\n"
"ds_read_b64_tr_b16 a[48:49], v99 offset:8192\n"
"v_mfma_f32_16x16x32_bf16 v[52:55], a[168:171], v[176:179], v[52:55]\n"
"ds_read_b64_tr_b16 a[50:51], v99 offset:10240\n"
"v_mfma_f32_16x16x32_bf16 v[52:55], a[172:175], v[184:187], v[52:55]\n"
"ds_read_b64_tr_b16 a[52:53], v100 offset:8192\n"
"v_mfma_f32_16x16x32_bf16 v[56:59], a[160:163], v[164:167], v[56:59]\n"
"ds_read_b64_tr_b16 a[54:55], v100 offset:10240\n"
"v_mfma_f32_16x16x32_bf16 v[56:59], a[164:167], v[172:175], v[56:59]\n"
"ds_read_b64_tr_b16 a[56:57], v101 offset:8192\n"
"v_mfma_f32_16x16x32_bf16 v[56:59], a[168:171], v[180:183], v[56:59]\n"
"ds_read_b64_tr_b16 a[58:59], v101 offset:10240\n"
"v_mfma_f32_16x16x32_bf16 v[56:59], a[172:175], v[188:191], v[56:59]\n"
"s_waitcnt vmcnt(4)\n"
"buffer_load_dwordx4 v9, s[36:39], s63 offen offset:1024 nt lds\n"
"ds_read_b64_tr_b16 a[244:245], v102 offset:8192\n"
"v_mfma_f32_16x16x32_bf16 a[108:111], a[160:163], v[112:115], a[108:111]\n"
"s_waitcnt lgkmcnt(6)\n"
"ds_read_b64_tr_b16 a[246:247], v102 offset:10240\n"
"v_mfma_f32_16x16x32_bf16 v[28:31], a[228:231], v[192:195], v[28:31]\n"
"ds_read_b64_tr_b16 a[64:65], v103 offset:8192\n"
"v_rcp_f32 v22, v84\n"
"v_mfma_f32_16x16x32_bf16 v[28:31], a[232:235], v[200:203], v[28:31]\n"
"ds_read_b64_tr_b16 a[66:67], v103 offset:10240\n"
"v_rcp_f32 v23, v88\n"
"v_mfma_f32_16x16x32_bf16 v[28:31], a[236:239], v[208:211], v[28:31]\n"
"s_waitcnt lgkmcnt(6)\n"
"ds_read_b64_tr_b16 a[68:69], v104 offset:8192\n"
"v_mfma_f32_16x16x32_bf16 v[28:31], a[48:51], v[216:219], v[28:31]\n"
"ds_read_b64_tr_b16 a[70:71], v104 offset:10240\n"
"v_add_f32 v27, v12, v14\n"
"v_mfma_f32_16x16x32_bf16 v[32:35], a[228:231], v[196:199], v[32:35]\n"
"ds_read_b64_tr_b16 a[72:73], v105 offset:8192\n"
"v_add_f32 v126, v16, v18\n"
"v_mfma_f32_16x16x32_bf16 v[32:35], a[232:235], v[204:207], v[32:35]\n"
"ds_read_b64_tr_b16 a[74:75], v105 offset:10240\n"
"v_add_f32 v27, v27, v126\n"
"v_mfma_f32_16x16x32_bf16 v[32:35], a[236:239], v[212:215], v[32:35]\n"
"ds_read_b64_tr_b16 a[60:61], v106 offset:8192\n"
"v_mfma_f32_16x16x32_bf16 v[32:35], a[48:51], v[220:223], v[32:35]\n"
"ds_read_b64_tr_b16 a[62:63], v106 offset:10240\n"
"v_mfma_f32_16x16x32_bf16 a[96:99], a[228:231], v[116:119], a[96:99]\n"
"ds_read_b64_tr_b16 a[80:81], v107 offset:8192\n"
"v_mfma_f32_16x16x32_bf16 v[36:39], a[52:55], v[192:195], v[36:39]\n"
"s_waitcnt lgkmcnt(6)\n"
"ds_read_b64_tr_b16 a[82:83], v107 offset:10240\n"
"v_mfma_f32_16x16x32_bf16 v[36:39], a[56:59], v[200:203], v[36:39]\n"
"ds_read_b64_tr_b16 a[84:85], v108 offset:8192\n"
"v_mfma_f32_16x16x32_bf16 v[36:39], a[244:247], v[208:211], v[36:39]\n"
"ds_read_b64_tr_b16 a[86:87], v108 offset:10240\n"
"v_rcp_f32 v24, v27\n"
"v_mfma_f32_16x16x32_bf16 v[36:39], a[64:67], v[216:219], v[36:39]\n"
"ds_read_b64_tr_b16 a[88:89], v109 offset:8192\n"
"v_add_f32 v27, v13, v15\n"
"v_add_f32 v126, v17, v19\n"
"v_mfma_f32_16x16x32_bf16 v[40:43], a[52:55], v[196:199], v[40:43]\n"
"s_waitcnt vmcnt(4)\n"
"buffer_load_dwordx4 v10, s[36:39], s63 offen offset:2048 nt lds\n"
"ds_read_b64_tr_b16 a[90:91], v109 offset:10240\n"
"v_add_f32 v27, v27, v126\n"
"v_cmp_ngt_f32 vcc, 0x4e800000, v27\n"
"v_mfma_f32_16x16x32_bf16 v[40:43], a[56:59], v[204:207], v[40:43]\n"
"ds_read_b64_tr_b16 a[76:77], v110 offset:8192\n"
"v_mfma_f32_16x16x32_bf16 v[40:43], a[244:247], v[212:215], v[40:43]\n"
"ds_read_b64_tr_b16 a[78:79], v110 offset:10240\n"
"v_mfma_f32_16x16x32_bf16 v[40:43], a[64:67], v[220:223], v[40:43]\n"
"ds_read_b64_tr_b16 a[216:217], v111 offset:8192\n"
"v_mfma_f32_16x16x32_bf16 a[100:103], a[52:55], v[116:119], a[100:103]\n"
"ds_read_b64_tr_b16 a[218:219], v111 offset:10240\n"
"s_cbranch_vccnz .Lfb_%=\n"
".Lfbret_%=:\n"
"s_waitcnt lgkmcnt(7)\n"
"v_mfma_f32_16x16x32_bf16 v[44:47], a[68:71], v[192:195], v[44:47]\n"
"s_barrier\n"
"s_mul_i32 s61, s52, 3584\n"
"v_mfma_f32_16x16x32_bf16 v[44:47], a[72:75], v[200:203], v[44:47]\n"
"s_sub_u32 s61, 16384, s61\n"
"ds_read_b64_tr_b16 a[220:221], v96 offset:4096\n"
"v_lshlrev_b32 v122, 3, v0\n"
"v_mfma_f32_16x16x32_bf16 v[44:47], a[60:63], v[208:211], v[44:47]\n"
"ds_read_b64_tr_b16 a[222:223], v96 offset:6144\n"
"v_mfma_f32_16x16x32_bf16 v[44:47], a[80:83], v[216:219], v[44:47]\n"
"ds_read_b64_tr_b16 a[224:225], v97 offset:4096\n"
"v_mfma_f32_16x16x32_bf16 v[48:51], a[68:71], v[196:199], v[48:51]\n"
"ds_read_b64_tr_b16 a[226:227], v97 offset:6144\n"
"v_mfma_f32_16x16x32_bf16 v[48:51], a[72:75], v[204:207], v[48:51]\n"
"ds_read_b64_tr_b16 a[92:93], v98 offset:4096\n"
"v_mfma_f32_16x16x32_bf16 v[48:51], a[60:63], v[212:215], v[48:51]\n"
"ds_read_b64_tr_b16 a[94:95], v98 offset:6144\n"
"v_mfma_f32_16x16x32_bf16 v[48:51], a[80:83], v[220:223], v[48:51]\n"
"ds_read_b64_tr_b16 a[44:45], v99 offset:4096\n"
"v_mfma_f32_16x16x32_bf16 a[104:107], a[68:71], v[116:119], a[104:107]\n"
"s_waitcnt lgkmcnt(6)\n"
"ds_read_b64_tr_b16 a[46:47], v99 offset:6144\n"
".Lpad12_%=:\n"
"s_cbranch_execz .Lwb_%=\n"
"v_mfma_f32_16x16x32_bf16 v[52:55], a[84:87], v[192:195], v[52:55]\n"
"s_waitcnt vmcnt(4)\n"
"buffer_load_dwordx4 v11, s[36:39], s63 offen offset:3072 nt lds\n"
"v_mfma_f32_16x16x32_bf16 v[52:55], a[88:91], v[200:203], v[52:55]\n"
"ds_read_b64_tr_b16 a[128:129], v100 offset:4096\n"
"v_mfma_f32_16x16x32_bf16 v[52:55], a[76:79], v[208:211], v[52:55]\n"
"ds_read_b64_tr_b16 a[130:131], v100 offset:6144\n"
"v_mfma_f32_16x16x32_bf16 v[52:55], a[216:219], v[216:219], v[52:55]\n"
"s_lshl_b32 s19, s52, 10\n"
"ds_read_b64_tr_b16 a[132:133], v101 offset:4096\n"
"v_add_u32 v20, s19, v1\n"
"v_mfma_f32_16x16x32_bf16 v[56:59], a[84:87], v[196:199], v[56:59]\n"
"ds_read_b64_tr_b16 a[134:135], v101 offset:6144\n"
"v_add_u32 v21, 81920, v20\n"
"v_mfma_f32_16x16x32_bf16 v[56:59], a[88:91], v[204:207], v[56:59]\n"
"s_mov_b32 s63, 49408\n"
"ds_read_b64_tr_b16 a[40:41], v102 offset:4096\n"
"s_add_u32 m0, s53, 0\n"
"v_mfma_f32_16x16x32_bf16 v[56:59], a[76:79], v[212:215], v[56:59]\n"
"ds_read_b64_tr_b16 a[42:43], v102 offset:6144\n"
"v_mfma_f32_16x16x32_bf16 v[56:59], a[216:219], v[220:223], v[56:59]\n"
"ds_read_b64_tr_b16 a[140:141], v103 offset:4096\n"
"v_mfma_f32_16x16x32_bf16 a[108:111], a[84:87], v[116:119], a[108:111]\n"
"s_waitcnt lgkmcnt(6)\n"
"ds_read_b64_tr_b16 a[142:143], v103 offset:6144\n"
"v_mfma_f32_16x16x32_bf16 v[28:31], a[220:223], v[224:227], v[28:31]\n"
"ds_read_b64_tr_b16 a[192:193], v104 offset:4096\n"
"v_mfma_f32_16x16x32_bf16 v[28:31], a[224:227], v[232:235], v[28:31]\n"
"ds_read_b64_tr_b16 a[194:195], v104 offset:6144\n"
"v_mfma_f32_16x16x32_bf16 v[28:31], a[92:95], v[240:243], v[28:31]\n"
"ds_read_b64_tr_b16 a[196:197], v105 offset:4096\n"
"v_mfma_f32_16x16x32_bf16 v[28:31], a[44:47], v[248:251], v[28:31]\n"
"ds_read_b64_tr_b16 a[198:199], v105 offset:6144\n"
"v_mfma_f32_16x16x32_bf16 v[32:35], a[220:223], v[228:231], v[32:35]\n"
"s_waitcnt vmcnt(4)\n"
"buffer_load_dwordx4 v8, s[36:39], s63 offen nt lds\n"
"v_mfma_f32_16x16x32_bf16 v[32:35], a[224:227], v[236:239], v[32:35]\n"
"ds_read_b64_tr_b16 a[136:137], v106 offset:4096\n"
"v_mfma_f32_16x16x32_bf16 v[32:35], a[92:95], v[244:247], v[32:35]\n"
"ds_read_b64_tr_b16 a[138:139], v106 offset:6144\n"
"v_mfma_f32_16x16x32_bf16 v[32:35], a[44:47], v[252:255], v[32:35]\n"
"ds_read_b64_tr_b16 a[148:149], v107 offset:4096\n"
"v_mul_f32 v28, v22, v28\n"
"v_mfma_f32_16x16x32_bf16 a[96:99], a[220:223], a[212:215], a[96:99]\n"
"s_waitcnt lgkmcnt(5)\n"
"ds_read_b64_tr_b16 a[150:151], v107 offset:6144\n"
"v_mul_f32 v29, v22, v29\n"
"v_mfma_f32_16x16x32_bf16 v[36:39], a[128:131], v[224:227], v[36:39]\n"
"ds_read_b64_tr_b16 a[152:153], v108 offset:4096\n"
"v_mul_f32 v30, v22, v30\n"
"v_mfma_f32_16x16x32_bf16 v[36:39], a[132:135], v[232:235], v[36:39]\n"
"ds_read_b64_tr_b16 a[154:155], v108 offset:6144\n"
"v_mul_f32 v31, v22, v31\n"
"v_mfma_f32_16x16x32_bf16 v[36:39], a[40:43], v[240:243], v[36:39]\n"
"ds_read_b64_tr_b16 a[156:157], v109 offset:4096\n"
"v_cvt_pk_bf16_f32 v64, v28, v29\n"
"v_mfma_f32_16x16x32_bf16 v[36:39], a[140:143], v[248:251], v[36:39]\n"
"ds_read_b64_tr_b16 a[158:159], v109 offset:6144\n"
"v_cvt_pk_bf16_f32 v65, v30, v31\n"
"v_mfma_f32_16x16x32_bf16 v[40:43], a[128:131], v[228:231], v[40:43]\n"
"ds_read_b64_tr_b16 a[144:145], v110 offset:4096\n"
"v_mul_f32 v32, v23, v32\n"
"v_mfma_f32_16x16x32_bf16 v[40:43], a[132:135], v[236:239], v[40:43]\n"
"ds_read_b64_tr_b16 a[146:147], v110 offset:6144\n"
"v_mul_f32 v33, v23, v33\n"
"v_mfma_f32_16x16x32_bf16 v[40:43], a[40:43], v[244:247], v[40:43]\n"
"ds_read_b64_tr_b16 a[164:165], v111 offset:4096\n"
"v_mul_f32 v34, v23, v34\n"
"v_mfma_f32_16x16x32_bf16 v[40:43], a[140:143], v[252:255], v[40:43]\n"
"ds_read_b64_tr_b16 a[166:167], v111 offset:6144\n"
"v_mul_f32 v35, v23, v35\n"
"v_mfma_f32_16x16x32_bf16 a[100:103], a[128:131], a[212:215], a[100:103]\n"
"s_waitcnt vmcnt(4)\n"
"buffer_load_dwordx4 v9, s[36:39], s63 offen offset:1024 nt lds\n"
"v_mfma_f32_16x16x32_bf16 v[44:47], a[192:195], v[224:227], v[44:47]\n"
"s_waitcnt lgkmcnt(5)\n"
"s_barrier\n"
"v_mfma_f32_16x16x32_bf16 v[44:47], a[196:199], v[232:235], v[44:47]\n"
"ds_write_b64 v26, a[96:97] offset:16384\n"
"v_mfma_f32_16x16x32_bf16 v[44:47], a[136:139], v[240:243], v[44:47]\n"
"ds_write_b64 v26, a[98:99] offset:16392\n"
"v_mfma_f32_16x16x32_bf16 v[44:47], a[148:151], v[248:251], v[44:47]\n"
"ds_read_b64_tr_b16 a[168:169], v96 offset:28672\n"
"v_cvt_pk_bf16_f32 v72, v32, v33\n"
"v_mfma_f32_16x16x32_bf16 v[48:51], a[192:195], v[228:231], v[48:51]\n"
"ds_read_b64_tr_b16 a[170:171], v96 offset:30720\n"
"v_cvt_pk_bf16_f32 v73, v34, v35\n"
"v_mfma_f32_16x16x32_bf16 v[48:51], a[196:199], v[236:239], v[48:51]\n"
"ds_read_b64_tr_b16 a[172:173], v97 offset:28672\n"
"v_mul_f32 v36, v22, v36\n"
"v_mfma_f32_16x16x32_bf16 v[48:51], a[136:139], v[244:247], v[48:51]\n"
"ds_read_b64_tr_b16 a[174:175], v97 offset:30720\n"
"v_mul_f32 v37, v22, v37\n"
"v_mfma_f32_16x16x32_bf16 v[48:51], a[148:151], v[252:255], v[48:51]\n"
"ds_read_b64_tr_b16 a[160:161], v98 offset:28672\n"
"v_mul_f32 v38, v22, v38\n"
"v_mfma_f32_16x16x32_bf16 a[104:107], a[192:195], a[212:215], a[104:107]\n"
"ds_read_b64_tr_b16 a[162:163], v98 offset:30720\n"
"v_mul_f32 v39, v22, v39\n"
"v_mfma_f32_16x16x32_bf16 v[52:55], a[152:155], v[224:227], v[52:55]\n"
"s_waitcnt lgkmcnt(6)\n"
"ds_write_b64 v26, a[100:101] offset:17408\n"
"v_mfma_f32_16x16x32_bf16 v[52:55], a[156:159], v[232:235], v[52:55]\n"
"ds_read_b64_tr_b16 a[232:233], v99 offset:28672\n"
"v_cvt_pk_bf16_f32 v66, v36, v37\n"
"v_mfma_f32_16x16x32_bf16 v[52:55], a[144:147], v[240:243], v[52:55]\n"
"ds_write_b64 v26, a[102:103] offset:17416\n"
"v_mfma_f32_16x16x32_bf16 v[52:55], a[164:167], v[248:251], v[52:55]\n"
"s_waitcnt vmcnt(4)\n"
"buffer_load_dwordx4 v10, s[36:39], s63 offen offset:2048 nt lds\n"
"v_mfma_f32_16x16x32_bf16 v[56:59], a[152:155], v[228:231], v[56:59]\n"
"ds_read_b64_tr_b16 a[234:235], v99 offset:30720\n"
"v_cvt_pk_bf16_f32 v67, v38, v39\n"
"v_mfma_f32_16x16x32_bf16 v[56:59], a[156:159], v[236:239], v[56:59]\n"
"ds_read_b64_tr_b16 a[236:237], v100 offset:28672\n"
"v_mul_f32 v40, v23, v40\n"
"v_mfma_f32_16x16x32_bf16 v[56:59], a[144:147], v[244:247], v[56:59]\n"
"ds_read_b64_tr_b16 a[238:239], v100 offset:30720\n"
"v_mul_f32 v41, v23, v41\n"
"v_mfma_f32_16x16x32_bf16 v[56:59], a[164:167], v[252:255], v[56:59]\n"
"ds_read_b64_tr_b16 a[48:49], v101 offset:28672\n"
"v_mul_f32 v42, v23, v42\n"
"v_mfma_f32_16x16x32_bf16 a[108:111], a[152:155], a[212:215], a[108:111]\n"
"s_waitcnt lgkmcnt(5)\n"
"ds_read_b64_tr_b16 a[50:51], v101 offset:30720\n"
"v_mul_f32 v43, v23, v43\n"
"v_mfma_f32_16x16x32_bf16 v[28:31], a[168:171], v[128:131], 0\n"
"ds_read_b64_tr_b16 a[228:229], v102 offset:28672\n"
"v_cvt_pk_bf16_f32 v74, v40, v41\n"
"v_mfma_f32_16x16x32_bf16 v[28:31], a[172:175], v[136:139], v[28:31]\n"
"ds_read_b64_tr_b16 a[230:231], v102 offset:30720\n"
"v_cvt_pk_bf16_f32 v75, v42, v43\n"
"v_mfma_f32_16x16x32_bf16 v[28:31], a[160:163], v[144:147], v[28:31]\n"
"s_waitcnt lgkmcnt(6)\n"
"ds_write_b64 v26, a[104:105] offset:18432\n"
"v_mfma_f32_16x16x32_bf16 v[28:31], a[232:235], v[152:155], v[28:31]\n"
"ds_read_b64_tr_b16 a[56:57], v103 offset:28672\n"
"ds_write_b64 v26, a[106:107] offset:18440\n"
"v_mfma_f32_16x16x32_bf16 v[32:35], a[168:171], v[132:135], 0\n"
"ds_read_b64_tr_b16 a[58:59], v103 offset:30720\n"
"v_mul_f32 v44, v22, v44\n"
"v_mfma_f32_16x16x32_bf16 v[32:35], a[172:175], v[140:143], v[32:35]\n"
"ds_read_b64_tr_b16 a[244:245], v104 offset:28672\n"
"v_mul_f32 v45, v22, v45\n"
"v_mfma_f32_16x16x32_bf16 v[32:35], a[160:163], v[148:151], v[32:35]\n"
"ds_read_b64_tr_b16 a[246:247], v104 offset:30720\n"
"v_mul_f32 v46, v22, v46\n"
"v_mfma_f32_16x16x32_bf16 v[32:35], a[232:235], v[156:159], v[32:35]\n"
"s_waitcnt vmcnt(4)\n"
"buffer_load_dwordx4 v11, s[36:39], s63 offen offset:3072 nt lds\n"
"v_mfma_f32_16x16x32_bf16 a[96:99], a[168:171], v[60:63], 0\n"
"s_waitcnt lgkmcnt(6)\n"
"ds_read_b64_tr_b16 a[64:65], v105 offset:28672\n"
"v_mul_f32 v47, v22, v47\n"
"v_mfma_f32_16x16x32_bf16 v[36:39], a[236:239], v[128:131], 0\n"
"ds_read_b64_tr_b16 a[66:67], v105 offset:30720\n"
"v_cvt_pk_bf16_f32 v68, v44, v45\n"
"v_mfma_f32_16x16x32_bf16 v[36:39], a[48:51], v[136:139], v[36:39]\n"
"ds_read_b64_tr_b16 a[52:53], v106 offset:28672\n"
"v_cvt_pk_bf16_f32 v69, v46, v47\n"
"v_mfma_f32_16x16x32_bf16 v[36:39], a[228:231], v[144:147], v[36:39]\n"
"s_waitcnt lgkmcnt(5)\n"
"ds_read_b64_tr_b16 a[54:55], v106 offset:30720\n"
"ds_write_b64 v26, a[108:109] offset:19456\n"
"v_mfma_f32_16x16x32_bf16 v[36:39], a[56:59], v[152:155], v[36:39]\n"
"ds_read_b64_tr_b16 a[72:73], v107 offset:28672\n"
"ds_write_b64 v26, a[110:111] offset:19464\n"
"v_mfma_f32_16x16x32_bf16 v[40:43], a[236:239], v[132:135], 0\n"
"s_mov_b32 s63, 98560\n"
"ds_read_b64_tr_b16 a[74:75], v107 offset:30720\n"
"v_mul_f32 v48, v23, v48\n"
"v_mfma_f32_16x16x32_bf16 v[40:43], a[48:51], v[140:143], v[40:43]\n"
"s_add_u32 m0, s53, 8192\n"
"ds_read_b64_tr_b16 a[60:61], v108 offset:28672\n"
"v_mul_f32 v49, v23, v49\n"
"v_mfma_f32_16x16x32_bf16 v[40:43], a[228:231], v[148:151], v[40:43]\n"
"ds_read_b64_tr_b16 a[62:63], v108 offset:30720\n"
"v_mul_f32 v50, v23, v50\n"
"v_mfma_f32_16x16x32_bf16 v[40:43], a[56:59], v[156:159], v[40:43]\n"
"ds_read_b64_tr_b16 a[80:81], v109 offset:28672\n"
"v_mul_f32 v51, v23, v51\n"
"v_mfma_f32_16x16x32_bf16 a[100:103], a[236:239], v[60:63], 0\n"
"s_waitcnt lgkmcnt(8)\n"
"ds_read_b64_tr_b16 a[82:83], v109 offset:30720\n"
"v_cvt_pk_bf16_f32 v76, v48, v49\n"
"v_cvt_pk_bf16_f32 v77, v50, v51\n"
"v_mfma_f32_16x16x32_bf16 v[44:47], a[244:247], v[128:131], 0\n"
"ds_read_b64_tr_b16 a[68:69], v110 offset:28672\n"
"v_mul_f32 v52, v22, v52\n"
"v_mfma_f32_16x16x32_bf16 v[44:47], a[64:67], v[136:139], v[44:47]\n"
"s_waitcnt lgkmcnt(6)\n"
"ds_read_b64_tr_b16 a[70:71], v110 offset:30720\n"
"v_mul_f32 v53, v22, v53\n"
"v_mfma_f32_16x16x32_bf16 v[44:47], a[52:55], v[144:147], v[44:47]\n"
"s_waitcnt lgkmcnt(6)\n"
"ds_read_b64_tr_b16 a[88:89], v111 offset:28672\n"
"v_mul_f32 v54, v22, v54\n"
"v_mfma_f32_16x16x32_bf16 v[44:47], a[72:75], v[152:155], v[44:47]\n"
"s_waitcnt vmcnt(4)\n"
"buffer_load_dwordx4 v8, s[36:39], s63 offen nt lds\n"
"ds_read_b64_tr_b16 a[90:91], v111 offset:30720\n"
"v_mul_f32 v55, v22, v55\n"
"v_mfma_f32_16x16x32_bf16 v[48:51], a[244:247], v[132:135], 0\n"
"ds_read_b64_tr_b16 a[76:77], v96 offset:32768\n"
"v_cvt_pk_bf16_f32 v70, v52, v53\n"
"v_cvt_pk_bf16_f32 v71, v54, v55\n"
"v_mfma_f32_16x16x32_bf16 v[48:51], a[64:67], v[140:143], v[48:51]\n"
"ds_read_b64_tr_b16 a[78:79], v96 offset:34816\n"
"v_mul_f32 v56, v23, v56\n"
"v_mfma_f32_16x16x32_bf16 v[48:51], a[52:55], v[148:151], v[48:51]\n"
"ds_read_b64_tr_b16 a[216:217], v97 offset:32768\n"
"v_mul_f32 v57, v23, v57\n"
"v_mul_f32 v58, v23, v58\n"
"v_mfma_f32_16x16x32_bf16 v[48:51], a[72:75], v[156:159], v[48:51]\n"
"ds_read_b64_tr_b16 a[218:219], v97 offset:34816\n"
"v_mul_f32 v59, v23, v59\n"
"v_mfma_f32_16x16x32_bf16 a[104:107], a[244:247], v[60:63], 0\n"
"s_waitcnt lgkmcnt(6)\n"
"ds_read_b64_tr_b16 a[84:85], v98 offset:32768\n"
"v_cvt_pk_bf16_f32 v78, v56, v57\n"
"v_cvt_pk_bf16_f32 v79, v58, v59\n"
"v_mfma_f32_16x16x32_bf16 v[52:55], a[60:63], v[128:131], 0\n"
"ds_read_b64_tr_b16 a[86:87], v98 offset:34816\n"
"v_mfma_f32_16x16x32_bf16 v[52:55], a[80:83], v[136:139], v[52:55]\n"
"ds_read_b64_tr_b16 a[224:225], v99 offset:32768\n"
"v_mfma_f32_16x16x32_bf16 v[52:55], a[68:71], v[144:147], v[52:55]\n"
"s_waitcnt lgkmcnt(6)\n"
"ds_read_b64_tr_b16 a[226:227], v99 offset:34816\n"
"v_mfma_f32_16x16x32_bf16 v[52:55], a[88:91], v[152:155], v[52:55]\n"
"ds_read_b64_tr_b16 a[92:93], v100 offset:32768\n"
"v_mfma_f32_16x16x32_bf16 v[56:59], a[60:63], v[132:135], 0\n"
"ds_read_b64_tr_b16 a[94:95], v100 offset:34816\n"
"v_mfma_f32_16x16x32_bf16 v[56:59], a[80:83], v[140:143], v[56:59]\n"
"ds_read_b64_tr_b16 a[44:45], v101 offset:32768\n"
"v_mfma_f32_16x16x32_bf16 v[56:59], a[68:71], v[148:151], v[56:59]\n"
"ds_read_b64_tr_b16 a[46:47], v101 offset:34816\n"
"v_mfma_f32_16x16x32_bf16 v[56:59], a[88:91], v[156:159], v[56:59]\n"
"s_waitcnt vmcnt(4)\n"
"buffer_load_dwordx4 v9, s[36:39], s63 offen offset:1024 nt lds\n"
"ds_read_b64_tr_b16 a[220:221], v102 offset:32768\n"
"v_mfma_f32_16x16x32_bf16 a[108:111], a[60:63], v[60:63], 0\n"
"s_waitcnt lgkmcnt(6)\n"
"ds_read_b64_tr_b16 a[222:223], v102 offset:34816\n"
"v_mfma_f32_16x16x32_bf16 v[28:31], a[76:79], v[160:163], v[28:31]\n"
"ds_read_b64_tr_b16 a[132:133], v103 offset:32768\n"
"v_mfma_f32_16x16x32_bf16 v[28:31], a[216:219], v[168:171], v[28:31]\n"
"ds_read_b64_tr_b16 a[134:135], v103 offset:34816\n"
"v_mfma_f32_16x16x32_bf16 v[28:31], a[84:87], v[176:179], v[28:31]\n"
"s_waitcnt lgkmcnt(6)\n"
"ds_read_b64_tr_b16 a[40:41], v104 offset:32768\n"
"v_mfma_f32_16x16x32_bf16 v[28:31], a[224:227], v[184:187], v[28:31]\n"
"ds_read_b64_tr_b16 a[42:43], v104 offset:34816\n"
"v_mfma_f32_16x16x32_bf16 v[32:35], a[76:79], v[164:167], v[32:35]\n"
"ds_read_b64_tr_b16 a[140:141], v105 offset:32768\n"
"v_mfma_f32_16x16x32_bf16 v[32:35], a[216:219], v[172:175], v[32:35]\n"
"ds_read_b64_tr_b16 a[142:143], v105 offset:34816\n"
"v_mfma_f32_16x16x32_bf16 v[32:35], a[84:87], v[180:183], v[32:35]\n"
"ds_read_b64_tr_b16 a[128:129], v106 offset:32768\n"
"v_mfma_f32_16x16x32_bf16 v[32:35], a[224:227], v[188:191], v[32:35]\n"
"ds_read_b64_tr_b16 a[130:131], v106 offset:34816\n"
"v_mfma_f32_16x16x32_bf16 a[96:99], a[76:79], v[112:115], a[96:99]\n"
"ds_read_b64_tr_b16 a[196:197], v107 offset:32768\n"
"v_mfma_f32_16x16x32_bf16 v[36:39], a[92:95], v[160:163], v[36:39]\n"
"s_waitcnt lgkmcnt(6)\n"
"ds_read_b64_tr_b16 a[198:199], v107 offset:34816\n"
"v_mfma_f32_16x16x32_bf16 v[36:39], a[44:47], v[168:171], v[36:39]\n"
"ds_read_b64_tr_b16 a[136:137], v108 offset:32768\n"
"v_mfma_f32_16x16x32_bf16 v[36:39], a[220:223], v[176:179], v[36:39]\n"
"s_waitcnt vmcnt(4)\n"
"buffer_load_dwordx4 v10, s[36:39], s63 offen offset:2048 nt lds\n"
"ds_read_b64_tr_b16 a[138:139], v108 offset:34816\n"
"v_mfma_f32_16x16x32_bf16 v[36:39], a[132:135], v[184:187], v[36:39]\n"
"ds_read_b64_tr_b16 a[148:149], v109 offset:32768\n"
"v_mfma_f32_16x16x32_bf16 v[40:43], a[92:95], v[164:167], v[40:43]\n"
"ds_read_b64_tr_b16 a[150:151], v109 offset:34816\n"
"v_mfma_f32_16x16x32_bf16 v[40:43], a[44:47], v[172:175], v[40:43]\n"
"ds_read_b64_tr_b16 a[192:193], v110 offset:32768\n"
"v_mfma_f32_16x16x32_bf16 v[40:43], a[220:223], v[180:183], v[40:43]\n"
"ds_read_b64_tr_b16 a[194:195], v110 offset:34816\n"
"v_mfma_f32_16x16x32_bf16 v[40:43], a[132:135], v[188:191], v[40:43]\n"
"ds_read_b64_tr_b16 a[156:157], v111 offset:32768\n"
"v_mfma_f32_16x16x32_bf16 a[100:103], a[92:95], v[112:115], a[100:103]\n"
"s_waitcnt lgkmcnt(6)\n"
"ds_read_b64_tr_b16 a[158:159], v111 offset:34816\n"
"v_mfma_f32_16x16x32_bf16 v[44:47], a[40:43], v[160:163], v[44:47]\n"
"s_barrier\n"
"ds_read_b64_tr_b16 a[144:145], v96 offset:36864\n"
"v_mfma_f32_16x16x32_bf16 v[44:47], a[140:143], v[168:171], v[44:47]\n"
"ds_read_b64_tr_b16 a[146:147], v96 offset:38912\n"
"v_mfma_f32_16x16x32_bf16 v[44:47], a[128:131], v[176:179], v[44:47]\n"
"ds_read_b128 v[12:15], v20 offset:16384\n"
"v_mfma_f32_16x16x32_bf16 v[44:47], a[196:199], v[184:187], v[44:47]\n"
"ds_read_b128 v[16:19], v20 offset:57344\n"
"v_mfma_f32_16x16x32_bf16 v[48:51], a[40:43], v[164:167], v[48:51]\n"
"ds_read_b64_tr_b16 a[164:165], v97 offset:36864\n"
"v_mfma_f32_16x16x32_bf16 v[48:51], a[140:143], v[172:175], v[48:51]\n"
"s_waitcnt vmcnt(4)\n"
"buffer_load_dwordx4 v11, s[36:39], s63 offen offset:3072 nt lds\n"
"v_mfma_f32_16x16x32_bf16 v[48:51], a[128:131], v[180:183], v[48:51]\n"
"ds_read_b64_tr_b16 a[166:167], v97 offset:38912\n"
"v_mfma_f32_16x16x32_bf16 v[48:51], a[196:199], v[188:191], v[48:51]\n"
"ds_read_b64_tr_b16 a[152:153], v98 offset:36864\n"
"v_mfma_f32_16x16x32_bf16 a[104:107], a[40:43], v[112:115], a[104:107]\n"
"s_waitcnt lgkmcnt(5)\n"
"ds_read_b64_tr_b16 a[154:155], v98 offset:38912\n"
"v_mfma_f32_16x16x32_bf16 v[52:55], a[136:139], v[160:163], v[52:55]\n"
"ds_read_b64_tr_b16 a[172:173], v99 offset:36864\n"
"v_mfma_f32_16x16x32_bf16 v[52:55], a[148:151], v[168:171], v[52:55]\n"
"ds_read_b64_tr_b16 a[174:175], v99 offset:38912\n"
"s_waitcnt lgkmcnt(6)\n"
"v_add_f32 v12, v12, v16\n"
"v_mfma_f32_16x16x32_bf16 v[52:55], a[192:195], v[176:179], v[52:55]\n"
"s_mov_b32 s63, 147712\n"
"ds_read_b64_tr_b16 a[160:161], v100 offset:36864\n"
"v_add_f32 v13, v13, v17\n"
"v_mfma_f32_16x16x32_bf16 v[52:55], a[156:159], v[184:187], v[52:55]\n"
"s_add_u32 m0, s53, 4096\n"
"ds_read_b64_tr_b16 a[162:163], v100 offset:38912\n"
"v_add_f32 v14, v14, v18\n"
"v_mfma_f32_16x16x32_bf16 v[56:59], a[136:139], v[164:167], v[56:59]\n"
"ds_read_b64_tr_b16 a[232:233], v101 offset:36864\n"
"v_add_f32 v15, v15, v19\n"
"v_mfma_f32_16x16x32_bf16 v[56:59], a[148:151], v[172:175], v[56:59]\n"
"ds_read_b64_tr_b16 a[234:235], v101 offset:38912\n"
"v_mfma_f32_16x16x32_bf16 v[56:59], a[192:195], v[180:183], v[56:59]\n"
"ds_read_b64_tr_b16 a[168:169], v102 offset:36864\n"
"v_mfma_f32_16x16x32_bf16 v[56:59], a[156:159], v[188:191], v[56:59]\n"
"ds_read_b128 v[16:19], v21 offset:16384\n"
"v_mfma_f32_16x16x32_bf16 a[108:111], a[136:139], v[112:115], a[108:111]\n"
"ds_read_b64_tr_b16 a[170:171], v102 offset:38912\n"
"v_mfma_f32_16x16x32_bf16 v[28:31], a[144:147], v[192:195], v[28:31]\n"
"s_waitcnt lgkmcnt(6)\n"
"ds_read_b64_tr_b16 a[48:49], v103 offset:36864\n"
"v_mfma_f32_16x16x32_bf16 v[28:31], a[164:167], v[200:203], v[28:31]\n"
"s_waitcnt vmcnt(4)\n"
"buffer_load_dwordx4 v8, s[36:39], s63 offen nt lds\n"
"ds_read_b64_tr_b16 a[50:51], v103 offset:38912\n"
"v_mfma_f32_16x16x32_bf16 v[28:31], a[152:155], v[208:211], v[28:31]\n"
"ds_read_b64_tr_b16 a[228:229], v104 offset:36864\n"
"v_mfma_f32_16x16x32_bf16 v[28:31], a[172:175], v[216:219], v[28:31]\n"
"ds_read_b64_tr_b16 a[230:231], v104 offset:38912\n"
"s_waitcnt lgkmcnt(5)\n"
"v_add_f32 v12, v12, v16\n"
"v_mfma_f32_16x16x32_bf16 v[32:35], a[144:147], v[196:199], v[32:35]\n"
"ds_read_b64_tr_b16 a[56:57], v105 offset:36864\n"
"v_add_f32 v13, v13, v17\n"
"v_mfma_f32_16x16x32_bf16 v[32:35], a[164:167], v[204:207], v[32:35]\n"
"ds_read_b64_tr_b16 a[58:59], v105 offset:38912\n"
"v_add_f32 v14, v14, v18\n"
"v_mfma_f32_16x16x32_bf16 v[32:35], a[152:155], v[212:215], v[32:35]\n"
"ds_read_b64_tr_b16 a[236:237], v106 offset:36864\n"
"v_add_f32 v15, v15, v19\n"
"v_mfma_f32_16x16x32_bf16 v[32:35], a[172:175], v[220:223], v[32:35]\n"
"ds_read_b64_tr_b16 a[238:239], v106 offset:38912\n"
"v_mfma_f32_16x16x32_bf16 a[96:99], a[144:147], v[116:119], a[96:99]\n"
"ds_read_b64_tr_b16 a[64:65], v107 offset:36864\n"
"v_mfma_f32_16x16x32_bf16 v[36:39], a[160:163], v[192:195], v[36:39]\n"
"ds_read_b64_tr_b16 a[66:67], v107 offset:38912\n"
"ds_read_b128 v[16:19], v21 offset:57344\n"
"v_mfma_f32_16x16x32_bf16 v[36:39], a[232:235], v[200:203], v[36:39]\n"
"s_waitcnt lgkmcnt(7)\n"
"ds_read_b64_tr_b16 a[52:53], v108 offset:36864\n"
"v_mov_b32 v124, s58\n"
"v_mfma_f32_16x16x32_bf16 v[36:39], a[168:171], v[208:211], v[36:39]\n"
"ds_read_b64_tr_b16 a[54:55], v108 offset:38912\n"
"v_lshlrev_b32 v125, 2, v0\n"
"v_mfma_f32_16x16x32_bf16 v[36:39], a[48:51], v[216:219], v[36:39]\n"
"ds_read_b64_tr_b16 a[72:73], v109 offset:36864\n"
"v_mfma_f32_16x16x32_bf16 v[40:43], a[160:163], v[196:199], v[40:43]\n"
"ds_read_b64_tr_b16 a[74:75], v109 offset:38912\n"
"v_mfma_f32_16x16x32_bf16 v[40:43], a[232:235], v[204:207], v[40:43]\n"
"s_waitcnt vmcnt(4)\n"
"buffer_load_dwordx4 v9, s[36:39], s63 offen offset:1024 nt lds\n"
"ds_read_b64_tr_b16 a[244:245], v110 offset:36864\n"
"v_mfma_f32_16x16x32_bf16 v[40:43], a[168:171], v[212:215], v[40:43]\n"
"ds_read_b64_tr_b16 a[246:247], v110 offset:38912\n"
"s_waitcnt lgkmcnt(6)\n"
"v_add_f32 v12, v12, v16\n"
"v_mfma_f32_16x16x32_bf16 v[40:43], a[48:51], v[220:223], v[40:43]\n"
"s_add_u32 s62, s8, 0\n"
"ds_read_b64_tr_b16 a[80:81], v111 offset:36864\n"
"v_add_f32 v13, v13, v17\n"
"v_mfma_f32_16x16x32_bf16 a[100:103], a[160:163], v[116:119], a[100:103]\n"
"s_add_u32 m0, s53, 30720\n"
"ds_read_b64_tr_b16 a[82:83], v111 offset:38912\n"
"v_add_f32 v14, v14, v18\n"
"v_mfma_f32_16x16x32_bf16 v[44:47], a[228:231], v[192:195], v[44:47]\n"
"s_mov_b32 exec_hi, 0\n"
"buffer_store_dword v124, v125, s[44:47], s70 offen offset:1536 sc1\n"
"s_mov_b32 exec_hi, -1\n"
"s_barrier\n"
"v_add_f32 v15, v15, v19\n"
"v_mfma_f32_16x16x32_bf16 v[44:47], a[56:59], v[200:203], v[44:47]\n"
"v_mul_f32 v12, v24, v12\n"
"v_mul_f32 v13, v24, v13\n"
"v_mfma_f32_16x16x32_bf16 v[44:47], a[236:239], v[208:211], v[44:47]\n"
"v_mul_f32 v14, v24, v14\n"
"v_mul_f32 v15, v24, v15\n"
"v_mfma_f32_16x16x32_bf16 v[44:47], a[64:67], v[216:219], v[44:47]\n"
"ds_read_b64_tr_b16 a[68:69], v96 offset:24576\n"
"v_cvt_pk_bf16_f32 v120, v12, v13\n"
"v_mfma_f32_16x16x32_bf16 v[48:51], a[228:231], v[196:199], v[48:51]\n"
"ds_read_b64_tr_b16 a[70:71], v96 offset:26624\n"
"v_cvt_pk_bf16_f32 v121, v14, v15\n"
"v_mfma_f32_16x16x32_bf16 v[48:51], a[56:59], v[204:207], v[48:51]\n"
"buffer_load_dwordx4 v2, s[40:43], s62 offen lds\n"
"v_mfma_f32_16x16x32_bf16 v[48:51], a[236:239], v[212:215], v[48:51]\n"
"s_add_u32 m0, s53, 4096\n"
"ds_read_b64_tr_b16 a[88:89], v97 offset:24576\n"
"v_add_u32 v123, s22, v25\n"
"v_mfma_f32_16x16x32_bf16 v[48:51], a[64:67], v[220:223], v[48:51]\n"
"ds_read_b64_tr_b16 a[90:91], v97 offset:26624\n"
"v_mfma_f32_16x16x32_bf16 a[104:107], a[228:231], v[116:119], a[104:107]\n"
"s_waitcnt lgkmcnt(4)\n"
"ds_read_b64_tr_b16 a[60:61], v98 offset:24576\n"
"v_mfma_f32_16x16x32_bf16 v[52:55], a[52:55], v[192:195], v[52:55]\n"
"ds_read_b64_tr_b16 a[62:63], v98 offset:26624\n"
"v_mfma_f32_16x16x32_bf16 v[52:55], a[72:75], v[200:203], v[52:55]\n"
"s_waitcnt vmcnt(6)\n"
"buffer_load_dwordx4 v10, s[36:39], s63 offen offset:2048 nt lds\n"
"ds_read_b64_tr_b16 a[216:217], v99 offset:24576\n"
"ds_write_b64 v123, v[120:121] offset:61440\n"
"v_mfma_f32_16x16x32_bf16 v[52:55], a[244:247], v[208:211], v[52:55]\n"
"ds_read_b64_tr_b16 a[218:219], v99 offset:26624\n"
"v_mfma_f32_16x16x32_bf16 v[52:55], a[80:83], v[216:219], v[52:55]\n"
"ds_read_b64_tr_b16 a[84:85], v100 offset:24576\n"
"v_mfma_f32_16x16x32_bf16 v[56:59], a[52:55], v[196:199], v[56:59]\n"
"ds_read_b64_tr_b16 a[86:87], v100 offset:26624\n"
"v_mfma_f32_16x16x32_bf16 v[56:59], a[72:75], v[204:207], v[56:59]\n"
"ds_read_b64_tr_b16 a[224:225], v101 offset:24576\n"
"v_mfma_f32_16x16x32_bf16 v[56:59], a[244:247], v[212:215], v[56:59]\n"
"ds_read_b64_tr_b16 a[226:227], v101 offset:26624\n"
"v_mfma_f32_16x16x32_bf16 v[56:59], a[80:83], v[220:223], v[56:59]\n"
"ds_read_b64_tr_b16 a[76:77], v102 offset:24576\n"
"v_mfma_f32_16x16x32_bf16 a[108:111], a[52:55], v[116:119], a[108:111]\n"
"s_waitcnt lgkmcnt(6)\n"
"ds_read_b64_tr_b16 a[78:79], v102 offset:26624\n"
"v_mfma_f32_16x16x32_bf16 v[28:31], a[68:71], v[224:227], v[28:31]\n"
"ds_read_b64_tr_b16 a[44:45], v103 offset:24576\n"
"v_mfma_f32_16x16x32_bf16 v[28:31], a[88:91], v[232:235], v[28:31]\n"
"ds_read_b64_tr_b16 a[46:47], v103 offset:26624\n"
"v_mfma_f32_16x16x32_bf16 v[28:31], a[60:63], v[240:243], v[28:31]\n"
"s_waitcnt lgkmcnt(6)\n"
"ds_read_b64_tr_b16 a[220:221], v104 offset:24576\n"
"v_mfma_f32_16x16x32_bf16 v[28:31], a[216:219], v[248:251], v[28:31]\n"
"ds_read_b64_tr_b16 a[222:223], v104 offset:26624\n"
"v_mfma_f32_16x16x32_bf16 v[32:35], a[68:71], v[228:231], v[32:35]\n"
"s_waitcnt vmcnt(6)\n"
"buffer_load_dwordx4 v11, s[36:39], s63 offen offset:3072 nt lds\n"
"ds_read_b64_tr_b16 a[132:133], v105 offset:24576\n"
"v_mfma_f32_16x16x32_bf16 v[32:35], a[88:91], v[236:239], v[32:35]\n"
"s_add_u32 m0, s53, 30720\n"
"ds_read_b64_tr_b16 a[134:135], v105 offset:26624\n"
"v_mfma_f32_16x16x32_bf16 v[32:35], a[60:63], v[244:247], v[32:35]\n"
"ds_read_b64_tr_b16 a[92:93], v106 offset:24576\n"
"v_lshrrev_b32 v124, 5, v0\n"
"v_mfma_f32_16x16x32_bf16 v[32:35], a[216:219], v[252:255], v[32:35]\n"
"ds_read_b64_tr_b16 a[94:95], v106 offset:26624\n"
"v_xor_b32 v124, s55, v124\n"
"v_mfma_f32_16x16x32_bf16 a[96:99], a[68:71], a[212:215], a[96:99]\n"
"ds_read_b64_tr_b16 a[140:141], v107 offset:24576\n"
"buffer_load_dwordx4 v3, s[40:43], s62 offen offset:1024 lds\n"
"v_mfma_f32_16x16x32_bf16 v[36:39], a[84:87], v[224:227], v[36:39]\n"
"s_waitcnt lgkmcnt(6)\n"
"ds_read_b64_tr_b16 a[142:143], v107 offset:26624\n"
"v_lshlrev_b32 v124, 5, v124\n"
"v_mfma_f32_16x16x32_bf16 v[36:39], a[224:227], v[232:235], v[36:39]\n"
"ds_read_b64_tr_b16 a[128:129], v108 offset:24576\n"
"v_bfe_u32 v125, v0, 2, 3\n"
"v_mfma_f32_16x16x32_bf16 v[36:39], a[76:79], v[240:243], v[36:39]\n"
"ds_read_b64_tr_b16 a[130:131], v108 offset:26624\n"
"v_add_u32 v124, v124, v125\n"
"v_mfma_f32_16x16x32_bf16 v[36:39], a[44:47], v[248:251], v[36:39]\n"
"ds_read_b64_tr_b16 a[196:197], v109 offset:24576\n"
"v_lshlrev_b32 v125, 11, v124\n"
"v_mfma_f32_16x16x32_bf16 v[40:43], a[84:87], v[228:231], v[40:43]\n"
"ds_read_b64_tr_b16 a[198:199], v109 offset:26624\n"
"buffer_load_dwordx4 v4, s[40:43], s62 offen offset:2048 lds\n"
"v_mfma_f32_16x16x32_bf16 v[40:43], a[224:227], v[236:239], v[40:43]\n"
"s_lshl_b32 s5, s52, 14\n"
"ds_read_b64_tr_b16 a[40:41], v110 offset:24576\n"
"s_lshl_b32 s4, s56, 7\n"
"v_mfma_f32_16x16x32_bf16 v[40:43], a[76:79], v[244:247], v[40:43]\n"
"s_add_u32 s5, s5, s4\n"
"ds_read_b64_tr_b16 a[42:43], v110 offset:26624\n"
"s_add_u32 s5, s5, 525824\n"
"v_mfma_f32_16x16x32_bf16 v[40:43], a[44:47], v[252:255], v[40:43]\n"
"ds_read_b64_tr_b16 a[148:149], v111 offset:24576\n"
"v_mul_f32 v28, v22, v28\n"
"v_mfma_f32_16x16x32_bf16 a[100:103], a[84:87], a[212:215], a[100:103]\n"
"s_waitcnt lgkmcnt(6)\n"
"ds_read_b64_tr_b16 a[150:151], v111 offset:26624\n"
"v_mul_f32 v29, v22, v29\n"
"v_mfma_f32_16x16x32_bf16 v[44:47], a[220:223], v[224:227], v[44:47]\n"
"s_barrier\n"
"buffer_load_dwordx4 v5, s[40:43], s62 offen offset:3072 lds\n"
"v_mfma_f32_16x16x32_bf16 v[44:47], a[132:135], v[232:235], v[44:47]\n"
"ds_write_b64 v26, a[96:97] offset:16384\n"
"v_mfma_f32_16x16x32_bf16 v[44:47], a[92:95], v[240:243], v[44:47]\n"
"ds_write_b64 v26, a[98:99] offset:16392\n"
"v_mfma_f32_16x16x32_bf16 v[44:47], a[140:143], v[248:251], v[44:47]\n"
"s_add_u32 m0, s53, 34816\n"
"ds_read_b64_tr_b16 a[192:193], v96 offset:12288\n"
"v_mul_f32 v30, v22, v30\n"
"v_mfma_f32_16x16x32_bf16 v[48:51], a[220:223], v[228:231], v[48:51]\n"
"ds_read_b64_tr_b16 a[194:195], v96 offset:14336\n"
"v_mul_f32 v31, v22, v31\n"
"v_mfma_f32_16x16x32_bf16 v[48:51], a[132:135], v[236:239], v[48:51]\n"
"buffer_load_dword v8, v125, s[44:47], s5 offen sc1\n"
"v_mfma_f32_16x16x32_bf16 v[48:51], a[92:95], v[244:247], v[48:51]\n"
"ds_read_b64_tr_b16 a[156:157], v97 offset:12288\n"
"v_cvt_pk_bf16_f32 v80, v28, v29\n"
"v_mfma_f32_16x16x32_bf16 v[48:51], a[140:143], v[252:255], v[48:51]\n"
"ds_read_b64_tr_b16 a[158:159], v97 offset:14336\n"
"v_cvt_pk_bf16_f32 v81, v30, v31\n"
"v_mfma_f32_16x16x32_bf16 a[104:107], a[220:223], a[212:215], a[104:107]\n"
"s_waitcnt lgkmcnt(5)\n"
"ds_read_b64_tr_b16 a[136:137], v98 offset:12288\n"
"v_mul_f32 v32, v23, v32\n"
"v_mfma_f32_16x16x32_bf16 v[52:55], a[128:131], v[224:227], v[52:55]\n"
"ds_read_b64_tr_b16 a[138:139], v98 offset:14336\n"
"v_mul_f32 v33, v23, v33\n"
"v_mfma_f32_16x16x32_bf16 v[52:55], a[196:199], v[232:235], v[52:55]\n"
"ds_read_b64_tr_b16 a[164:165], v99 offset:12288\n"
"buffer_load_dwordx4 v6, s[40:43], s62 offen lds\n"
"v_mfma_f32_16x16x32_bf16 v[52:55], a[40:43], v[240:243], v[52:55]\n"
"ds_read_b64_tr_b16 a[166:167], v99 offset:14336\n"
"ds_write_b64 v26, a[100:101] offset:17408\n"
"v_mfma_f32_16x16x32_bf16 v[52:55], a[148:151], v[248:251], v[52:55]\n"
"ds_read_b64_tr_b16 a[152:153], v100 offset:12288\n"
"ds_write_b64 v26, a[102:103] offset:17416\n"
"v_mfma_f32_16x16x32_bf16 v[56:59], a[128:131], v[228:231], v[56:59]\n"
"s_add_u32 s62, s8, 81920\n"
"ds_read_b64_tr_b16 a[154:155], v100 offset:14336\n"
"s_add_u32 m0, s53, 35840\n"
"v_mfma_f32_16x16x32_bf16 v[56:59], a[196:199], v[236:239], v[56:59]\n"
"ds_read_b64_tr_b16 a[172:173], v101 offset:12288\n"
"v_mul_f32 v34, v23, v34\n"
"v_mfma_f32_16x16x32_bf16 v[56:59], a[40:43], v[244:247], v[56:59]\n"
"ds_read_b64_tr_b16 a[174:175], v101 offset:14336\n"
"buffer_load_dwordx4 v2, s[40:43], s62 offen lds\n"
"v_mfma_f32_16x16x32_bf16 v[56:59], a[148:151], v[252:255], v[56:59]\n"
"ds_read_b64_tr_b16 a[144:145], v102 offset:12288\n"
"v_mul_f32 v35, v23, v35\n"
"v_mfma_f32_16x16x32_bf16 a[108:111], a[128:131], a[212:215], a[108:111]\n"
"s_waitcnt lgkmcnt(8)\n"
"ds_read_b64_tr_b16 a[146:147], v102 offset:14336\n"
"v_cvt_pk_bf16_f32 v88, v32, v33\n"
"v_cvt_pk_bf16_f32 v89, v34, v35\n"
"v_mfma_f32_16x16x32_bf16 v[28:31], a[192:195], v[128:131], 0\n"
"ds_read_b64_tr_b16 a[232:233], v103 offset:12288\n"
"v_mul_f32 v36, v22, v36\n"
"v_mfma_f32_16x16x32_bf16 v[28:31], a[156:159], v[136:139], v[28:31]\n"
"ds_read_b64_tr_b16 a[234:235], v103 offset:14336\n"
"v_mul_f32 v37, v22, v37\n"
"v_mfma_f32_16x16x32_bf16 v[28:31], a[136:139], v[144:147], v[28:31]\n"
"s_waitcnt lgkmcnt(6)\n"
"ds_read_b64_tr_b16 a[168:169], v104 offset:12288\n"
"ds_write_b64 v26, a[104:105] offset:18432\n"
"buffer_load_dwordx4 v3, s[40:43], s62 offen offset:1024 lds\n"
"v_mul_f32 v38, v22, v38\n"
"v_mfma_f32_16x16x32_bf16 v[28:31], a[164:167], v[152:155], v[28:31]\n"
"ds_read_b64_tr_b16 a[170:171], v104 offset:14336\n"
"ds_write_b64 v26, a[106:107] offset:18440\n"
"v_mul_f32 v39, v22, v39\n"
"v_mfma_f32_16x16x32_bf16 v[32:35], a[192:195], v[132:135], 0\n"
"ds_read_b64_tr_b16 a[48:49], v105 offset:12288\n"
"v_cvt_pk_bf16_f32 v82, v36, v37\n"
"v_cvt_pk_bf16_f32 v83, v38, v39\n"
"v_mfma_f32_16x16x32_bf16 v[32:35], a[156:159], v[140:143], v[32:35]\n"
"ds_read_b64_tr_b16 a[50:51], v105 offset:14336\n"
"v_mul_f32 v40, v23, v40\n"
"v_mfma_f32_16x16x32_bf16 v[32:35], a[136:139], v[148:151], v[32:35]\n"
"ds_read_b64_tr_b16 a[160:161], v106 offset:12288\n"
"v_mul_f32 v41, v23, v41\n"
".Lpad13_%=:\n"
"s_cbranch_execz .Lwb_%=\n"
"v_mul_f32 v42, v23, v42\n"
"v_mfma_f32_16x16x32_bf16 v[32:35], a[164:167], v[156:159], v[32:35]\n"
"ds_read_b64_tr_b16 a[162:163], v106 offset:14336\n"
"buffer_load_dwordx4 v4, s[40:43], s62 offen offset:2048 lds\n"
"v_mul_f32 v43, v23, v43\n"
"v_mfma_f32_16x16x32_bf16 a[96:99], a[192:195], v[60:63], 0\n"
"ds_read_b64_tr_b16 a[56:57], v107 offset:12288\n"
"v_cvt_pk_bf16_f32 v90, v40, v41\n"
"v_cvt_pk_bf16_f32 v91, v42, v43\n"
"v_mfma_f32_16x16x32_bf16 v[36:39], a[152:155], v[128:131], 0\n"
"s_waitcnt lgkmcnt(7)\n"
"ds_read_b64_tr_b16 a[58:59], v107 offset:14336\n"
"v_mul_f32 v44, v22, v44\n"
"v_mfma_f32_16x16x32_bf16 v[36:39], a[172:175], v[136:139], v[36:39]\n"
"ds_read_b64_tr_b16 a[236:237], v108 offset:12288\n"
"v_mul_f32 v45, v22, v45\n"
"v_mfma_f32_16x16x32_bf16 v[36:39], a[144:147], v[144:147], v[36:39]\n"
"ds_read_b64_tr_b16 a[238:239], v108 offset:14336\n"
"ds_write_b64 v26, a[108:109] offset:19456\n"
"v_mul_f32 v46, v22, v46\n"
"v_mfma_f32_16x16x32_bf16 v[36:39], a[232:235], v[152:155], v[36:39]\n"
"ds_read_b64_tr_b16 a[64:65], v109 offset:12288\n"
"ds_write_b64 v26, a[110:111] offset:19464\n"
"buffer_load_dwordx4 v5, s[40:43], s62 offen offset:3072 lds\n"
"v_mul_f32 v47, v22, v47\n"
"v_mfma_f32_16x16x32_bf16 v[40:43], a[152:155], v[132:135], 0\n"
"ds_read_b64_tr_b16 a[66:67], v109 offset:14336\n"
"v_cvt_pk_bf16_f32 v84, v44, v45\n"
"v_cvt_pk_bf16_f32 v85, v46, v47\n"
"v_mfma_f32_16x16x32_bf16 v[40:43], a[172:175], v[140:143], v[40:43]\n"
"s_add_u32 m0, s53, 39936\n"
"ds_read_b64_tr_b16 a[228:229], v110 offset:12288\n"
"v_mul_f32 v48, v23, v48\n"
"v_mfma_f32_16x16x32_bf16 v[40:43], a[144:147], v[148:151], v[40:43]\n"
"ds_read_b64_tr_b16 a[230:231], v110 offset:14336\n"
"v_mul_f32 v49, v23, v49\n"
"v_mul_f32 v50, v23, v50\n"
"v_mfma_f32_16x16x32_bf16 v[40:43], a[232:235], v[156:159], v[40:43]\n"
"ds_read_b64_tr_b16 a[72:73], v111 offset:12288\n"
"v_mul_f32 v51, v23, v51\n"
"v_mfma_f32_16x16x32_bf16 a[100:103], a[152:155], v[60:63], 0\n"
"s_waitcnt lgkmcnt(8)\n"
"ds_read_b64_tr_b16 a[74:75], v111 offset:14336\n"
"buffer_load_dwordx4 v6, s[40:43], s62 offen lds\n"
"v_cvt_pk_bf16_f32 v92, v48, v49\n"
"v_cvt_pk_bf16_f32 v93, v50, v51\n"
"v_mfma_f32_16x16x32_bf16 v[44:47], a[168:171], v[128:131], 0\n"
"s_waitcnt lgkmcnt(5)\n"
"s_barrier\n"
"v_mul_f32 v52, v22, v52\n"
"v_mfma_f32_16x16x32_bf16 v[44:47], a[48:51], v[136:139], v[44:47]\n"
"s_add_u32 s62, s8, 163840\n"
"v_mul_f32 v53, v22, v53\n"
"v_mul_f32 v54, v22, v54\n"
"v_mfma_f32_16x16x32_bf16 v[44:47], a[160:163], v[144:147], v[44:47]\n"
"s_add_u32 m0, s53, 25600\n"
"ds_read_b64_tr_b16 a[244:245], v96 offset:0\n"
"v_mul_f32 v55, v22, v55\n"
"v_mfma_f32_16x16x32_bf16 v[44:47], a[56:59], v[152:155], v[44:47]\n"
"v_cvt_pk_bf16_f32 v86, v52, v53\n"
"v_cvt_pk_bf16_f32 v87, v54, v55\n"
"v_mfma_f32_16x16x32_bf16 v[48:51], a[168:171], v[132:135], 0\n"
"buffer_load_dwordx4 v2, s[40:43], s62 offen lds\n"
"v_accvgpr_read_b32 v124, a243\n"
"v_mfma_f32_16x16x32_bf16 v[48:51], a[48:51], v[140:143], v[48:51]\n"
"ds_read_b128 v[12:15], v20 offset:16384\n"
"ds_read_b64_tr_b16 a[246:247], v96 offset:2048\n"
"ds_read_b128 v[16:19], v20 offset:57344\n"
"v_mul_f32 v56, v23, v56\n"
"v_add_u32 v124, 1, v124\n"
"v_mfma_f32_16x16x32_bf16 v[48:51], a[160:163], v[148:151], v[48:51]\n"
"ds_read_b64_tr_b16 a[80:81], v97 offset:0\n"
"v_mul_f32 v57, v23, v57\n"
"v_mul_f32 v58, v23, v58\n"
"v_mfma_f32_16x16x32_bf16 v[48:51], a[56:59], v[156:159], v[48:51]\n"
"ds_read_b64_tr_b16 a[82:83], v97 offset:2048\n"
"v_mul_f32 v59, v23, v59\n"
"v_mfma_f32_16x16x32_bf16 a[104:107], a[168:171], v[60:63], 0\n"
"ds_read_b64_tr_b16 a[52:53], v98 offset:0\n"
"v_cvt_pk_bf16_f32 v94, v56, v57\n"
"v_cvt_pk_bf16_f32 v95, v58, v59\n"
"v_mfma_f32_16x16x32_bf16 v[52:55], a[236:239], v[128:131], 0\n"
"s_waitcnt lgkmcnt(6)\n"
"ds_read_b64_tr_b16 a[54:55], v98 offset:2048\n"
"buffer_load_dwordx4 v3, s[40:43], s62 offen offset:1024 lds\n"
"s_waitcnt vmcnt(8)\n"
"v_cmp_ne_u32 vcc, v8, v124\n"
"v_mfma_f32_16x16x32_bf16 v[52:55], a[64:67], v[136:139], v[52:55]\n"
"ds_read_b64_tr_b16 a[88:89], v99 offset:0\n"
"s_waitcnt lgkmcnt(5)\n"
"v_add_f32 v12, v12, v16\n"
"v_mfma_f32_16x16x32_bf16 v[52:55], a[228:231], v[144:147], v[52:55]\n"
"s_add_u32 s20, s66, 0\n"
"ds_read_b64_tr_b16 a[90:91], v99 offset:2048\n"
"v_add_f32 v13, v13, v17\n"
"v_mfma_f32_16x16x32_bf16 v[52:55], a[72:75], v[152:155], v[52:55]\n"
"s_cbranch_vccnz .Lsh_%=\n"
".Lshr_%=:\n"
"ds_read_b64_tr_b16 a[60:61], v100 offset:0\n"
"v_add_f32 v14, v14, v18\n"
"v_mfma_f32_16x16x32_bf16 v[56:59], a[236:239], v[132:135], 0\n"
"s_barrier\n"
"ds_read_b64_tr_b16 a[62:63], v100 offset:2048\n"
"v_add_f32 v15, v15, v19\n"
"v_mfma_f32_16x16x32_bf16 v[56:59], a[64:67], v[140:143], v[56:59]\n"
"ds_read_b64_tr_b16 a[216:217], v101 offset:0\n"
"buffer_load_dwordx4 v4, s[40:43], s62 offen offset:2048 lds\n"
"v_mfma_f32_16x16x32_bf16 v[56:59], a[228:231], v[148:151], v[56:59]\n"
"ds_read_b64_tr_b16 a[218:219], v101 offset:2048\n"
"v_mfma_f32_16x16x32_bf16 v[56:59], a[72:75], v[156:159], v[56:59]\n"
"ds_read_b64_tr_b16 a[68:69], v102 offset:0\n"
"ds_read_b128 v[16:19], v21 offset:16384\n"
"v_mfma_f32_16x16x32_bf16 a[108:111], a[236:239], v[60:63], 0\n"
"ds_read_b64_tr_b16 a[70:71], v102 offset:2048\n"
"v_mfma_f32_16x16x32_bf16 v[28:31], a[244:247], v[160:163], v[28:31]\n"
"s_waitcnt lgkmcnt(7)\n"
"ds_read_b64_tr_b16 a[224:225], v103 offset:0\n"
"v_mfma_f32_16x16x32_bf16 v[28:31], a[80:83], v[168:171], v[28:31]\n"
"ds_read_b64_tr_b16 a[226:227], v103 offset:2048\n"
"buffer_store_dwordx4 v[64:67], v1, s[44:47], s20 offen offset:0 sc1\n"
"v_mfma_f32_16x16x32_bf16 v[28:31], a[52:55], v[176:179], v[28:31]\n"
"ds_read_b64_tr_b16 a[76:77], v104 offset:0\n"
"v_mfma_f32_16x16x32_bf16 v[28:31], a[88:91], v[184:187], v[28:31]\n"
"ds_read_b64_tr_b16 a[78:79], v104 offset:2048\n"
"s_waitcnt lgkmcnt(5)\n"
"v_add_f32 v12, v12, v16\n"
"v_mfma_f32_16x16x32_bf16 v[32:35], a[244:247], v[164:167], v[32:35]\n"
"ds_read_b64_tr_b16 a[44:45], v105 offset:0\n"
"v_add_f32 v13, v13, v17\n"
"v_mfma_f32_16x16x32_bf16 v[32:35], a[80:83], v[172:175], v[32:35]\n"
"ds_read_b64_tr_b16 a[46:47], v105 offset:2048\n"
"v_add_f32 v14, v14, v18\n"
"v_mfma_f32_16x16x32_bf16 v[32:35], a[52:55], v[180:183], v[32:35]\n"
"ds_read_b64_tr_b16 a[84:85], v106 offset:0\n"
"v_add_f32 v15, v15, v19\n"
"v_mfma_f32_16x16x32_bf16 v[32:35], a[88:91], v[188:191], v[32:35]\n"
"ds_read_b64_tr_b16 a[86:87], v106 offset:2048\n"
"buffer_store_dwordx4 v[72:75], v1, s[44:47], s20 offen offset:2048 sc1\n"
"v_mfma_f32_16x16x32_bf16 a[96:99], a[244:247], v[112:115], a[96:99]\n"
"ds_read_b64_tr_b16 a[132:133], v107 offset:0\n"
"v_mfma_f32_16x16x32_bf16 v[36:39], a[60:63], v[160:163], v[36:39]\n"
"ds_read_b64_tr_b16 a[134:135], v107 offset:2048\n"
"ds_read_b128 v[16:19], v21 offset:57344\n"
"v_mfma_f32_16x16x32_bf16 v[36:39], a[216:219], v[168:171], v[36:39]\n"
"s_waitcnt lgkmcnt(7)\n"
"ds_read_b64_tr_b16 a[92:93], v108 offset:0\n"
"v_mfma_f32_16x16x32_bf16 v[36:39], a[68:71], v[176:179], v[36:39]\n"
"ds_read_b64_tr_b16 a[94:95], v108 offset:2048\n"
"v_mfma_f32_16x16x32_bf16 v[36:39], a[224:227], v[184:187], v[36:39]\n"
"ds_read_b64_tr_b16 a[140:141], v109 offset:0\n"
"v_mfma_f32_16x16x32_bf16 v[40:43], a[60:63], v[164:167], v[40:43]\n"
"ds_read_b64_tr_b16 a[142:143], v109 offset:2048\n"
"buffer_store_dwordx4 v[68:71], v1, s[44:47], s20 offen offset:1024 sc1\n"
"v_mfma_f32_16x16x32_bf16 v[40:43], a[216:219], v[172:175], v[40:43]\n"
"ds_read_b64_tr_b16 a[220:221], v110 offset:0\n"
"s_waitcnt lgkmcnt(5)\n"
"v_add_f32 v12, v12, v16\n"
"v_mfma_f32_16x16x32_bf16 v[40:43], a[68:71], v[180:183], v[40:43]\n"
"ds_read_b64_tr_b16 a[222:223], v110 offset:2048\n"
"v_add_f32 v13, v13, v17\n"
"v_mfma_f32_16x16x32_bf16 v[40:43], a[224:227], v[188:191], v[40:43]\n"
"ds_read_b64_tr_b16 a[196:197], v111 offset:0\n"
"v_add_f32 v14, v14, v18\n"
"v_mfma_f32_16x16x32_bf16 a[100:103], a[60:63], v[112:115], a[100:103]\n"
"ds_read_b64_tr_b16 a[198:199], v111 offset:2048\n"
"v_add_f32 v15, v15, v19\n"
"v_mfma_f32_16x16x32_bf16 v[44:47], a[76:79], v[160:163], v[44:47]\n"
"s_barrier\n"
"v_mul_f32 v12, v24, v12\n"
"v_mfma_f32_16x16x32_bf16 v[44:47], a[44:47], v[168:171], v[44:47]\n"
"buffer_store_dwordx4 v[76:79], v1, s[44:47], s20 offen offset:3072 sc1\n"
"v_mul_f32 v13, v24, v13\n"
"v_mfma_f32_16x16x32_bf16 v[44:47], a[84:87], v[176:179], v[44:47]\n"
"s_add_u32 s20, s20, s61\n"
"v_mul_f32 v14, v24, v14\n"
"v_mul_f32 v15, v24, v15\n"
"v_mfma_f32_16x16x32_bf16 v[44:47], a[132:135], v[184:187], v[44:47]\n"
"ds_read_b64_tr_b16 a[40:41], v96 offset:8192\n"
"v_cvt_pk_bf16_f32 v126, v12, v13\n"
"v_mfma_f32_16x16x32_bf16 v[48:51], a[76:79], v[164:167], v[48:51]\n"
"ds_read_b64_tr_b16 a[42:43], v96 offset:10240\n"
"v_cvt_pk_bf16_f32 v127, v14, v15\n"
"v_mfma_f32_16x16x32_bf16 v[48:51], a[44:47], v[172:175], v[48:51]\n"
"ds_read_b64_tr_b16 a[148:149], v97 offset:8192\n"
"v_add_u32 v123, s22, v25\n"
"v_mfma_f32_16x16x32_bf16 v[48:51], a[84:87], v[180:183], v[48:51]\n"
"ds_read_b64_tr_b16 a[150:151], v97 offset:10240\n"
"v_mfma_f32_16x16x32_bf16 v[48:51], a[132:135], v[188:191], v[48:51]\n"
"buffer_store_dwordx2 v[120:121], v122, s[44:47], s20 offen sc1\n"
"v_mfma_f32_16x16x32_bf16 a[104:107], a[76:79], v[112:115], a[104:107]\n"
"s_waitcnt lgkmcnt(4)\n"
"ds_read_b64_tr_b16 a[128:129], v98 offset:8192\n"
"s_add_u32 s20, s66, 18432\n"
"v_mfma_f32_16x16x32_bf16 v[52:55], a[92:95], v[160:163], v[52:55]\n"
"ds_read_b64_tr_b16 a[130:131], v98 offset:10240\n"
"v_mfma_f32_16x16x32_bf16 v[52:55], a[140:143], v[168:171], v[52:55]\n"
"ds_read_b64_tr_b16 a[156:157], v99 offset:8192\n"
"ds_write_b64 v123, v[126:127] offset:63488\n"
"v_mfma_f32_16x16x32_bf16 v[52:55], a[220:223], v[176:179], v[52:55]\n"
"ds_read_b64_tr_b16 a[158:159], v99 offset:10240\n"
"v_mfma_f32_16x16x32_bf16 v[52:55], a[196:199], v[184:187], v[52:55]\n"
"ds_read_b64_tr_b16 a[136:137], v100 offset:8192\n"
"v_mfma_f32_16x16x32_bf16 v[56:59], a[92:95], v[164:167], v[56:59]\n"
"ds_read_b64_tr_b16 a[138:139], v100 offset:10240\n"
"buffer_store_dwordx4 v[80:83], v1, s[44:47], s20 offen offset:0 sc1\n"
"v_mfma_f32_16x16x32_bf16 v[56:59], a[140:143], v[172:175], v[56:59]\n"
"ds_read_b64_tr_b16 a[164:165], v101 offset:8192\n"
"v_mfma_f32_16x16x32_bf16 v[56:59], a[220:223], v[180:183], v[56:59]\n"
"ds_read_b64_tr_b16 a[166:167], v101 offset:10240\n"
"v_mfma_f32_16x16x32_bf16 v[56:59], a[196:199], v[188:191], v[56:59]\n"
"ds_read_b64_tr_b16 a[192:193], v102 offset:8192\n"
"v_mfma_f32_16x16x32_bf16 a[108:111], a[92:95], v[112:115], a[108:111]\n"
"s_waitcnt lgkmcnt(6)\n"
"ds_read_b64_tr_b16 a[194:195], v102 offset:10240\n"
"v_mfma_f32_16x16x32_bf16 v[28:31], a[40:43], v[192:195], v[28:31]\n"
"ds_read_b64_tr_b16 a[172:173], v103 offset:8192\n"
"v_mfma_f32_16x16x32_bf16 v[28:31], a[148:151], v[200:203], v[28:31]\n"
"ds_read_b64_tr_b16 a[174:175], v103 offset:10240\n"
"buffer_store_dwordx4 v[88:91], v1, s[44:47], s20 offen offset:2048 sc1\n"
"v_mfma_f32_16x16x32_bf16 v[28:31], a[128:131], v[208:211], v[28:31]\n"
"s_waitcnt lgkmcnt(6)\n"
"ds_read_b64_tr_b16 a[144:145], v104 offset:8192\n"
"v_mfma_f32_16x16x32_bf16 v[28:31], a[156:159], v[216:219], v[28:31]\n"
"ds_read_b64_tr_b16 a[146:147], v104 offset:10240\n"
"v_mfma_f32_16x16x32_bf16 v[32:35], a[40:43], v[196:199], v[32:35]\n"
"ds_read_b64_tr_b16 a[232:233], v105 offset:8192\n"
"v_mfma_f32_16x16x32_bf16 v[32:35], a[148:151], v[204:207], v[32:35]\n"
"ds_read_b64_tr_b16 a[234:235], v105 offset:10240\n"
"v_mfma_f32_16x16x32_bf16 v[32:35], a[128:131], v[212:215], v[32:35]\n"
"ds_read_b64_tr_b16 a[152:153], v106 offset:8192\n"
"v_accvgpr_read_b32 v12, a112\n"
"v_mfma_f32_16x16x32_bf16 v[32:35], a[156:159], v[220:223], v[32:35]\n"
"ds_read_b64_tr_b16 a[154:155], v106 offset:10240\n"
"buffer_store_dwordx4 v[84:87], v1, s[44:47], s20 offen offset:1024 sc1\n"
"v_mfma_f32_16x16x32_bf16 a[96:99], a[40:43], v[116:119], a[96:99]\n"
"ds_read_b64_tr_b16 a[48:49], v107 offset:8192\n"
"v_accvgpr_read_b32 v13, a113\n"
"v_mfma_f32_16x16x32_bf16 v[36:39], a[136:139], v[192:195], v[36:39]\n"
"s_waitcnt lgkmcnt(6)\n"
"ds_read_b64_tr_b16 a[50:51], v107 offset:10240\n"
"v_accvgpr_read_b32 v14, a114\n"
"v_mfma_f32_16x16x32_bf16 v[36:39], a[164:167], v[200:203], v[36:39]\n"
"ds_read_b64_tr_b16 a[160:161], v108 offset:8192\n"
"v_accvgpr_read_b32 v15, a115\n"
"v_mfma_f32_16x16x32_bf16 v[36:39], a[192:195], v[208:211], v[36:39]\n"
"ds_read_b64_tr_b16 a[162:163], v108 offset:10240\n"
"v_accvgpr_read_b32 v16, a116\n"
"v_mfma_f32_16x16x32_bf16 v[36:39], a[172:175], v[216:219], v[36:39]\n"
"ds_read_b64_tr_b16 a[56:57], v109 offset:8192\n"
"v_accvgpr_read_b32 v17, a117\n"
"v_mfma_f32_16x16x32_bf16 v[40:43], a[136:139], v[196:199], v[40:43]\n"
"ds_read_b64_tr_b16 a[58:59], v109 offset:10240\n"
"buffer_store_dwordx4 v[92:95], v1, s[44:47], s20 offen offset:3072 sc1\n"
"v_mfma_f32_16x16x32_bf16 v[40:43], a[164:167], v[204:207], v[40:43]\n"
"s_add_u32 s20, s20, s61\n"
"ds_read_b64_tr_b16 a[168:169], v110 offset:8192\n"
"v_accvgpr_read_b32 v18, a118\n"
"v_mfma_f32_16x16x32_bf16 v[40:43], a[192:195], v[212:215], v[40:43]\n"
"ds_read_b64_tr_b16 a[170:171], v110 offset:10240\n"
"v_accvgpr_read_b32 v19, a119\n"
"v_mfma_f32_16x16x32_bf16 v[40:43], a[172:175], v[220:223], v[40:43]\n"
"ds_read_b64_tr_b16 a[64:65], v111 offset:8192\n"
"v_accvgpr_read_b32 v20, a120\n"
"v_mfma_f32_16x16x32_bf16 a[100:103], a[136:139], v[116:119], a[100:103]\n"
"s_waitcnt lgkmcnt(6)\n"
"ds_read_b64_tr_b16 a[66:67], v111 offset:10240\n"
"v_accvgpr_read_b32 v21, a121\n"
"v_mfma_f32_16x16x32_bf16 v[44:47], a[144:147], v[192:195], v[44:47]\n"
"s_barrier\n"
"ds_read_b64_tr_b16 a[228:229], v96 offset:4096\n"
"v_mfma_f32_16x16x32_bf16 v[44:47], a[232:235], v[200:203], v[44:47]\n"
"buffer_store_dwordx2 v[126:127], v122, s[44:47], s20 offen sc1\n"
"v_mfma_f32_16x16x32_bf16 v[44:47], a[152:155], v[208:211], v[44:47]\n"
"ds_read_b64_tr_b16 a[230:231], v96 offset:6144\n"
"v_mfma_f32_16x16x32_bf16 v[44:47], a[48:51], v[216:219], v[44:47]\n"
"ds_read_b64_tr_b16 a[72:73], v97 offset:4096\n"
"v_mfma_f32_16x16x32_bf16 v[48:51], a[144:147], v[196:199], v[48:51]\n"
"ds_read_b64_tr_b16 a[74:75], v97 offset:6144\n"
"v_mfma_f32_16x16x32_bf16 v[48:51], a[232:235], v[204:207], v[48:51]\n"
"ds_read_b64_tr_b16 a[236:237], v98 offset:4096\n"
"v_mfma_f32_16x16x32_bf16 v[48:51], a[152:155], v[212:215], v[48:51]\n"
"buffer_load_dwordx4 v5, s[40:43], s62 offen offset:3072 lds\n"
"v_mfma_f32_16x16x32_bf16 v[48:51], a[48:51], v[220:223], v[48:51]\n"
"s_add_u32 m0, s53, 29696\n"
"ds_read_b64_tr_b16 a[238:239], v98 offset:6144\n"
"v_mfma_f32_16x16x32_bf16 a[104:107], a[144:147], v[116:119], a[104:107]\n"
"s_waitcnt lgkmcnt(5)\n"
"ds_read_b64_tr_b16 a[80:81], v99 offset:4096\n"
"v_mfma_f32_16x16x32_bf16 v[52:55], a[160:163], v[192:195], v[52:55]\n"
"ds_read_b64_tr_b16 a[82:83], v99 offset:6144\n"
"v_mfma_f32_16x16x32_bf16 v[52:55], a[56:59], v[200:203], v[52:55]\n"
"ds_read_b64_tr_b16 a[52:53], v100 offset:4096\n"
"v_mfma_f32_16x16x32_bf16 v[52:55], a[168:171], v[208:211], v[52:55]\n"
"buffer_load_dwordx4 v6, s[40:43], s62 offen lds\n"
"v_mfma_f32_16x16x32_bf16 v[52:55], a[64:67], v[216:219], v[52:55]\n"
"s_add_u32 s62, s9, 0\n"
"ds_read_b64_tr_b16 a[54:55], v100 offset:6144\n"
"s_add_u32 m0, s53, 15360\n"
"v_mfma_f32_16x16x32_bf16 v[56:59], a[160:163], v[196:199], v[56:59]\n"
"s_lshl_b32 s19, s52, 10\n"
"ds_read_b64_tr_b16 a[88:89], v101 offset:4096\n"
"v_add_u32 v9, s19, v1\n"
"v_mfma_f32_16x16x32_bf16 v[56:59], a[56:59], v[204:207], v[56:59]\n"
"ds_read_b64_tr_b16 a[90:91], v101 offset:6144\n"
"v_add_u32 v10, 81920, v9\n"
"v_mfma_f32_16x16x32_bf16 v[56:59], a[168:171], v[212:215], v[56:59]\n"
"ds_read_b64_tr_b16 a[244:245], v102 offset:4096\n"
"v_lshrrev_b32 v8, 5, v0\n"
"v_mfma_f32_16x16x32_bf16 v[56:59], a[64:67], v[220:223], v[56:59]\n"
"ds_read_b64_tr_b16 a[246:247], v102 offset:6144\n"
"v_and_b32 v11, 31, v0\n"
"v_mfma_f32_16x16x32_bf16 a[108:111], a[160:163], v[116:119], a[108:111]\n"
"s_waitcnt lgkmcnt(5)\n"
"buffer_load_dwordx4 v2, s[40:43], s62 offen lds\n"
"v_mfma_f32_16x16x32_bf16 v[28:31], a[228:231], v[224:227], v[28:31]\n"
"ds_read_b64_tr_b16 a[216:217], v103 offset:4096\n"
"v_lshlrev_b32 v11, 5, v11\n"
"v_mfma_f32_16x16x32_bf16 v[28:31], a[72:75], v[232:235], v[28:31]\n"
"ds_read_b64_tr_b16 a[218:219], v103 offset:6144\n"
"v_lshl_add_u32 v11, v8, 3, v11\n"
"v_mfma_f32_16x16x32_bf16 v[28:31], a[236:239], v[240:243], v[28:31]\n"
"ds_read_b64_tr_b16 a[68:69], v104 offset:4096\n"
"v_add_u32 v11, s23, v11\n"
"v_mfma_f32_16x16x32_bf16 v[28:31], a[80:83], v[248:251], v[28:31]\n"
"ds_read_b64_tr_b16 a[70:71], v104 offset:6144\n"
"v_add_u32 v8, s53, v1\n"
"v_mfma_f32_16x16x32_bf16 v[32:35], a[228:231], v[228:231], v[32:35]\n"
"ds_read_b64_tr_b16 a[224:225], v105 offset:4096\n"
"buffer_load_dwordx4 v3, s[40:43], s62 offen offset:1024 lds\n"
"v_mfma_f32_16x16x32_bf16 v[32:35], a[72:75], v[236:239], v[32:35]\n"
"ds_read_b64_tr_b16 a[226:227], v105 offset:6144\n"
"v_mfma_f32_16x16x32_bf16 v[32:35], a[236:239], v[244:247], v[32:35]\n"
"ds_read_b64_tr_b16 a[60:61], v106 offset:4096\n"
"v_mfma_f32_16x16x32_bf16 v[32:35], a[80:83], v[252:255], v[32:35]\n"
"ds_read_b64_tr_b16 a[62:63], v106 offset:6144\n"
"v_mul_f32 v28, v22, v28\n"
"v_mfma_f32_16x16x32_bf16 a[96:99], a[228:231], a[212:215], a[96:99]\n"
"s_waitcnt lgkmcnt(6)\n"
"ds_read_b64_tr_b16 a[44:45], v107 offset:4096\n"
"v_mul_f32 v29, v22, v29\n"
"v_mfma_f32_16x16x32_bf16 v[36:39], a[52:55], v[224:227], v[36:39]\n"
"ds_read_b64_tr_b16 a[46:47], v107 offset:6144\n"
"buffer_load_dwordx4 v4, s[40:43], s62 offen offset:2048 lds\n"
"v_mfma_f32_16x16x32_bf16 v[36:39], a[88:91], v[232:235], v[36:39]\n"
"ds_read_b64_tr_b16 a[84:85], v108 offset:4096\n"
"v_mul_f32 v30, v22, v30\n"
"v_mfma_f32_16x16x32_bf16 v[36:39], a[244:247], v[240:243], v[36:39]\n"
"ds_read_b64_tr_b16 a[86:87], v108 offset:6144\n"
"v_mul_f32 v31, v22, v31\n"
"v_mfma_f32_16x16x32_bf16 v[36:39], a[216:219], v[248:251], v[36:39]\n"
"ds_read_b64_tr_b16 a[132:133], v109 offset:4096\n"
"v_cvt_pk_bf16_f32 v128, v28, v29\n"
"v_mfma_f32_16x16x32_bf16 v[40:43], a[52:55], v[228:231], v[40:43]\n"
"ds_read_b64_tr_b16 a[134:135], v109 offset:6144\n"
"v_cvt_pk_bf16_f32 v129, v30, v31\n"
"v_mfma_f32_16x16x32_bf16 v[40:43], a[88:91], v[236:239], v[40:43]\n"
"ds_read_b64_tr_b16 a[76:77], v110 offset:4096\n"
"buffer_load_dwordx4 v5, s[40:43], s62 offen offset:3072 lds\n"
"v_mfma_f32_16x16x32_bf16 v[40:43], a[244:247], v[244:247], v[40:43]\n"
"s_add_u32 m0, s53, 19456\n"
"ds_read_b64_tr_b16 a[78:79], v110 offset:6144\n"
"v_mul_f32 v32, v23, v32\n"
"v_mfma_f32_16x16x32_bf16 v[40:43], a[216:219], v[252:255], v[40:43]\n"
"ds_read_b64_tr_b16 v[160:161], v111 offset:4096\n"
"v_mul_f32 v33, v23, v33\n"
"v_mfma_f32_16x16x32_bf16 a[100:103], a[52:55], a[212:215], a[100:103]\n"
"s_waitcnt lgkmcnt(6)\n"
"ds_read_b64_tr_b16 v[162:163], v111 offset:6144\n"
"v_mul_f32 v34, v23, v34\n"
"v_mfma_f32_16x16x32_bf16 v[44:47], a[68:71], v[224:227], v[44:47]\n"
"s_waitcnt vmcnt(24)\n"
"s_barrier\n"
"ds_read_b64_tr_b16 a[112:113], v12 offset:30720\n"
"v_mfma_f32_16x16x32_bf16 v[44:47], a[224:227], v[232:235], v[44:47]\n"
"buffer_load_dwordx4 v6, s[40:43], s62 offen lds\n"
"v_mfma_f32_16x16x32_bf16 v[44:47], a[60:63], v[240:243], v[44:47]\n"
"ds_write_b64 v8, a[96:97] offset:0\n"
"v_mfma_f32_16x16x32_bf16 v[44:47], a[44:47], v[248:251], v[44:47]\n"
"ds_write_b64 v8, a[98:99] offset:8\n"
"v_mfma_f32_16x16x32_bf16 v[48:51], a[68:71], v[228:231], v[48:51]\n"
"s_add_u32 s62, s10, 0\n"
"s_add_u32 m0, s53, 10240\n"
"v_mul_f32 v35, v23, v35\n"
"v_mfma_f32_16x16x32_bf16 v[48:51], a[224:227], v[236:239], v[48:51]\n"
"buffer_load_dwordx4 v2, s[40:43], s62 offen lds\n"
"v_mfma_f32_16x16x32_bf16 v[48:51], a[60:63], v[244:247], v[48:51]\n"
"ds_read_b64_tr_b16 a[114:115], v12 offset:32000\n"
"v_cvt_pk_bf16_f32 v136, v32, v33\n"
"v_mfma_f32_16x16x32_bf16 v[48:51], a[44:47], v[252:255], v[48:51]\n"
"ds_read_b64_tr_b16 a[116:117], v12 offset:33280\n"
"v_cvt_pk_bf16_f32 v137, v34, v35\n"
"v_mfma_f32_16x16x32_bf16 a[104:107], a[68:71], a[212:215], a[104:107]\n"
"s_waitcnt lgkmcnt(4)\n"
"ds_read_b64_tr_b16 a[118:119], v12 offset:34560\n"
"v_mul_f32 v36, v22, v36\n"
"v_mfma_f32_16x16x32_bf16 v[52:55], a[84:87], v[224:227], v[52:55]\n"
"ds_read_b64_tr_b16 a[120:121], v13 offset:30720\n"
"v_mul_f32 v37, v22, v37\n"
"v_mfma_f32_16x16x32_bf16 v[52:55], a[132:135], v[232:235], v[52:55]\n"
"ds_read_b64_tr_b16 a[122:123], v13 offset:32000\n"
"v_mul_f32 v38, v22, v38\n"
"v_mfma_f32_16x16x32_bf16 v[52:55], a[76:79], v[240:243], v[52:55]\n"
"ds_write_b64 v8, a[100:101] offset:1024\n"
"buffer_load_dwordx4 v3, s[40:43], s62 offen offset:1024 lds\n"
"v_mul_f32 v39, v22, v39\n"
"v_mfma_f32_16x16x32_bf16 v[52:55], v[160:163], v[248:251], v[52:55]\n"
"ds_read_b64_tr_b16 a[124:125], v13 offset:33280\n"
"ds_write_b64 v8, a[102:103] offset:1032\n"
"v_cvt_pk_bf16_f32 v130, v36, v37\n"
"v_cvt_pk_bf16_f32 v131, v38, v39\n"
"v_mov_b32 v26, s58\n"
"v_mov_b32 v27, s59\n"
"v_mfma_f32_16x16x32_bf16 v[56:59], a[84:87], v[228:231], v[56:59]\n"
"ds_read_b64_tr_b16 a[126:127], v13 offset:34560\n"
"v_mul_f32 v40, v23, v40\n"
"v_mfma_f32_16x16x32_bf16 v[56:59], a[132:135], v[236:239], v[56:59]\n"
"s_waitcnt vmcnt(9)\n"
"s_barrier\n"
"ds_read_b64_tr_b16 a[140:141], v14 offset:30720\n"
"v_mul_f32 v41, v23, v41\n"
"v_mul_f32 v42, v23, v42\n"
"v_mfma_f32_16x16x32_bf16 v[56:59], a[76:79], v[244:247], v[56:59]\n"
"ds_read_b64_tr_b16 a[142:143], v14 offset:32000\n"
"v_mul_f32 v43, v23, v43\n"
"v_mfma_f32_16x16x32_bf16 v[56:59], v[160:163], v[252:255], v[56:59]\n"
"s_mov_b64 exec, 0xff\n"
"buffer_store_dwordx4 v[24:27], v1, s[44:47], s70 offen offset:512 sc1\n"
"s_mov_b64 exec, -1\n"
"ds_read_b64_tr_b16 a[220:221], v14 offset:33280\n"
"v_cvt_pk_bf16_f32 v138, v40, v41\n"
"v_cvt_pk_bf16_f32 v139, v42, v43\n"
"v_mfma_f32_16x16x32_bf16 a[108:111], a[84:87], a[212:215], a[108:111]\n"
"s_waitcnt lgkmcnt(7)\n"
"ds_read_b64_tr_b16 a[222:223], v14 offset:34560\n"
"v_mul_f32 v44, v22, v44\n"
"v_mfma_f32_16x16x32_bf16 v[164:167], a[112:115], v[64:67], 0\n"
"ds_read_b64_tr_b16 a[196:197], v15 offset:30720\n"
"v_mul_f32 v45, v22, v45\n"
"v_mfma_f32_16x16x32_bf16 v[164:167], a[116:119], v[68:71], v[164:167]\n"
"ds_read_b64_tr_b16 a[198:199], v15 offset:32000\n"
"v_mul_f32 v46, v22, v46\n"
"v_mfma_f32_16x16x32_bf16 v[168:171], a[112:115], v[72:75], 0\n"
"ds_read_b64_tr_b16 a[148:149], v15 offset:33280\n"
"ds_write_b64 v8, a[104:105] offset:2048\n"
"buffer_load_dwordx4 v4, s[40:43], s62 offen offset:2048 lds\n"
"v_mul_f32 v47, v22, v47\n"
"v_mfma_f32_16x16x32_bf16 v[168:171], a[116:119], v[76:79], v[168:171]\n"
"ds_read_b64_tr_b16 a[150:151], v15 offset:34560\n"
"ds_write_b64 v8, a[106:107] offset:2056\n"
"v_cvt_pk_bf16_f32 v132, v44, v45\n"
"v_cvt_pk_bf16_f32 v133, v46, v47\n"
"v_mfma_f32_16x16x32_bf16 v[172:175], a[120:123], v[64:67], 0\n"
"s_waitcnt lgkmcnt(8)\n"
"ds_read_b64_tr_b16 a[128:129], v16 offset:30720\n"
"v_mul_f32 v48, v23, v48\n"
"v_mfma_f32_16x16x32_bf16 v[172:175], a[124:127], v[68:71], v[172:175]\n"
"ds_read_b64_tr_b16 a[130:131], v16 offset:32000\n"
"v_mul_f32 v49, v23, v49\n"
"v_mul_f32 v50, v23, v50\n"
"v_mfma_f32_16x16x32_bf16 v[176:179], a[120:123], v[72:75], 0\n"
"ds_read_b64_tr_b16 a[156:157], v16 offset:33280\n"
"v_mul_f32 v51, v23, v51\n"
"v_mfma_f32_16x16x32_bf16 v[176:179], a[124:127], v[76:79], v[176:179]\n"
"ds_read_b64_tr_b16 a[158:159], v16 offset:34560\n"
"buffer_load_dwordx4 v5, s[40:43], s62 offen offset:3072 lds\n"
"v_cvt_pk_bf16_f32 v140, v48, v49\n"
"v_cvt_pk_bf16_f32 v141, v50, v51\n"
"v_mfma_f32_16x16x32_bf16 v[180:183], a[140:143], v[64:67], 0\n"
"s_waitcnt lgkmcnt(8)\n"
"ds_read_b128 v[144:147], v25 offset:61440\n"
"ds_read_b64_tr_b16 a[40:41], v17 offset:30720\n"
"ds_read_b128 v[148:151], v25 offset:62464\n"
"v_mfma_f32_16x16x32_bf16 v[180:183], a[220:223], v[68:71], v[180:183]\n"
"ds_read_b64_tr_b16 a[42:43], v17 offset:32000\n"
"v_mul_f32 v52, v22, v52\n"
"v_mul_f32 v53, v22, v53\n"
"v_mfma_f32_16x16x32_bf16 v[184:187], a[140:143], v[72:75], 0\n"
"s_add_u32 m0, s53, 14336\n"
"ds_read_b64_tr_b16 a[164:165], v17 offset:33280\n"
"v_mul_f32 v54, v22, v54\n"
"v_mfma_f32_16x16x32_bf16 v[184:187], a[220:223], v[76:79], v[184:187]\n"
"ds_read_b64_tr_b16 a[166:167], v17 offset:34560\n"
"ds_write_b64 v8, a[108:109] offset:3072\n"
"v_mul_f32 v55, v22, v55\n"
"v_mfma_f32_16x16x32_bf16 v[188:191], a[196:199], v[64:67], 0\n"
"s_waitcnt lgkmcnt(9)\n"
"ds_read_b64_tr_b16 a[192:193], v18 offset:30720\n"
"ds_write_b64 v8, a[110:111] offset:3080\n"
"buffer_load_dwordx4 v6, s[40:43], s62 offen lds\n"
"v_cvt_pk_bf16_f32 v134, v52, v53\n"
"v_cvt_pk_bf16_f32 v135, v54, v55\n"
"v_mfma_f32_16x16x32_bf16 v[188:191], a[148:151], v[68:71], v[188:191]\n"
"ds_read_b64_tr_b16 a[194:195], v18 offset:32000\n"
"v_mul_f32 v56, v23, v56\n"
"v_mfma_f32_16x16x32_bf16 v[192:195], a[196:199], v[72:75], 0\n"
"ds_read_b64_tr_b16 a[172:173], v18 offset:33280\n"
"v_mul_f32 v57, v23, v57\n"
"v_mul_f32 v58, v23, v58\n"
"v_mfma_f32_16x16x32_bf16 v[192:195], a[148:151], v[76:79], v[192:195]\n"
"s_waitcnt lgkmcnt(8)\n"
"ds_read_b64_tr_b16 a[174:175], v18 offset:34560\n"
"v_mul_f32 v59, v23, v59\n"
"v_mfma_f32_16x16x32_bf16 v[244:247], a[196:199], v[144:147], 0\n"
"ds_read_b64_tr_b16 a[136:137], v19 offset:30720\n"
"v_cvt_pk_bf16_f32 v142, v56, v57\n"
"v_cvt_pk_bf16_f32 v143, v58, v59\n"
"v_mfma_f32_16x16x32_bf16 v[244:247], a[148:151], v[148:151], v[244:247]\n"
"ds_read_b64_tr_b16 a[138:139], v19 offset:32000\n"
"v_mfma_f32_16x16x32_bf16 v[196:199], a[128:131], v[64:67], 0\n"
"ds_read_b64_tr_b16 a[232:233], v19 offset:33280\n"
"v_mfma_f32_16x16x32_bf16 v[196:199], a[156:159], v[68:71], v[196:199]\n"
"ds_read_b64_tr_b16 a[234:235], v19 offset:34560\n"
"v_mfma_f32_16x16x32_bf16 v[200:203], a[128:131], v[72:75], 0\n"
"ds_read_b64_tr_b16 a[152:153], v20 offset:30720\n"
"v_mfma_f32_16x16x32_bf16 v[200:203], a[156:159], v[76:79], v[200:203]\n"
"s_waitcnt lgkmcnt(6)\n"
"ds_read_b64_tr_b16 a[154:155], v20 offset:32000\n"
"s_add_u32 s62, s11, 0\n"
"v_mfma_f32_16x16x32_bf16 v[204:207], a[40:43], v[64:67], 0\n"
"s_add_u32 m0, s53, 5120\n"
"ds_read_b64_tr_b16 a[144:145], v20 offset:33280\n"
"v_mfma_f32_16x16x32_bf16 v[204:207], a[164:167], v[68:71], v[204:207]\n"
"ds_read_b64_tr_b16 a[146:147], v20 offset:34560\n"
"v_mfma_f32_16x16x32_bf16 v[208:211], a[40:43], v[72:75], 0\n"
"ds_read_b64_tr_b16 a[168:169], v21 offset:30720\n"
"v_mfma_f32_16x16x32_bf16 v[208:211], a[164:167], v[76:79], v[208:211]\n"
"ds_read_b64_tr_b16 a[170:171], v21 offset:32000\n"
"v_mfma_f32_16x16x32_bf16 v[212:215], a[192:195], v[64:67], 0\n"
"s_waitcnt lgkmcnt(6)\n"
"ds_read_b64_tr_b16 a[160:161], v21 offset:33280\n"
"v_mfma_f32_16x16x32_bf16 v[212:215], a[172:175], v[68:71], v[212:215]\n"
"ds_read_b64_tr_b16 a[162:163], v21 offset:34560\n"
"v_mfma_f32_16x16x32_bf16 v[216:219], a[192:195], v[72:75], 0\n"
"s_barrier\n"
"ds_read_b64_tr_b16 a[236:237], v12 offset:35840\n"
"v_mfma_f32_16x16x32_bf16 v[216:219], a[172:175], v[76:79], v[216:219]\n"
"buffer_load_dwordx4 v2, s[40:43], s62 offen lds\n"
"v_mfma_f32_16x16x32_bf16 v[248:251], a[192:195], v[144:147], 0\n"
"ds_read_b128 v[112:115], v9 offset:0\n"
"v_mfma_f32_16x16x32_bf16 v[248:251], a[172:175], v[148:151], v[248:251]\n"
"ds_read_b128 v[116:119], v9 offset:40960\n"
"v_mfma_f32_16x16x32_bf16 v[220:223], a[136:139], v[64:67], 0\n"
"ds_read_b64_tr_b16 a[238:239], v12 offset:37120\n"
"v_mfma_f32_16x16x32_bf16 v[220:223], a[232:235], v[68:71], v[220:223]\n"
"ds_read_b64_tr_b16 a[228:229], v12 offset:38400\n"
"v_mfma_f32_16x16x32_bf16 v[224:227], a[136:139], v[72:75], 0\n"
"buffer_load_dwordx2 v[124:125], v7, s[44:47], 0 offen offset:520 sc1\n"
"v_mfma_f32_16x16x32_bf16 v[224:227], a[232:235], v[76:79], v[224:227]\n"
"s_waitcnt lgkmcnt(4)\n"
"ds_read_b64_tr_b16 a[230:231], v12 offset:39680\n"
"v_mfma_f32_16x16x32_bf16 v[228:231], a[152:155], v[64:67], 0\n"
"ds_read_b64_tr_b16 a[244:245], v13 offset:35840\n"
"v_mfma_f32_16x16x32_bf16 v[228:231], a[144:147], v[68:71], v[228:231]\n"
"ds_read_b64_tr_b16 a[246:247], v13 offset:37120\n"
"v_mfma_f32_16x16x32_bf16 v[232:235], a[152:155], v[72:75], 0\n"
"buffer_load_dwordx4 v3, s[40:43], s62 offen offset:1024 lds\n"
"v_mfma_f32_16x16x32_bf16 v[232:235], a[144:147], v[76:79], v[232:235]\n"
"ds_read_b64_tr_b16 a[216:217], v13 offset:38400\n"
"s_waitcnt lgkmcnt(5)\n"
"v_add_f32 v112, v112, v116\n"
"v_mfma_f32_16x16x32_bf16 v[252:255], a[152:155], v[144:147], 0\n"
"ds_read_b64_tr_b16 a[218:219], v13 offset:39680\n"
"v_add_f32 v113, v113, v117\n"
"v_mfma_f32_16x16x32_bf16 v[252:255], a[144:147], v[148:151], v[252:255]\n"
"ds_read_b64_tr_b16 a[224:225], v14 offset:35840\n"
"v_add_f32 v114, v114, v118\n"
"v_mfma_f32_16x16x32_bf16 v[236:239], a[168:171], v[64:67], 0\n"
"ds_read_b64_tr_b16 a[226:227], v14 offset:37120\n"
"v_add_f32 v115, v115, v119\n"
"v_mfma_f32_16x16x32_bf16 v[236:239], a[160:163], v[68:71], v[236:239]\n"
"ds_read_b64_tr_b16 a[44:45], v14 offset:38400\n"
"v_mfma_f32_16x16x32_bf16 v[240:243], a[168:171], v[72:75], 0\n"
"ds_read_b128 v[116:119], v10 offset:0\n"
"buffer_load_dwordx4 v4, s[40:43], s62 offen offset:2048 lds\n"
"v_mfma_f32_16x16x32_bf16 v[240:243], a[160:163], v[76:79], v[240:243]\n"
"ds_read_b64_tr_b16 a[46:47], v14 offset:39680\n"
"s_nop 0\n"
".Lpad14_%=:\n"
"s_cbranch_execz .Lwb_%=\n"
"v_mfma_f32_16x16x32_bf16 v[164:167], a[236:239], v[80:83], v[164:167]\n"
"s_waitcnt lgkmcnt(6)\n"
"ds_read_b64_tr_b16 a[132:133], v15 offset:35840\n"
"v_mfma_f32_16x16x32_bf16 v[164:167], a[228:231], v[84:87], v[164:167]\n"
"ds_read_b64_tr_b16 a[134:135], v15 offset:37120\n"
"v_mfma_f32_16x16x32_bf16 v[168:171], a[236:239], v[88:91], v[168:171]\n"
"ds_read_b64_tr_b16 v[160:161], v15 offset:38400\n"
"v_mfma_f32_16x16x32_bf16 v[168:171], a[228:231], v[92:95], v[168:171]\n"
"ds_read_b64_tr_b16 v[162:163], v15 offset:39680\n"
"buffer_load_dwordx4 v5, s[40:43], s62 offen offset:3072 lds\n"
"v_mfma_f32_16x16x32_bf16 v[172:175], a[244:247], v[80:83], v[172:175]\n"
"ds_read_b64_tr_b16 a[212:213], v16 offset:35840\n"
"s_waitcnt lgkmcnt(6)\n"
"v_add_f32 v112, v112, v116\n"
"v_mfma_f32_16x16x32_bf16 v[172:175], a[216:219], v[84:87], v[172:175]\n"
"s_add_u32 m0, s53, 9216\n"
"ds_read_b64_tr_b16 a[214:215], v16 offset:37120\n"
"v_add_f32 v113, v113, v117\n"
"v_mfma_f32_16x16x32_bf16 v[176:179], a[244:247], v[88:91], v[176:179]\n"
"ds_read_b64_tr_b16 v[108:109], v16 offset:38400\n"
"v_add_f32 v114, v114, v118\n"
"v_mfma_f32_16x16x32_bf16 v[176:179], a[216:219], v[92:95], v[176:179]\n"
"ds_read_b64_tr_b16 v[110:111], v16 offset:39680\n"
"v_add_f32 v115, v115, v119\n"
"v_mfma_f32_16x16x32_bf16 v[180:183], a[224:227], v[80:83], v[180:183]\n"
"s_waitcnt lgkmcnt(6)\n"
"ds_read_b128 v[152:155], v25 offset:63488\n"
"ds_read_b64_tr_b16 v[96:97], v17 offset:35840\n"
"ds_read_b128 v[156:159], v25 offset:64512\n"
"buffer_load_dwordx4 v6, s[40:43], s62 offen lds\n"
"v_mfma_f32_16x16x32_bf16 v[180:183], a[44:47], v[84:87], v[180:183]\n"
"ds_read_b64_tr_b16 v[98:99], v17 offset:37120\n"
"ds_read_b128 v[116:119], v10 offset:40960\n"
"v_mfma_f32_16x16x32_bf16 v[184:187], a[224:227], v[88:91], v[184:187]\n"
"ds_read_b64_tr_b16 v[100:101], v17 offset:38400\n"
"v_mfma_f32_16x16x32_bf16 v[184:187], a[44:47], v[92:95], v[184:187]\n"
"ds_read_b64_tr_b16 v[102:103], v17 offset:39680\n"
"v_mfma_f32_16x16x32_bf16 v[188:191], a[132:135], v[80:83], v[188:191]\n"
"s_waitcnt lgkmcnt(9)\n"
"ds_read_b64_tr_b16 v[104:105], v18 offset:35840\n"
"v_mfma_f32_16x16x32_bf16 v[188:191], v[160:163], v[84:87], v[188:191]\n"
"ds_read_b64_tr_b16 v[106:107], v18 offset:37120\n"
"v_mfma_f32_16x16x32_bf16 v[192:195], a[132:135], v[88:91], v[192:195]\n"
"ds_read_b64_tr_b16 a[112:113], v18 offset:38400\n"
"s_waitcnt lgkmcnt(5)\n"
"v_add_f32 v112, v112, v116\n"
"v_mfma_f32_16x16x32_bf16 v[192:195], v[160:163], v[92:95], v[192:195]\n"
"ds_read_b64_tr_b16 a[114:115], v18 offset:39680\n"
"v_add_f32 v113, v113, v117\n"
"v_mfma_f32_16x16x32_bf16 v[244:247], a[132:135], v[152:155], v[244:247]\n"
"ds_read_b64_tr_b16 a[116:117], v19 offset:35840\n"
"v_add_f32 v114, v114, v118\n"
"v_mfma_f32_16x16x32_bf16 v[244:247], v[160:163], v[156:159], v[244:247]\n"
"ds_read_b64_tr_b16 a[118:119], v19 offset:37120\n"
"v_add_f32 v115, v115, v119\n"
"v_mfma_f32_16x16x32_bf16 v[196:199], a[212:215], v[80:83], v[196:199]\n"
"ds_read_b64_tr_b16 a[120:121], v19 offset:38400\n"
"v_mul_f32 v112, v24, v112\n"
"v_mfma_f32_16x16x32_bf16 v[196:199], v[108:111], v[84:87], v[196:199]\n"
"ds_read_b64_tr_b16 a[122:123], v19 offset:39680\n"
"v_mul_f32 v113, v24, v113\n"
"v_mfma_f32_16x16x32_bf16 v[200:203], a[212:215], v[88:91], v[200:203]\n"
"ds_read_b64_tr_b16 a[124:125], v20 offset:35840\n"
"v_mul_f32 v114, v24, v114\n"
"v_mul_f32 v115, v24, v115\n"
"v_mfma_f32_16x16x32_bf16 v[200:203], v[108:111], v[92:95], v[200:203]\n"
"s_add_u32 s62, s9, 81920\n"
"ds_read_b64_tr_b16 a[126:127], v20 offset:37120\n"
"v_cvt_pk_bf16_f32 v120, v112, v113\n"
"v_mfma_f32_16x16x32_bf16 v[204:207], v[96:99], v[80:83], v[204:207]\n"
"s_waitcnt lgkmcnt(6)\n"
"ds_read_b64_tr_b16 a[140:141], v20 offset:38400\n"
"v_cvt_pk_bf16_f32 v121, v114, v115\n"
"v_mfma_f32_16x16x32_bf16 v[204:207], v[100:103], v[84:87], v[204:207]\n"
"s_add_u32 m0, s53, 0\n"
"ds_read_b64_tr_b16 a[142:143], v20 offset:39680\n"
"v_add_u32 v123, s22, v25\n"
"v_mfma_f32_16x16x32_bf16 v[208:211], v[96:99], v[88:91], v[208:211]\n"
"ds_read_b64_tr_b16 a[220:221], v21 offset:35840\n"
"v_mfma_f32_16x16x32_bf16 v[208:211], v[100:103], v[92:95], v[208:211]\n"
"s_add_u32 s21, s73, 0\n"
"ds_read_b64_tr_b16 a[222:223], v21 offset:37120\n"
"v_mfma_f32_16x16x32_bf16 v[212:215], v[104:107], v[80:83], v[212:215]\n"
"ds_read_b64_tr_b16 a[196:197], v21 offset:38400\n"
"v_mfma_f32_16x16x32_bf16 v[212:215], a[112:115], v[84:87], v[212:215]\n"
"s_waitcnt vmcnt(4)\n"
"v_cmp_ne_u64 vcc, v[124:125], s[58:59]\n"
"ds_read_b64_tr_b16 a[198:199], v21 offset:39680\n"
"v_mfma_f32_16x16x32_bf16 v[216:219], v[104:107], v[88:91], v[216:219]\n"
"s_barrier\n"
"ds_write_b64 v123, v[120:121] offset:61440\n"
"v_mfma_f32_16x16x32_bf16 v[216:219], a[112:115], v[92:95], v[216:219]\n"
"buffer_load_dwordx4 v2, s[40:43], s62 offen lds\n"
"v_mfma_f32_16x16x32_bf16 v[248:251], v[104:107], v[152:155], v[248:251]\n"
"s_cbranch_vccnz .Lsp0_%=\n"
".Lpr1_%=:\n"
"ds_read_b64_tr_b16 a[148:149], v12 offset:25600\n"
"v_mfma_f32_16x16x32_bf16 v[248:251], a[112:115], v[156:159], v[248:251]\n"
"s_waitcnt lgkmcnt(5)\n"
"ds_read_b64_tr_b16 a[150:151], v12 offset:26880\n"
"v_mfma_f32_16x16x32_bf16 v[220:223], a[116:119], v[80:83], v[220:223]\n"
"ds_read_b64_tr_b16 a[128:129], v12 offset:28160\n"
"v_mfma_f32_16x16x32_bf16 v[220:223], a[120:123], v[84:87], v[220:223]\n"
"buffer_load_dwordx4 a[248:251], v1, s[44:47], s21 offen offset:0 sc1\n"
"v_mfma_f32_16x16x32_bf16 v[224:227], a[116:119], v[88:91], v[224:227]\n"
"ds_read_b64_tr_b16 a[130:131], v12 offset:29440\n"
"v_mfma_f32_16x16x32_bf16 v[224:227], a[120:123], v[92:95], v[224:227]\n"
"ds_read_b64_tr_b16 a[156:157], v13 offset:25600\n"
"v_mfma_f32_16x16x32_bf16 v[228:231], a[124:127], v[80:83], v[228:231]\n"
"buffer_load_dwordx4 a[252:255], v1, s[44:47], s21 offen offset:1024 sc1\n"
"v_mfma_f32_16x16x32_bf16 v[228:231], a[140:143], v[84:87], v[228:231]\n"
"s_add_u32 s21, s67, 0\n"
"ds_read_b64_tr_b16 a[158:159], v13 offset:26880\n"
"v_mfma_f32_16x16x32_bf16 v[232:235], a[124:127], v[88:91], v[232:235]\n"
"ds_read_b64_tr_b16 a[40:41], v13 offset:28160\n"
"v_mfma_f32_16x16x32_bf16 v[232:235], a[140:143], v[92:95], v[232:235]\n"
"buffer_load_dwordx4 a[48:51], v1, s[44:47], s21 offen offset:0 sc1\n"
"v_mfma_f32_16x16x32_bf16 v[252:255], a[124:127], v[152:155], v[252:255]\n"
"ds_read_b64_tr_b16 a[42:43], v13 offset:29440\n"
"v_mfma_f32_16x16x32_bf16 v[252:255], a[140:143], v[156:159], v[252:255]\n"
"s_waitcnt lgkmcnt(4)\n"
"ds_read_b64_tr_b16 a[164:165], v14 offset:25600\n"
"v_mfma_f32_16x16x32_bf16 v[236:239], a[220:223], v[80:83], v[236:239]\n"
"ds_read_b64_tr_b16 a[166:167], v14 offset:26880\n"
"v_mfma_f32_16x16x32_bf16 v[236:239], a[196:199], v[84:87], v[236:239]\n"
"buffer_load_dwordx4 a[52:55], v1, s[44:47], s21 offen offset:1024 sc1\n"
"v_mfma_f32_16x16x32_bf16 v[240:243], a[220:223], v[88:91], v[240:243]\n"
"ds_read_b64_tr_b16 a[192:193], v14 offset:28160\n"
"v_mfma_f32_16x16x32_bf16 v[240:243], a[196:199], v[92:95], v[240:243]\n"
"ds_read_b64_tr_b16 a[194:195], v14 offset:29440\n"
"v_mfma_f32_16x16x32_bf16 v[164:167], a[148:151], v[128:131], v[164:167]\n"
"s_mov_b32 s6, 320\n"
"ds_read_b64_tr_b16 a[172:173], v15 offset:25600\n"
"v_mfma_f32_16x16x32_bf16 v[164:167], a[128:131], v[132:135], v[164:167]\n"
"ds_read_b64_tr_b16 a[174:175], v15 offset:26880\n"
"buffer_load_dwordx4 a[56:59], v1, s[44:47], s21 offen offset:2048 sc1\n"
"v_mfma_f32_16x16x32_bf16 v[168:171], a[148:151], v[136:139], v[168:171]\n"
"s_barrier\n"
"ds_read_b64_tr_b16 a[136:137], v15 offset:28160\n"
"v_mfma_f32_16x16x32_bf16 v[168:171], a[128:131], v[140:143], v[168:171]\n"
"s_waitcnt lgkmcnt(5)\n"
"ds_read_b64_tr_b16 a[138:139], v15 offset:29440\n"
"s_mul_i32 s14, s52, 90\n"
"v_mfma_f32_16x16x32_bf16 v[172:175], a[156:159], v[128:131], v[172:175]\n"
"ds_read_b64_tr_b16 a[232:233], v16 offset:25600\n"
"v_add_u32 v30, s14, v0\n"
"v_mfma_f32_16x16x32_bf16 v[172:175], a[40:43], v[132:135], v[172:175]\n"
"ds_read_b64_tr_b16 a[234:235], v16 offset:26880\n"
"v_cmp_gt_u32 s[4:5], s6, v30\n"
"v_mfma_f32_16x16x32_bf16 v[176:179], a[156:159], v[136:139], v[176:179]\n"
"ds_read_b64_tr_b16 a[152:153], v16 offset:28160\n"
"buffer_load_dwordx4 a[60:63], v1, s[44:47], s21 offen offset:3072 sc1\n"
"v_mfma_f32_16x16x32_bf16 v[176:179], a[40:43], v[140:143], v[176:179]\n"
"s_add_u32 s21, s68, 0\n"
"ds_read_b64_tr_b16 a[154:155], v16 offset:29440\n"
"s_mul_i32 s15, s54, 5\n"
"v_mfma_f32_16x16x32_bf16 v[180:183], a[164:167], v[128:131], v[180:183]\n"
"s_waitcnt lgkmcnt(6)\n"
"ds_read_b128 v[144:147], v25 offset:61440\n"
"ds_read_b64_tr_b16 a[144:145], v17 offset:25600\n"
"ds_read_b128 v[148:151], v25 offset:62464\n"
"v_mfma_f32_16x16x32_bf16 v[180:183], a[192:195], v[132:135], v[180:183]\n"
"ds_read_b64_tr_b16 a[146:147], v17 offset:26880\n"
"v_lshrrev_b32 v31, 6, v30\n"
"v_mfma_f32_16x16x32_bf16 v[184:187], a[164:167], v[136:139], v[184:187]\n"
"ds_read_b64_tr_b16 v[72:73], v17 offset:28160\n"
"v_add_u32 v31, s15, v31\n"
"v_mfma_f32_16x16x32_bf16 v[184:187], a[192:195], v[140:143], v[184:187]\n"
"ds_read_b64_tr_b16 v[74:75], v17 offset:29440\n"
"buffer_load_dwordx4 a[64:67], v1, s[44:47], s21 offen offset:0 sc1\n"
"v_mfma_f32_16x16x32_bf16 v[188:191], a[172:175], v[128:131], v[188:191]\n"
"s_waitcnt lgkmcnt(8)\n"
"ds_read_b64_tr_b16 a[168:169], v18 offset:25600\n"
"v_and_b32 v32, 63, v30\n"
"v_mfma_f32_16x16x32_bf16 v[188:191], a[136:139], v[132:135], v[188:191]\n"
"ds_read_b64_tr_b16 a[170:171], v18 offset:26880\n"
"v_lshlrev_b32 v33, 10, v31\n"
"v_mfma_f32_16x16x32_bf16 v[192:195], a[172:175], v[136:139], v[192:195]\n"
"ds_read_b64_tr_b16 v[76:77], v18 offset:28160\n"
"v_lshl_add_u32 v33, v32, 4, v33\n"
"v_mfma_f32_16x16x32_bf16 v[192:195], a[136:139], v[140:143], v[192:195]\n"
"s_waitcnt lgkmcnt(6)\n"
"ds_read_b64_tr_b16 v[78:79], v18 offset:29440\n"
"v_mul_u32_u24 v34, 205, v31\n"
"v_mfma_f32_16x16x32_bf16 v[244:247], a[172:175], v[144:147], v[244:247]\n"
"ds_read_b64_tr_b16 a[160:161], v19 offset:25600\n"
"buffer_load_dwordx4 a[68:71], v1, s[44:47], s21 offen offset:1024 sc1\n"
"v_mfma_f32_16x16x32_bf16 v[244:247], a[136:139], v[148:151], v[244:247]\n"
"ds_read_b64_tr_b16 a[162:163], v19 offset:26880\n"
"v_lshrrev_b32 v34, 11, v34\n"
"v_mfma_f32_16x16x32_bf16 v[196:199], a[232:235], v[128:131], v[196:199]\n"
"ds_read_b64_tr_b16 a[236:237], v19 offset:28160\n"
"v_mul_u32_u24 v35, 10, v34\n"
"v_mfma_f32_16x16x32_bf16 v[196:199], a[152:155], v[132:135], v[196:199]\n"
"ds_read_b64_tr_b16 a[238:239], v19 offset:29440\n"
"v_sub_u32 v35, v31, v35\n"
"v_mfma_f32_16x16x32_bf16 v[200:203], a[232:235], v[136:139], v[200:203]\n"
"ds_read_b64_tr_b16 a[228:229], v20 offset:25600\n"
"buffer_load_dwordx4 a[72:75], v1, s[44:47], s21 offen offset:2048 sc1\n"
"v_mfma_f32_16x16x32_bf16 v[200:203], a[152:155], v[140:143], v[200:203]\n"
"s_waitcnt lgkmcnt(6)\n"
"ds_read_b64_tr_b16 a[230:231], v20 offset:26880\n"
"v_and_b32 v36, 15, v32\n"
"v_mfma_f32_16x16x32_bf16 v[204:207], a[144:147], v[128:131], v[204:207]\n"
"ds_read_b64_tr_b16 a[244:245], v20 offset:28160\n"
"v_lshl_add_u32 v36, v34, 5, v36\n"
"v_mfma_f32_16x16x32_bf16 v[204:207], v[72:75], v[132:135], v[204:207]\n"
"ds_read_b64_tr_b16 a[246:247], v20 offset:29440\n"
"v_lshrrev_b32 v32, 4, v32\n"
"v_mfma_f32_16x16x32_bf16 v[208:211], a[144:147], v[136:139], v[208:211]\n"
"s_mul_i32 s15, s54, 40\n"
"ds_read_b64_tr_b16 a[216:217], v21 offset:25600\n"
"v_mfma_f32_16x16x32_bf16 v[208:211], v[72:75], v[140:143], v[208:211]\n"
"ds_read_b64_tr_b16 a[218:219], v21 offset:26880\n"
"buffer_load_dwordx4 a[76:79], v1, s[44:47], s21 offen offset:3072 sc1\n"
"v_mfma_f32_16x16x32_bf16 v[212:215], a[168:171], v[128:131], v[212:215]\n"
"s_waitcnt lgkmcnt(6)\n"
"ds_read_b64_tr_b16 a[224:225], v21 offset:28160\n"
"s_add_u32 s21, s69, 0\n"
"v_mfma_f32_16x16x32_bf16 v[212:215], v[76:79], v[132:135], v[212:215]\n"
"ds_read_b64_tr_b16 a[226:227], v21 offset:29440\n"
"v_add_u32 v37, s15, v30\n"
"v_mfma_f32_16x16x32_bf16 v[216:219], a[168:171], v[136:139], v[216:219]\n"
"s_barrier\n"
"ds_read_b64_tr_b16 a[44:45], v12 offset:15360\n"
"v_mfma_f32_16x16x32_bf16 v[216:219], v[76:79], v[140:143], v[216:219]\n"
"buffer_load_dwordx4 a[80:83], v1, s[44:47], s21 offen offset:0 sc1\n"
"v_mfma_f32_16x16x32_bf16 v[248:251], a[168:171], v[144:147], v[248:251]\n"
"ds_read_b64_tr_b16 a[46:47], v12 offset:16640\n"
"v_subrev_u32 v37, 320, v37\n"
"v_mfma_f32_16x16x32_bf16 v[248:251], v[76:79], v[148:151], v[248:251]\n"
"ds_read_b64_tr_b16 a[132:133], v12 offset:17920\n"
"v_lshlrev_b32 v38, 4, v37\n"
"v_mfma_f32_16x16x32_bf16 v[220:223], a[160:163], v[128:131], v[220:223]\n"
"ds_read_b64_tr_b16 a[134:135], v12 offset:19200\n"
"v_add_u32 v38, 40960, v38\n"
"v_mfma_f32_16x16x32_bf16 v[220:223], a[236:239], v[132:135], v[220:223]\n"
"buffer_load_dwordx4 a[84:87], v1, s[44:47], s21 offen offset:1024 sc1\n"
"v_mfma_f32_16x16x32_bf16 v[224:227], a[160:163], v[136:139], v[224:227]\n"
"ds_read_b64_tr_b16 v[160:161], v13 offset:15360\n"
"v_cndmask_b32 v108, v38, v33, s[4:5]\n"
"v_mfma_f32_16x16x32_bf16 v[224:227], a[236:239], v[140:143], v[224:227]\n"
"s_waitcnt lgkmcnt(4)\n"
"ds_read_b64_tr_b16 v[162:163], v13 offset:16640\n"
"v_lshrrev_b32 v38, 5, v37\n"
"v_mfma_f32_16x16x32_bf16 v[228:231], a[228:231], v[128:131], v[228:231]\n"
"ds_read_b64_tr_b16 a[212:213], v13 offset:17920\n"
"v_cndmask_b32 v35, v38, v35, s[4:5]\n"
"v_mfma_f32_16x16x32_bf16 v[228:231], a[244:247], v[132:135], v[228:231]\n"
"buffer_load_dwordx4 a[88:91], v1, s[44:47], s21 offen offset:2048 sc1\n"
"v_mfma_f32_16x16x32_bf16 v[232:235], a[228:231], v[136:139], v[232:235]\n"
"s_waitcnt vmcnt(11)\n"
"ds_write_b64 v11, a[248:249] offset:20480\n"
"v_mfma_f32_16x16x32_bf16 v[232:235], a[244:247], v[140:143], v[232:235]\n"
"s_add_u32 s20, s66, 36864\n"
"ds_read_b64_tr_b16 a[214:215], v13 offset:19200\n"
"v_and_b32 v38, 7, v37\n"
"v_mfma_f32_16x16x32_bf16 v[252:255], a[228:231], v[144:147], v[252:255]\n"
"ds_write_b64 v11, a[250:251] offset:20496\n"
"v_mfma_f32_16x16x32_bf16 v[252:255], a[244:247], v[148:151], v[252:255]\n"
"ds_read_b64_tr_b16 v[96:97], v14 offset:15360\n"
"v_lshlrev_b32 v38, 1, v38\n"
"v_mfma_f32_16x16x32_bf16 v[236:239], a[216:219], v[128:131], v[236:239]\n"
"buffer_load_dwordx4 a[92:95], v1, s[44:47], s21 offen offset:3072 sc1\n"
"v_mfma_f32_16x16x32_bf16 v[236:239], a[224:227], v[132:135], v[236:239]\n"
"ds_read_b64_tr_b16 v[98:99], v14 offset:16640\n"
"v_add_u32 v38, 128, v38\n"
"v_mfma_f32_16x16x32_bf16 v[240:243], a[216:219], v[136:139], v[240:243]\n"
"ds_write_b64 v11, a[252:253] offset:21504\n"
"v_mfma_f32_16x16x32_bf16 v[240:243], a[224:227], v[140:143], v[240:243]\n"
"s_waitcnt lgkmcnt(5)\n"
"ds_read_b64_tr_b16 v[100:101], v14 offset:17920\n"
"s_waitcnt vmcnt(8)\n"
"v_mfma_f32_16x16x32_bf16 v[164:167], a[44:47], a[48:51], v[164:167]\n"
"ds_write_b64 v11, a[254:255] offset:21520\n"
"v_mfma_f32_16x16x32_bf16 v[164:167], a[132:135], a[52:55], v[164:167]\n"
"ds_read_b64_tr_b16 v[102:103], v14 offset:19200\n"
"buffer_store_dwordx4 v[128:131], v1, s[44:47], s20 offen offset:0 sc1\n"
"v_mfma_f32_16x16x32_bf16 v[168:171], a[44:47], a[56:59], v[168:171]\n"
"s_waitcnt lgkmcnt(1)\n"
"s_barrier\n"
"ds_read_b64_tr_b16 v[104:105], v15 offset:15360\n"
"v_mfma_f32_16x16x32_bf16 v[168:171], a[132:135], a[60:63], v[168:171]\n"
"ds_read_b64_tr_b16 v[106:107], v15 offset:16640\n"
"v_cndmask_b32 v36, v38, v36, s[4:5]\n"
"v_mfma_f32_16x16x32_bf16 v[172:175], v[160:163], a[48:51], v[172:175]\n"
"ds_read_b64_tr_b16 a[112:113], v15 offset:17920\n"
"v_bfe_u32 v38, v37, 3, 2\n"
"v_mfma_f32_16x16x32_bf16 v[172:175], a[212:215], a[52:55], v[172:175]\n"
"ds_read_b64_tr_b16 a[114:115], v15 offset:19200\n"
"v_cndmask_b32 v32, v38, v32, s[4:5]\n"
"v_mfma_f32_16x16x32_bf16 v[176:179], v[160:163], a[56:59], v[176:179]\n"
"buffer_store_dwordx4 v[136:139], v1, s[44:47], s20 offen offset:2048 sc1\n"
"v_mfma_f32_16x16x32_bf16 v[176:179], a[212:215], a[60:63], v[176:179]\n"
"ds_read_b64_tr_b16 a[116:117], v16 offset:15360\n"
"v_mul_u32_u24 v36, 1280, v36\n"
"v_mfma_f32_16x16x32_bf16 v[180:183], v[96:99], a[48:51], v[180:183]\n"
"s_waitcnt lgkmcnt(5)\n"
"ds_read_b128 v[152:155], v1 offset:20480\n"
"ds_read_b128 v[156:159], v1 offset:21504\n"
"v_mfma_f32_16x16x32_bf16 v[180:183], v[100:103], a[52:55], v[180:183]\n"
"ds_read_b64_tr_b16 a[118:119], v16 offset:16640\n"
"v_lshl_add_u32 v36, v35, 5, v36\n"
"v_mfma_f32_16x16x32_bf16 v[184:187], v[96:99], a[56:59], v[184:187]\n"
"ds_read_b64_tr_b16 a[120:121], v16 offset:17920\n"
"v_lshl_add_u32 v110, v32, 3, v36\n"
"v_mfma_f32_16x16x32_bf16 v[184:187], v[100:103], a[60:63], v[184:187]\n"
"s_waitcnt lgkmcnt(6)\n"
"ds_read_b64_tr_b16 a[122:123], v16 offset:19200\n"
"v_mov_b32 v38, 1280\n"
"v_mfma_f32_16x16x32_bf16 v[188:191], v[104:107], a[48:51], v[188:191]\n"
"s_waitcnt lgkmcnt(6)\n"
"ds_read_b64_tr_b16 a[124:125], v17 offset:15360\n"
"v_mov_b32 v39, 20480\n"
"v_mfma_f32_16x16x32_bf16 v[188:191], a[112:115], a[52:55], v[188:191]\n"
"ds_read_b64_tr_b16 a[126:127], v17 offset:16640\n"
"buffer_store_dwordx4 v[132:135], v1, s[44:47], s20 offen offset:1024 sc1\n"
"v_mfma_f32_16x16x32_bf16 v[192:195], v[104:107], a[56:59], v[192:195]\n"
"ds_read_b64_tr_b16 a[140:141], v17 offset:17920\n"
"v_cndmask_b32 v38, v38, v39, s[4:5]\n"
"v_mfma_f32_16x16x32_bf16 v[192:195], a[112:115], a[60:63], v[192:195]\n"
"s_waitcnt lgkmcnt(6)\n"
"ds_read_b64_tr_b16 a[142:143], v17 offset:19200\n"
"v_add_u32 v112, v38, v110\n"
"v_mfma_f32_16x16x32_bf16 v[244:247], v[104:107], v[152:155], v[244:247]\n"
"ds_read_b64_tr_b16 v[80:81], v18 offset:15360\n"
"v_min_u32 v30, 25, v0\n"
"v_mfma_f32_16x16x32_bf16 v[244:247], a[112:115], v[156:159], v[244:247]\n"
"s_waitcnt lgkmcnt(6)\n"
"ds_read_b64_tr_b16 v[82:83], v18 offset:16640\n"
"v_add_u32 v30, 64, v30\n"
"v_mfma_f32_16x16x32_bf16 v[196:199], a[116:119], a[48:51], v[196:199]\n"
"s_waitcnt lgkmcnt(6)\n"
"ds_read_b64_tr_b16 v[84:85], v18 offset:17920\n"
"v_add_u32 v30, s14, v30\n"
"v_mfma_f32_16x16x32_bf16 v[196:199], a[120:123], a[52:55], v[196:199]\n"
"ds_read_b64_tr_b16 v[86:87], v18 offset:19200\n"
"buffer_store_dwordx4 v[140:143], v1, s[44:47], s20 offen offset:3072 sc1\n"
"v_mfma_f32_16x16x32_bf16 v[200:203], a[116:119], a[56:59], v[200:203]\n"
"s_add_u32 s20, s20, s61\n"
"ds_read_b64_tr_b16 a[220:221], v19 offset:15360\n"
"v_cmp_gt_u32 s[4:5], s6, v30\n"
"v_mfma_f32_16x16x32_bf16 v[200:203], a[120:123], a[60:63], v[200:203]\n"
"s_waitcnt lgkmcnt(6)\n"
"ds_read_b64_tr_b16 a[222:223], v19 offset:16640\n"
"s_mul_i32 s15, s54, 5\n"
"v_mfma_f32_16x16x32_bf16 v[204:207], a[124:127], a[48:51], v[204:207]\n"
"s_waitcnt lgkmcnt(6)\n"
"ds_read_b64_tr_b16 v[88:89], v19 offset:17920\n"
"v_lshrrev_b32 v31, 6, v30\n"
"v_mfma_f32_16x16x32_bf16 v[204:207], a[140:143], a[52:55], v[204:207]\n"
"ds_read_b64_tr_b16 v[90:91], v19 offset:19200\n"
"v_add_u32 v31, s15, v31\n"
"v_mfma_f32_16x16x32_bf16 v[208:211], a[124:127], a[56:59], v[208:211]\n"
"buffer_store_dwordx2 v[120:121], v122, s[44:47], s20 offen sc1\n"
"v_mfma_f32_16x16x32_bf16 v[208:211], a[140:143], a[60:63], v[208:211]\n"
"s_waitcnt lgkmcnt(5)\n"
"ds_read_b64_tr_b16 v[92:93], v20 offset:15360\n"
"v_and_b32 v32, 63, v30\n"
"v_mfma_f32_16x16x32_bf16 v[212:215], v[80:83], a[48:51], v[212:215]\n"
"s_waitcnt lgkmcnt(5)\n"
"ds_read_b64_tr_b16 v[94:95], v20 offset:16640\n"
"v_lshlrev_b32 v33, 10, v31\n"
"v_mfma_f32_16x16x32_bf16 v[212:215], v[84:87], a[52:55], v[212:215]\n"
"ds_read_b64_tr_b16 a[196:197], v20 offset:17920\n"
"v_lshl_add_u32 v33, v32, 4, v33\n"
"v_mfma_f32_16x16x32_bf16 v[216:219], v[80:83], a[56:59], v[216:219]\n"
"ds_read_b64_tr_b16 a[198:199], v20 offset:19200\n"
"v_mul_u32_u24 v34, 205, v31\n"
"v_mfma_f32_16x16x32_bf16 v[216:219], v[84:87], a[60:63], v[216:219]\n"
"ds_read_b64_tr_b16 a[128:129], v21 offset:15360\n"
"v_lshrrev_b32 v34, 11, v34\n"
"v_mfma_f32_16x16x32_bf16 v[248:251], v[80:83], v[152:155], v[248:251]\n"
"ds_read_b64_tr_b16 a[130:131], v21 offset:16640\n"
"v_mul_u32_u24 v35, 10, v34\n"
"v_mfma_f32_16x16x32_bf16 v[248:251], v[84:87], v[156:159], v[248:251]\n"
"s_waitcnt lgkmcnt(6)\n"
"buffer_load_dwordx4 v3, s[40:43], s62 offen offset:1024 lds\n"
"v_mfma_f32_16x16x32_bf16 v[220:223], a[220:223], a[48:51], v[220:223]\n"
"ds_read_b64_tr_b16 a[40:41], v21 offset:17920\n"
"v_sub_u32 v35, v31, v35\n"
"v_mfma_f32_16x16x32_bf16 v[220:223], v[88:91], a[52:55], v[220:223]\n"
"ds_read_b64_tr_b16 a[42:43], v21 offset:19200\n"
"v_and_b32 v36, 15, v32\n"
"v_mfma_f32_16x16x32_bf16 v[224:227], a[220:223], a[56:59], v[224:227]\n"
"ds_read_b64_tr_b16 a[192:193], v12 offset:10240\n"
"v_lshl_add_u32 v36, v34, 5, v36\n"
"v_mfma_f32_16x16x32_bf16 v[224:227], v[88:91], a[60:63], v[224:227]\n"
"s_waitcnt lgkmcnt(5)\n"
"ds_read_b64_tr_b16 a[194:195], v12 offset:11520\n"
"v_lshrrev_b32 v32, 4, v32\n"
"v_mfma_f32_16x16x32_bf16 v[228:231], v[92:95], a[48:51], v[228:231]\n"
"ds_read_b64_tr_b16 a[136:137], v12 offset:12800\n"
"buffer_load_dwordx4 v4, s[40:43], s62 offen offset:2048 lds\n"
"v_mfma_f32_16x16x32_bf16 v[228:231], a[196:199], a[52:55], v[228:231]\n"
"s_mul_i32 s15, s54, 40\n"
"ds_read_b64_tr_b16 a[138:139], v12 offset:14080\n"
"v_mfma_f32_16x16x32_bf16 v[232:235], v[92:95], a[56:59], v[232:235]\n"
"ds_read_b64_tr_b16 a[232:233], v13 offset:10240\n"
"v_add_u32 v37, s15, v30\n"
"v_mfma_f32_16x16x32_bf16 v[232:235], a[196:199], a[60:63], v[232:235]\n"
"ds_read_b64_tr_b16 a[234:235], v13 offset:11520\n"
"v_subrev_u32 v37, 320, v37\n"
"v_mfma_f32_16x16x32_bf16 v[252:255], v[92:95], v[152:155], v[252:255]\n"
"ds_read_b64_tr_b16 v[72:73], v13 offset:12800\n"
"v_lshlrev_b32 v38, 4, v37\n"
"v_mfma_f32_16x16x32_bf16 v[252:255], a[196:199], v[156:159], v[252:255]\n"
"s_waitcnt lgkmcnt(6)\n"
"ds_read_b64_tr_b16 v[74:75], v13 offset:14080\n"
"buffer_load_dwordx4 v5, s[40:43], s62 offen offset:3072 lds\n"
"v_mfma_f32_16x16x32_bf16 v[236:239], a[128:131], a[48:51], v[236:239]\n"
"s_add_u32 m0, s53, 4096\n"
"ds_read_b64_tr_b16 v[76:77], v14 offset:10240\n"
"v_add_u32 v38, 40960, v38\n"
"v_mfma_f32_16x16x32_bf16 v[236:239], a[40:43], a[52:55], v[236:239]\n"
"s_add_u32 s21, s73, 18432\n"
"ds_read_b64_tr_b16 v[78:79], v14 offset:11520\n"
"v_cndmask_b32 v109, v38, v33, s[4:5]\n"
"v_mfma_f32_16x16x32_bf16 v[240:243], a[128:131], a[56:59], v[240:243]\n"
"ds_read_b64_tr_b16 a[236:237], v14 offset:12800\n"
"v_lshrrev_b32 v38, 5, v37\n"
"v_mfma_f32_16x16x32_bf16 v[240:243], a[40:43], a[60:63], v[240:243]\n"
"s_waitcnt lgkmcnt(6)\n"
"ds_read_b64_tr_b16 a[238:239], v14 offset:14080\n"
"s_waitcnt vmcnt(12)\n"
"v_mfma_f32_16x16x32_bf16 v[164:167], a[192:195], a[64:67], v[164:167]\n"
"ds_read_b64_tr_b16 a[228:229], v15 offset:10240\n"
"buffer_load_dwordx4 v6, s[40:43], s62 offen lds\n"
"v_mfma_f32_16x16x32_bf16 v[164:167], a[136:139], a[68:71], v[164:167]\n"
"ds_read_b64_tr_b16 a[230:231], v15 offset:11520\n"
"v_cndmask_b32 v35, v38, v35, s[4:5]\n"
"v_mfma_f32_16x16x32_bf16 v[168:171], a[192:195], a[72:75], v[168:171]\n"
"ds_read_b64_tr_b16 a[244:245], v15 offset:12800\n"
"v_and_b32 v38, 7, v37\n"
"v_mfma_f32_16x16x32_bf16 v[168:171], a[136:139], a[76:79], v[168:171]\n"
"s_waitcnt lgkmcnt(6)\n"
"ds_read_b64_tr_b16 a[246:247], v15 offset:14080\n"
"v_lshlrev_b32 v38, 1, v38\n"
"v_mfma_f32_16x16x32_bf16 v[172:175], a[232:235], a[64:67], v[172:175]\n"
"ds_read_b64_tr_b16 v[128:129], v16 offset:10240\n"
"v_add_u32 v38, 128, v38\n"
"v_mfma_f32_16x16x32_bf16 v[172:175], v[72:75], a[68:71], v[172:175]\n"
"ds_read_b64_tr_b16 v[130:131], v16 offset:11520\n"
"buffer_load_dwordx4 a[248:251], v1, s[44:47], s21 offen offset:0 sc1\n"
"v_mfma_f32_16x16x32_bf16 v[176:179], a[232:235], a[72:75], v[176:179]\n"
"ds_read_b64_tr_b16 v[132:133], v16 offset:12800\n"
"v_cndmask_b32 v36, v38, v36, s[4:5]\n"
"v_mfma_f32_16x16x32_bf16 v[176:179], v[72:75], a[76:79], v[176:179]\n"
"s_waitcnt lgkmcnt(6)\n"
"ds_read_b64_tr_b16 v[134:135], v16 offset:14080\n"
"v_bfe_u32 v38, v37, 3, 2\n"
"v_mfma_f32_16x16x32_bf16 v[180:183], v[76:79], a[64:67], v[180:183]\n"
"ds_read_b128 v[144:147], v1 offset:61440\n"
"ds_read_b64_tr_b16 a[216:217], v17 offset:10240\n"
"ds_read_b128 v[148:151], v1 offset:62464\n"
"v_mfma_f32_16x16x32_bf16 v[180:183], a[236:239], a[68:71], v[180:183]\n"
"ds_read_b64_tr_b16 a[218:219], v17 offset:11520\n"
"v_cndmask_b32 v32, v38, v32, s[4:5]\n"
"v_mfma_f32_16x16x32_bf16 v[184:187], v[76:79], a[72:75], v[184:187]\n"
"ds_read_b64_tr_b16 v[136:137], v17 offset:12800\n"
"buffer_load_dwordx4 a[252:255], v1, s[44:47], s21 offen offset:1024 sc1\n"
"v_mfma_f32_16x16x32_bf16 v[184:187], a[236:239], a[76:79], v[184:187]\n"
"s_waitcnt lgkmcnt(8)\n"
"ds_read_b64_tr_b16 v[138:139], v17 offset:14080\n"
"s_add_u32 s21, s67, 18432\n"
"v_mfma_f32_16x16x32_bf16 v[188:191], a[228:231], a[64:67], v[188:191]\n"
"ds_read_b64_tr_b16 a[224:225], v18 offset:10240\n"
"v_mul_u32_u24 v36, 1280, v36\n"
"v_mfma_f32_16x16x32_bf16 v[188:191], a[244:247], a[68:71], v[188:191]\n"
"ds_read_b64_tr_b16 a[226:227], v18 offset:11520\n"
"v_lshl_add_u32 v36, v35, 5, v36\n"
"v_mfma_f32_16x16x32_bf16 v[192:195], a[228:231], a[72:75], v[192:195]\n"
"ds_read_b64_tr_b16 v[140:141], v18 offset:12800\n"
"v_lshl_add_u32 v111, v32, 3, v36\n"
"v_mfma_f32_16x16x32_bf16 v[192:195], a[244:247], a[76:79], v[192:195]\n"
"s_waitcnt lgkmcnt(6)\n"
"ds_read_b64_tr_b16 v[142:143], v18 offset:14080\n"
"buffer_load_dwordx4 a[144:147], v1, s[44:47], s21 offen offset:0 sc1\n"
"v_mfma_f32_16x16x32_bf16 v[244:247], a[228:231], v[144:147], v[244:247]\n"
"ds_read_b64_tr_b16 a[44:45], v19 offset:10240\n"
"v_mov_b32 v38, 1280\n"
"v_mfma_f32_16x16x32_bf16 v[244:247], a[244:247], v[148:151], v[244:247]\n"
"ds_read_b64_tr_b16 a[46:47], v19 offset:11520\n"
"v_mov_b32 v39, 20480\n"
"v_mfma_f32_16x16x32_bf16 v[196:199], v[128:131], a[64:67], v[196:199]\n"
"ds_read_b64_tr_b16 a[132:133], v19 offset:12800\n"
"v_cndmask_b32 v38, v38, v39, s[4:5]\n"
"v_mfma_f32_16x16x32_bf16 v[196:199], v[132:135], a[68:71], v[196:199]\n"
"ds_read_b64_tr_b16 a[134:135], v19 offset:14080\n"
"v_add_u32 v113, v38, v111\n"
"v_mfma_f32_16x16x32_bf16 v[200:203], v[128:131], a[72:75], v[200:203]\n"
"ds_read_b64_tr_b16 v[160:161], v20 offset:10240\n"
"buffer_load_dwordx4 a[148:151], v1, s[44:47], s21 offen offset:1024 sc1\n"
"v_mfma_f32_16x16x32_bf16 v[200:203], v[132:135], a[76:79], v[200:203]\n"
"s_waitcnt lgkmcnt(6)\n"
"ds_read_b64_tr_b16 v[162:163], v20 offset:11520\n"
"s_and_b32 s15, s57, 0xf8\n"
"v_mfma_f32_16x16x32_bf16 v[204:207], a[216:219], a[64:67], v[204:207]\n"
"ds_read_b64_tr_b16 a[212:213], v20 offset:12800\n"
"v_lshrrev_b32 v30, 3, v0\n"
"v_mfma_f32_16x16x32_bf16 v[204:207], v[136:139], a[68:71], v[204:207]\n"
"s_and_b32 s14, s70, 0xffffc7ff\n"
"ds_read_b64_tr_b16 a[214:215], v20 offset:14080\n"
"v_mfma_f32_16x16x32_bf16 v[208:211], a[216:219], a[72:75], v[208:211]\n"
"ds_read_b64_tr_b16 v[96:97], v21 offset:10240\n"
"v_lshlrev_b32 v30, 11, v30\n"
"v_mfma_f32_16x16x32_bf16 v[208:211], v[136:139], a[76:79], v[208:211]\n"
"ds_read_b64_tr_b16 v[98:99], v21 offset:11520\n"
"buffer_load_dwordx4 a[152:155], v1, s[44:47], s21 offen offset:2048 sc1\n"
"v_mfma_f32_16x16x32_bf16 v[212:215], a[224:227], a[64:67], v[212:215]\n"
"s_waitcnt lgkmcnt(6)\n"
"ds_read_b64_tr_b16 v[100:101], v21 offset:12800\n"
"s_add_u32 s62, s10, 81920\n"
"v_mfma_f32_16x16x32_bf16 v[212:215], v[140:143], a[68:71], v[212:215]\n"
"s_add_u32 m0, s53, 30720\n"
"ds_read_b64_tr_b16 v[102:103], v21 offset:14080\n"
"v_add_u32 v114, s14, v30\n"
"v_mfma_f32_16x16x32_bf16 v[216:219], a[224:227], a[72:75], v[216:219]\n"
"s_barrier\n"
"s_waitcnt vmcnt(9)\n"
"s_barrier\n"
"v_mfma_f32_16x16x32_bf16 v[216:219], v[140:143], a[76:79], v[216:219]\n"
"s_mov_b64 exec, 0xff\n"
"buffer_store_dwordx4 v[24:27], v1, s[44:47], s70 offen offset:1024 sc1\n"
"s_mov_b64 exec, -1\n"
"v_mfma_f32_16x16x32_bf16 v[248:251], a[224:227], v[144:147], v[248:251]\n"
"ds_write_b64 v8, a[0:1] offset:15360\n"
"v_mfma_f32_16x16x32_bf16 v[248:251], v[140:143], v[148:151], v[248:251]\n"
"ds_read_b64_tr_b16 v[104:105], v12 offset:5120\n"
"v_lshrrev_b32 v30, 3, v0\n"
"v_mfma_f32_16x16x32_bf16 v[220:223], a[44:47], a[64:67], v[220:223]\n"
"ds_read_b64_tr_b16 v[106:107], v12 offset:6400\n"
"v_add_u32 v30, s15, v30\n"
"v_mfma_f32_16x16x32_bf16 v[220:223], a[132:135], a[68:71], v[220:223]\n"
"ds_read_b64_tr_b16 a[112:113], v12 offset:7680\n"
"v_lshlrev_b32 v30, 10, v30\n"
"v_mfma_f32_16x16x32_bf16 v[224:227], a[44:47], a[72:75], v[224:227]\n"
"s_lshl_b32 s14, s54, 7\n"
"buffer_load_dwordx4 v2, s[40:43], s62 offen lds\n"
"v_mfma_f32_16x16x32_bf16 v[224:227], a[132:135], a[76:79], v[224:227]\n"
"s_waitcnt lgkmcnt(4)\n"
"ds_write_b64 v8, a[2:3] offset:15368\n"
"v_mfma_f32_16x16x32_bf16 v[228:231], v[160:163], a[64:67], v[228:231]\n"
"s_add_u32 s14, s14, 15727104\n"
"ds_read_b64_tr_b16 a[114:115], v12 offset:8960\n"
"v_mfma_f32_16x16x32_bf16 v[228:231], a[212:215], a[68:71], v[228:231]\n"
"ds_read_b64_tr_b16 a[116:117], v13 offset:5120\n"
"v_add_u32 v114, s14, v30\n"
"v_mfma_f32_16x16x32_bf16 v[232:235], v[160:163], a[72:75], v[232:235]\n"
"s_lshl_b32 s14, s57, 2\n"
"ds_read_b64_tr_b16 a[118:119], v13 offset:6400\n"
"v_mfma_f32_16x16x32_bf16 v[232:235], a[212:215], a[76:79], v[232:235]\n"
"s_add_u32 s14, s14, s52\n"
"ds_read_b64_tr_b16 a[120:121], v13 offset:7680\n"
"v_mfma_f32_16x16x32_bf16 v[252:255], v[160:163], v[144:147], v[252:255]\n"
"ds_write_b64 v8, a[4:5] offset:16384\n"
"buffer_load_dwordx4 v3, s[40:43], s62 offen offset:1024 lds\n"
"v_mfma_f32_16x16x32_bf16 v[252:255], a[212:215], v[148:151], v[252:255]\n"
"s_lshl_b32 s14, s14, 8\n"
"ds_read_b64_tr_b16 a[122:123], v13 offset:8960\n"
"v_mfma_f32_16x16x32_bf16 v[236:239], v[96:99], a[64:67], v[236:239]\n"
"s_add_u32 s12, s14, 15727104\n"
"ds_read_b64_tr_b16 a[124:125], v14 offset:5120\n"
"v_mfma_f32_16x16x32_bf16 v[236:239], v[100:103], a[68:71], v[236:239]\n"
"s_mul_i32 s15, s15, 46080\n"
"ds_read_b64_tr_b16 a[126:127], v14 offset:6400\n"
"v_mfma_f32_16x16x32_bf16 v[240:243], v[96:99], a[72:75], v[240:243]\n"
"ds_read_b64_tr_b16 a[140:141], v14 offset:7680\n"
"ds_write_b64 v8, a[6:7] offset:16392\n"
"v_mfma_f32_16x16x32_bf16 v[240:243], v[100:103], a[76:79], v[240:243]\n"
"s_waitcnt lgkmcnt(7)\n"
"ds_read_b64_tr_b16 a[142:143], v14 offset:8960\n"
"s_nop 0\n"
".Lpad15_%=:\n"
"s_cbranch_execz .Lwb_%=\n"
"buffer_load_dwordx4 v4, s[40:43], s62 offen offset:2048 lds\n"
"v_mfma_f32_16x16x32_bf16 v[164:167], v[104:107], a[80:83], v[164:167]\n"
"s_add_u32 s7, s15, 16777216\n"
"ds_read_b64_tr_b16 v[80:81], v15 offset:5120\n"
"v_mfma_f32_16x16x32_bf16 v[164:167], a[112:115], a[84:87], v[164:167]\n"
"ds_read_b64_tr_b16 v[82:83], v15 offset:6400\n"
"v_bfe_u32 v30, v0, 2, 2\n"
"v_mfma_f32_16x16x32_bf16 v[168:171], v[104:107], a[88:91], v[168:171]\n"
"ds_read_b64_tr_b16 v[84:85], v15 offset:7680\n"
"ds_write_b64 v8, a[8:9] offset:17408\n"
"v_mfma_f32_16x16x32_bf16 v[168:171], a[112:115], a[92:95], v[168:171]\n"
"ds_read_b64_tr_b16 v[86:87], v15 offset:8960\n"
"v_lshrrev_b32 v31, 4, v0\n"
"v_mfma_f32_16x16x32_bf16 v[172:175], a[116:119], a[80:83], v[172:175]\n"
"s_waitcnt lgkmcnt(8)\n"
"ds_read_b64_tr_b16 a[220:221], v16 offset:5120\n"
"buffer_load_dwordx4 v5, s[40:43], s62 offen offset:3072 lds\n"
"v_mfma_f32_16x16x32_bf16 v[172:175], a[120:123], a[84:87], v[172:175]\n"
"s_add_u32 m0, s53, 34816\n"
"ds_read_b64_tr_b16 a[222:223], v16 offset:6400\n"
"v_cmp_eq_u32 s[4:5], v30, v31\n"
"v_mfma_f32_16x16x32_bf16 v[176:179], a[116:119], a[88:91], v[176:179]\n"
"ds_read_b64_tr_b16 v[88:89], v16 offset:7680\n"
"ds_write_b64 v8, a[10:11] offset:17416\n"
"v_mfma_f32_16x16x32_bf16 v[176:179], a[120:123], a[92:95], v[176:179]\n"
"ds_read_b64_tr_b16 v[90:91], v16 offset:8960\n"
"v_and_b32 v30, 1, v0\n"
"v_mfma_f32_16x16x32_bf16 v[180:183], a[124:127], a[80:83], v[180:183]\n"
"s_waitcnt lgkmcnt(8)\n"
"ds_read_b128 v[152:155], v25 offset:20480\n"
"ds_read_b64_tr_b16 v[92:93], v17 offset:5120\n"
"ds_read_b128 v[156:159], v25 offset:21504\n"
"v_mfma_f32_16x16x32_bf16 v[180:183], a[140:143], a[84:87], v[180:183]\n"
"ds_read_b64_tr_b16 v[94:95], v17 offset:6400\n"
"buffer_load_dwordx4 v6, s[40:43], s62 offen lds\n"
"v_mfma_f32_16x16x32_bf16 v[184:187], a[124:127], a[88:91], v[184:187]\n"
"ds_read_b64_tr_b16 a[196:197], v17 offset:7680\n"
"ds_write_b64 v8, a[12:13] offset:18432\n"
"v_mfma_f32_16x16x32_bf16 v[184:187], a[140:143], a[92:95], v[184:187]\n"
"ds_read_b64_tr_b16 a[198:199], v17 offset:8960\n"
"v_lshlrev_b32 v30, 4, v30\n"
"v_mfma_f32_16x16x32_bf16 v[188:191], v[80:83], a[80:83], v[188:191]\n"
"s_waitcnt lgkmcnt(10)\n"
"ds_read_b64_tr_b16 a[128:129], v18 offset:5120\n"
"buffer_load_dwordx4 a[156:159], v1, s[44:47], s21 offen offset:3072 sc1\n"
"v_mfma_f32_16x16x32_bf16 v[188:191], v[84:87], a[84:87], v[188:191]\n"
"s_add_u32 s21, s68, 18432\n"
"ds_read_b64_tr_b16 a[130:131], v18 offset:6400\n"
"v_mov_b32 v31, 0x3f80\n"
"v_mfma_f32_16x16x32_bf16 v[192:195], v[80:83], a[88:91], v[192:195]\n"
"ds_read_b64_tr_b16 a[40:41], v18 offset:7680\n"
"ds_write_b64 v8, a[14:15] offset:18440\n"
"v_mfma_f32_16x16x32_bf16 v[192:195], v[84:87], a[92:95], v[192:195]\n"
"s_waitcnt lgkmcnt(8)\n"
"ds_read_b64_tr_b16 a[42:43], v18 offset:8960\n"
"v_lshlrev_b32 v31, v30, v31\n"
"v_mfma_f32_16x16x32_bf16 v[244:247], v[80:83], v[152:155], v[244:247]\n"
"s_waitcnt vmcnt(10)\n"
"ds_write_b64 v11, a[248:249] offset:22528\n"
"ds_read_b64_tr_b16 a[192:193], v19 offset:5120\n"
"v_mfma_f32_16x16x32_bf16 v[244:247], v[84:87], v[156:159], v[244:247]\n"
"ds_read_b64_tr_b16 a[194:195], v19 offset:6400\n"
"buffer_load_dwordx4 a[160:163], v1, s[44:47], s21 offen offset:0 sc1\n"
"v_mfma_f32_16x16x32_bf16 v[196:199], a[220:223], a[80:83], v[196:199]\n"
"ds_read_b64_tr_b16 a[136:137], v19 offset:7680\n"
"ds_write_b64 v8, a[16:17] offset:19456\n"
"ds_write_b64 v11, a[250:251] offset:22544\n"
"v_mfma_f32_16x16x32_bf16 v[196:199], v[88:91], a[84:87], v[196:199]\n"
"ds_read_b64_tr_b16 a[138:139], v19 offset:8960\n"
"v_cndmask_b32 v31, 0, v31, s[4:5]\n"
"v_mfma_f32_16x16x32_bf16 v[200:203], a[220:223], a[88:91], v[200:203]\n"
"ds_read_b64_tr_b16 a[232:233], v20 offset:5120\n"
"ds_write_b64 v11, a[252:253] offset:23552\n"
"v_mfma_f32_16x16x32_bf16 v[200:203], v[88:91], a[92:95], v[200:203]\n"
"s_waitcnt lgkmcnt(10)\n"
"ds_read_b64_tr_b16 a[234:235], v20 offset:6400\n"
"v_and_b32 v30, 2, v0\n"
"v_mfma_f32_16x16x32_bf16 v[204:207], v[92:95], a[80:83], v[204:207]\n"
"ds_read_b64_tr_b16 v[72:73], v20 offset:7680\n"
"ds_write_b64 v8, a[18:19] offset:19464\n"
"buffer_load_dwordx2 v[124:125], v7, s[44:47], 0 offen offset:1032 sc1\n"
"ds_write_b64 v11, a[254:255] offset:23568\n"
"v_mfma_f32_16x16x32_bf16 v[204:207], a[196:199], a[84:87], v[204:207]\n"
"ds_read_b64_tr_b16 v[74:75], v20 offset:8960\n"
"v_cmp_eq_u32 s[4:5], 0, v30\n"
"v_mfma_f32_16x16x32_bf16 v[208:211], v[92:95], a[88:91], v[208:211]\n"
"ds_read_b64_tr_b16 v[76:77], v21 offset:5120\n"
"v_mov_b32 v42, 0\n"
"v_mfma_f32_16x16x32_bf16 v[208:211], a[196:199], a[92:95], v[208:211]\n"
"ds_read_b64_tr_b16 v[78:79], v21 offset:6400\n"
"v_mov_b32 v43, 0\n"
"v_mfma_f32_16x16x32_bf16 v[212:215], a[128:131], a[80:83], v[212:215]\n"
"s_waitcnt lgkmcnt(9)\n"
"ds_read_b64_tr_b16 a[236:237], v21 offset:7680\n"
"buffer_load_dwordx4 a[164:167], v1, s[44:47], s21 offen offset:1024 sc1\n"
"v_mfma_f32_16x16x32_bf16 v[212:215], a[40:43], a[84:87], v[212:215]\n"
"ds_read_b64_tr_b16 a[238:239], v21 offset:8960\n"
"v_cndmask_b32 v40, 0, v31, s[4:5]\n"
"v_mfma_f32_16x16x32_bf16 v[216:219], a[128:131], a[88:91], v[216:219]\n"
"s_waitcnt lgkmcnt(5)\n"
"s_barrier\n"
"ds_read_b64_tr_b16 a[228:229], v12 offset:0\n"
"v_mfma_f32_16x16x32_bf16 v[216:219], a[40:43], a[92:95], v[216:219]\n"
"ds_write_b64 v8, a[20:21] offset:10240\n"
"v_mfma_f32_16x16x32_bf16 v[248:251], a[128:131], v[152:155], v[248:251]\n"
"buffer_load_dwordx4 a[168:171], v1, s[44:47], s21 offen offset:2048 sc1\n"
"v_mfma_f32_16x16x32_bf16 v[248:251], a[40:43], v[156:159], v[248:251]\n"
"s_add_u32 s62, s11, 81920\n"
"ds_read_b64_tr_b16 a[230:231], v12 offset:1280\n"
"s_add_u32 m0, s53, 35840\n"
"v_mfma_f32_16x16x32_bf16 v[220:223], a[192:195], a[80:83], v[220:223]\n"
"ds_read_b64_tr_b16 a[244:245], v12 offset:2560\n"
"v_cndmask_b32 v41, v31, 0, s[4:5]\n"
"v_mfma_f32_16x16x32_bf16 v[220:223], a[136:139], a[84:87], v[220:223]\n"
"ds_write_b64 v8, a[22:23] offset:10248\n"
"v_mfma_f32_16x16x32_bf16 v[224:227], a[192:195], a[88:91], v[224:227]\n"
"buffer_load_dwordx4 a[172:175], v1, s[44:47], s21 offen offset:3072 sc1\n"
"v_mfma_f32_16x16x32_bf16 v[224:227], a[136:139], a[92:95], v[224:227]\n"
"ds_read_b64_tr_b16 a[246:247], v12 offset:3840\n"
"v_mov_b32 v44, v40\n"
"v_mfma_f32_16x16x32_bf16 v[228:231], a[232:235], a[80:83], v[228:231]\n"
"s_waitcnt lgkmcnt(4)\n"
"ds_read_b64_tr_b16 v[128:129], v13 offset:0\n"
"v_mov_b32 v45, v41\n"
"v_mfma_f32_16x16x32_bf16 v[228:231], v[72:75], a[84:87], v[228:231]\n"
"ds_write_b64 v8, a[24:25] offset:11264\n"
"v_mfma_f32_16x16x32_bf16 v[232:235], a[232:235], a[88:91], v[232:235]\n"
"ds_read_b64_tr_b16 v[130:131], v13 offset:1280\n"
"v_mfma_f32_16x16x32_bf16 v[232:235], v[72:75], a[92:95], v[232:235]\n"
"buffer_load_dwordx4 v2, s[40:43], s62 offen lds\n"
"v_mfma_f32_16x16x32_bf16 v[252:255], a[232:235], v[152:155], v[252:255]\n"
"ds_read_b64_tr_b16 v[132:133], v13 offset:2560\n"
"v_mfma_f32_16x16x32_bf16 v[252:255], v[72:75], v[156:159], v[252:255]\n"
"ds_read_b64_tr_b16 v[134:135], v13 offset:3840\n"
"v_mfma_f32_16x16x32_bf16 v[236:239], v[76:79], a[80:83], v[236:239]\n"
"ds_read_b64_tr_b16 a[216:217], v14 offset:0\n"
"v_mfma_f32_16x16x32_bf16 v[236:239], a[236:239], a[84:87], v[236:239]\n"
"ds_write_b64 v8, a[26:27] offset:11272\n"
"v_mfma_f32_16x16x32_bf16 v[240:243], v[76:79], a[88:91], v[240:243]\n"
"ds_read_b64_tr_b16 a[218:219], v14 offset:1280\n"
"v_mfma_f32_16x16x32_bf16 v[240:243], a[236:239], a[92:95], v[240:243]\n"
"s_waitcnt lgkmcnt(5)\n"
"buffer_load_dwordx4 v3, s[40:43], s62 offen offset:1024 lds\n"
"s_waitcnt vmcnt(7)\n"
"v_mfma_f32_16x16x32_bf16 v[164:167], a[228:231], a[144:147], v[164:167]\n"
"s_mul_i32 s18, s52, 3\n"
"ds_read_b64_tr_b16 v[136:137], v14 offset:2560\n"
"v_mfma_f32_16x16x32_bf16 v[164:167], a[244:247], a[148:151], v[164:167]\n"
"s_mul_i32 s19, s52, 10240\n"
"ds_read_b64_tr_b16 v[138:139], v14 offset:3840\n"
"v_mfma_f32_16x16x32_bf16 v[168:171], a[228:231], a[152:155], v[168:171]\n"
"s_waitcnt lgkmcnt(3)\n"
"s_barrier\n"
"ds_read_b64_tr_b16 a[224:225], v15 offset:0\n"
"ds_write_b64 v8, a[28:29] offset:12288\n"
"v_mfma_f32_16x16x32_bf16 v[168:171], a[244:247], a[156:159], v[168:171]\n"
"s_add_u32 s19, s19, s71\n"
"ds_read_b64_tr_b16 a[226:227], v15 offset:1280\n"
"v_mfma_f32_16x16x32_bf16 v[172:175], v[128:131], a[144:147], v[172:175]\n"
"ds_read_b64_tr_b16 v[140:141], v15 offset:2560\n"
"buffer_load_dwordx4 v4, s[40:43], s62 offen offset:2048 lds\n"
"v_mfma_f32_16x16x32_bf16 v[172:175], v[132:135], a[148:151], v[172:175]\n"
"s_add_u32 s16, s18, 3\n"
"ds_read_b64_tr_b16 v[142:143], v15 offset:3840\n"
"v_mfma_f32_16x16x32_bf16 v[176:179], v[128:131], a[152:155], v[176:179]\n"
"ds_write_b64 v8, a[30:31] offset:12296\n"
"v_mfma_f32_16x16x32_bf16 v[176:179], v[132:135], a[156:159], v[176:179]\n"
"s_waitcnt lgkmcnt(7)\n"
"ds_read_b64_tr_b16 a[44:45], v16 offset:0\n"
"v_mfma_f32_16x16x32_bf16 v[180:183], a[216:219], a[144:147], v[180:183]\n"
"s_waitcnt lgkmcnt(7)\n"
"ds_read_b128 v[144:147], v1 offset:22528\n"
"ds_read_b128 v[148:151], v1 offset:23552\n"
"v_mfma_f32_16x16x32_bf16 v[180:183], v[136:139], a[148:151], v[180:183]\n"
"ds_read_b64_tr_b16 a[46:47], v16 offset:1280\n"
"buffer_load_dwordx4 v5, s[40:43], s62 offen offset:3072 lds\n"
"v_mfma_f32_16x16x32_bf16 v[184:187], a[216:219], a[152:155], v[184:187]\n"
"ds_read_b64_tr_b16 a[132:133], v16 offset:2560\n"
"ds_write_b64 v8, a[32:33] offset:13312\n"
"v_mfma_f32_16x16x32_bf16 v[184:187], v[136:139], a[156:159], v[184:187]\n"
"s_waitcnt lgkmcnt(8)\n"
"ds_read_b64_tr_b16 a[134:135], v16 offset:3840\n"
"s_add_u32 m0, s53, 39936\n"
"v_mfma_f32_16x16x32_bf16 v[188:191], a[224:227], a[144:147], v[188:191]\n"
"s_waitcnt lgkmcnt(8)\n"
"ds_read_b64_tr_b16 v[160:161], v17 offset:0\n"
"s_add_u32 s21, s69, 18432\n"
"v_mfma_f32_16x16x32_bf16 v[188:191], v[140:143], a[148:151], v[188:191]\n"
"s_sub_u32 s17, s16, 10\n"
"s_cselect_b32 s16, s16, s17\n"
"ds_read_b64_tr_b16 v[162:163], v17 offset:1280\n"
"v_mfma_f32_16x16x32_bf16 v[192:195], a[224:227], a[152:155], v[192:195]\n"
"s_waitcnt vmcnt(7)\n"
"v_cmp_ne_u64 vcc, v[124:125], s[58:59]\n"
"ds_read_b64_tr_b16 a[212:213], v17 offset:2560\n"
"ds_write_b64 v8, a[34:35] offset:13320\n"
"buffer_load_dwordx4 v6, s[40:43], s62 offen lds\n"
"v_mfma_f32_16x16x32_bf16 v[192:195], v[140:143], a[156:159], v[192:195]\n"
"s_waitcnt lgkmcnt(8)\n"
"ds_read_b64_tr_b16 a[214:215], v17 offset:3840\n"
"s_lshl_b32 s17, s16, 10\n"
"v_mfma_f32_16x16x32_bf16 v[244:247], a[224:227], v[144:147], v[244:247]\n"
"s_add_u32 s24, s17, s19\n"
"ds_read_b64_tr_b16 v[96:97], v18 offset:0\n"
"v_mfma_f32_16x16x32_bf16 v[244:247], v[140:143], v[148:151], v[244:247]\n"
"ds_read_b64_tr_b16 v[98:99], v18 offset:1280\n"
"s_cbranch_vccnz .Lsp1_%=\n"
".Lpr2_%=:\n"
"s_waitcnt lgkmcnt(8)\n"
"v_mfma_f32_16x16x32_bf16 v[196:199], a[44:47], a[144:147], v[196:199]\n"
"s_waitcnt lgkmcnt(7)\n"
"ds_read_b64_tr_b16 v[100:101], v18 offset:2560\n"
"ds_write_b64 v8, a[36:37] offset:14336\n"
"v_mfma_f32_16x16x32_bf16 v[196:199], a[132:135], a[148:151], v[196:199]\n"
"ds_read_b64_tr_b16 v[102:103], v18 offset:3840\n"
"buffer_load_dwordx4 a[96:99], v1, s[44:47], s21 offen offset:0 sc1\n"
"v_mfma_f32_16x16x32_bf16 v[200:203], a[44:47], a[152:155], v[200:203]\n"
"s_add_u32 s16, s18, 4\n"
"ds_read_b64_tr_b16 v[104:105], v19 offset:0\n"
"v_mfma_f32_16x16x32_bf16 v[200:203], a[132:135], a[156:159], v[200:203]\n"
"s_waitcnt lgkmcnt(7)\n"
"ds_write_b64 v8, a[38:39] offset:14344\n"
"v_mfma_f32_16x16x32_bf16 v[204:207], v[160:163], a[144:147], v[204:207]\n"
"s_waitcnt lgkmcnt(7)\n"
"ds_read_b64_tr_b16 v[106:107], v19 offset:1280\n"
"v_mfma_f32_16x16x32_bf16 v[204:207], a[212:215], a[148:151], v[204:207]\n"
"buffer_load_dwordx4 a[100:103], v1, s[44:47], s21 offen offset:1024 sc1\n"
"v_mfma_f32_16x16x32_bf16 v[208:211], v[160:163], a[152:155], v[208:211]\n"
"s_sub_u32 s17, s16, 10\n"
"s_cselect_b32 s16, s16, s17\n"
"ds_read_b64_tr_b16 a[112:113], v19 offset:2560\n"
"v_mfma_f32_16x16x32_bf16 v[208:211], a[212:215], a[156:159], v[208:211]\n"
"s_waitcnt lgkmcnt(5)\n"
"ds_read_b64_tr_b16 a[114:115], v19 offset:3840\n"
"s_lshl_b32 s17, s16, 10\n"
"v_mfma_f32_16x16x32_bf16 v[212:215], v[96:99], a[144:147], v[212:215]\n"
"s_waitcnt lgkmcnt(5)\n"
"ds_read_b64_tr_b16 a[116:117], v20 offset:0\n"
"s_add_u32 s25, s17, s19\n"
"v_mfma_f32_16x16x32_bf16 v[212:215], v[100:103], a[148:151], v[212:215]\n"
"s_add_u32 s16, s18, 5\n"
"ds_read_b64_tr_b16 a[118:119], v20 offset:1280\n"
"v_mfma_f32_16x16x32_bf16 v[216:219], v[96:99], a[152:155], v[216:219]\n"
"ds_read_b64_tr_b16 a[120:121], v20 offset:2560\n"
"buffer_load_dwordx4 a[104:107], v1, s[44:47], s21 offen offset:2048 sc1\n"
"v_mfma_f32_16x16x32_bf16 v[216:219], v[100:103], a[156:159], v[216:219]\n"
"s_sub_u32 s17, s16, 10\n"
"s_cselect_b32 s16, s16, s17\n"
"ds_read_b64_tr_b16 a[122:123], v20 offset:3840\n"
"v_mfma_f32_16x16x32_bf16 v[248:251], v[96:99], v[144:147], v[248:251]\n"
"s_lshl_b32 s17, s16, 10\n"
"ds_read_b64_tr_b16 a[124:125], v21 offset:0\n"
"v_mfma_f32_16x16x32_bf16 v[248:251], v[100:103], v[148:151], v[248:251]\n"
"s_waitcnt lgkmcnt(6)\n"
"ds_read_b64_tr_b16 a[126:127], v21 offset:1280\n"
"s_add_u32 s26, s17, s19\n"
"v_mfma_f32_16x16x32_bf16 v[220:223], v[104:107], a[144:147], v[220:223]\n"
"s_waitcnt lgkmcnt(6)\n"
"ds_read_b64_tr_b16 a[140:141], v21 offset:2560\n"
"buffer_load_dwordx4 a[108:111], v1, s[44:47], s21 offen offset:3072 sc1\n"
"v_mfma_f32_16x16x32_bf16 v[220:223], a[112:115], a[148:151], v[220:223]\n"
"s_add_u32 s21, s67, 36864\n"
"ds_read_b64_tr_b16 a[142:143], v21 offset:3840\n"
"s_add_u32 s16, s18, 0\n"
"v_mfma_f32_16x16x32_bf16 v[224:227], v[104:107], a[152:155], v[224:227]\n"
"s_sub_u32 s17, s16, 10\n"
"s_cselect_b32 s16, s16, s17\n"
"ds_read_b64_tr_b16 v[80:81], v12 offset:30720\n"
"v_mfma_f32_16x16x32_bf16 v[224:227], a[112:115], a[156:159], v[224:227]\n"
"s_waitcnt lgkmcnt(6)\n"
"ds_read_b64_tr_b16 v[82:83], v12 offset:32000\n"
"s_lshl_b32 s17, s16, 10\n"
"v_mfma_f32_16x16x32_bf16 v[228:231], a[116:119], a[144:147], v[228:231]\n"
"s_waitcnt lgkmcnt(6)\n"
"ds_read_b64_tr_b16 v[84:85], v12 offset:33280\n"
"s_add_u32 s27, s17, s19\n"
"v_mfma_f32_16x16x32_bf16 v[228:231], a[120:123], a[148:151], v[228:231]\n"
"ds_read_b64_tr_b16 v[86:87], v12 offset:34560\n"
"buffer_load_dwordx4 a[48:51], v1, s[44:47], s21 offen offset:0 sc1\n"
"v_mfma_f32_16x16x32_bf16 v[232:235], a[116:119], a[152:155], v[232:235]\n"
"s_lshl_b32 s16, s16, 9\n"
"ds_read_b64_tr_b16 a[220:221], v13 offset:30720\n"
"v_mfma_f32_16x16x32_bf16 v[232:235], a[120:123], a[156:159], v[232:235]\n"
"s_add_u32 s16, s16, s71\n"
"ds_read_b64_tr_b16 a[222:223], v13 offset:32000\n"
"v_mfma_f32_16x16x32_bf16 v[252:255], a[116:119], v[144:147], v[252:255]\n"
"s_add_u32 s35, s16, 40960\n"
"ds_read_b64_tr_b16 v[88:89], v13 offset:33280\n"
"v_mfma_f32_16x16x32_bf16 v[252:255], a[120:123], v[148:151], v[252:255]\n"
"s_waitcnt lgkmcnt(6)\n"
"ds_read_b64_tr_b16 v[90:91], v13 offset:34560\n"
"s_add_u32 s16, s18, 6\n"
"v_mfma_f32_16x16x32_bf16 v[236:239], a[124:127], a[144:147], v[236:239]\n"
"ds_read_b64_tr_b16 v[92:93], v14 offset:30720\n"
"buffer_load_dwordx4 a[52:55], v1, s[44:47], s21 offen offset:1024 sc1\n"
"v_mfma_f32_16x16x32_bf16 v[236:239], a[140:143], a[148:151], v[236:239]\n"
"s_sub_u32 s17, s16, 10\n"
"s_cselect_b32 s16, s16, s17\n"
"ds_read_b64_tr_b16 v[94:95], v14 offset:32000\n"
"v_mfma_f32_16x16x32_bf16 v[240:243], a[124:127], a[152:155], v[240:243]\n"
"s_lshl_b32 s17, s16, 10\n"
"ds_read_b64_tr_b16 a[196:197], v14 offset:33280\n"
"v_mfma_f32_16x16x32_bf16 v[240:243], a[140:143], a[156:159], v[240:243]\n"
"s_waitcnt lgkmcnt(6)\n"
"ds_read_b64_tr_b16 a[198:199], v14 offset:34560\n"
"s_waitcnt vmcnt(11)\n"
"v_mfma_f32_16x16x32_bf16 v[164:167], v[80:83], a[160:163], v[164:167]\n"
"s_add_u32 s28, s17, s19\n"
"ds_read_b64_tr_b16 a[128:129], v15 offset:30720\n"
"v_mfma_f32_16x16x32_bf16 v[164:167], v[84:87], a[164:167], v[164:167]\n"
"ds_read_b64_tr_b16 a[130:131], v15 offset:32000\n"
"buffer_load_dwordx4 a[56:59], v1, s[44:47], s21 offen offset:2048 sc1\n"
"v_mfma_f32_16x16x32_bf16 v[168:171], v[80:83], a[168:171], v[168:171]\n"
"s_add_u32 s16, s18, 7\n"
"ds_read_b64_tr_b16 a[40:41], v15 offset:33280\n"
"v_mfma_f32_16x16x32_bf16 v[168:171], v[84:87], a[172:175], v[168:171]\n"
"s_waitcnt lgkmcnt(6)\n"
"ds_read_b64_tr_b16 a[42:43], v15 offset:34560\n"
"v_mfma_f32_16x16x32_bf16 v[172:175], a[220:223], a[160:163], v[172:175]\n"
"s_sub_u32 s17, s16, 10\n"
"s_cselect_b32 s16, s16, s17\n"
"ds_read_b64_tr_b16 a[192:193], v16 offset:30720\n"
"v_mfma_f32_16x16x32_bf16 v[172:175], v[88:91], a[164:167], v[172:175]\n"
"s_lshl_b32 s17, s16, 10\n"
"ds_read_b64_tr_b16 a[194:195], v16 offset:32000\n"
"v_mfma_f32_16x16x32_bf16 v[176:179], a[220:223], a[168:171], v[176:179]\n"
"ds_read_b64_tr_b16 a[136:137], v16 offset:33280\n"
"buffer_load_dwordx4 a[60:63], v1, s[44:47], s21 offen offset:3072 sc1\n"
"v_mfma_f32_16x16x32_bf16 v[176:179], v[88:91], a[172:175], v[176:179]\n"
"s_waitcnt lgkmcnt(6)\n"
"ds_read_b64_tr_b16 a[138:139], v16 offset:34560\n"
"s_add_u32 s21, s68, 36864\n"
"v_mfma_f32_16x16x32_bf16 v[180:183], v[92:95], a[160:163], v[180:183]\n"
"ds_read_b128 v[152:155], v1 offset:63488\n"
"ds_read_b64_tr_b16 a[232:233], v17 offset:30720\n"
"ds_read_b128 v[156:159], v1 offset:64512\n"
"v_mfma_f32_16x16x32_bf16 v[180:183], a[196:199], a[164:167], v[180:183]\n"
"s_add_u32 s29, s17, s19\n"
"ds_read_b64_tr_b16 a[234:235], v17 offset:32000\n"
"v_mfma_f32_16x16x32_bf16 v[184:187], v[92:95], a[168:171], v[184:187]\n"
"s_add_u32 s16, s18, 1\n"
"ds_read_b64_tr_b16 v[72:73], v17 offset:33280\n"
"v_mfma_f32_16x16x32_bf16 v[184:187], a[196:199], a[172:175], v[184:187]\n"
"s_waitcnt lgkmcnt(8)\n"
"ds_read_b64_tr_b16 v[74:75], v17 offset:34560\n"
"buffer_load_dwordx4 a[64:67], v1, s[44:47], s21 offen offset:0 sc1\n"
"v_mfma_f32_16x16x32_bf16 v[188:191], a[128:131], a[160:163], v[188:191]\n"
"s_sub_u32 s17, s16, 10\n"
"s_cselect_b32 s16, s16, s17\n"
"ds_read_b64_tr_b16 v[76:77], v18 offset:30720\n"
"v_mfma_f32_16x16x32_bf16 v[188:191], a[40:43], a[164:167], v[188:191]\n"
"s_lshl_b32 s17, s16, 10\n"
"ds_read_b64_tr_b16 v[78:79], v18 offset:32000\n"
"v_mfma_f32_16x16x32_bf16 v[192:195], a[128:131], a[168:171], v[192:195]\n"
"s_add_u32 s30, s17, s19\n"
"ds_read_b64_tr_b16 a[236:237], v18 offset:33280\n"
"v_mfma_f32_16x16x32_bf16 v[192:195], a[40:43], a[172:175], v[192:195]\n"
"s_waitcnt lgkmcnt(6)\n"
"ds_read_b64_tr_b16 a[238:239], v18 offset:34560\n"
"buffer_load_dwordx4 a[68:71], v1, s[44:47], s21 offen offset:1024 sc1\n"
"v_mfma_f32_16x16x32_bf16 v[244:247], a[128:131], v[152:155], v[244:247]\n"
"s_lshl_b32 s16, s16, 9\n"
"ds_read_b64_tr_b16 a[228:229], v19 offset:30720\n"
"v_mfma_f32_16x16x32_bf16 v[244:247], a[40:43], v[156:159], v[244:247]\n"
"s_add_u32 s16, s16, s71\n"
"ds_read_b64_tr_b16 a[230:231], v19 offset:32000\n"
"v_mfma_f32_16x16x32_bf16 v[196:199], a[192:195], a[160:163], v[196:199]\n"
"s_add_u32 s62, s9, 163840\n"
"ds_read_b64_tr_b16 a[244:245], v19 offset:33280\n"
"s_add_u32 m0, s53, 25600\n"
"v_mfma_f32_16x16x32_bf16 v[196:199], a[136:139], a[164:167], v[196:199]\n"
"s_add_u32 s74, s16, 40960\n"
"ds_read_b64_tr_b16 a[246:247], v19 offset:34560\n"
"v_mfma_f32_16x16x32_bf16 v[200:203], a[192:195], a[168:171], v[200:203]\n"
"ds_read_b64_tr_b16 v[128:129], v20 offset:30720\n"
"buffer_load_dwordx4 a[72:75], v1, s[44:47], s21 offen offset:2048 sc1\n"
"v_mfma_f32_16x16x32_bf16 v[200:203], a[136:139], a[172:175], v[200:203]\n"
"s_waitcnt lgkmcnt(6)\n"
"ds_read_b64_tr_b16 v[130:131], v20 offset:32000\n"
"s_add_u32 s16, s18, 8\n"
"v_mfma_f32_16x16x32_bf16 v[204:207], a[232:235], a[160:163], v[204:207]\n"
"s_sub_u32 s17, s16, 10\n"
"s_cselect_b32 s16, s16, s17\n"
"ds_read_b64_tr_b16 v[132:133], v20 offset:33280\n"
"v_mfma_f32_16x16x32_bf16 v[204:207], v[72:75], a[164:167], v[204:207]\n"
"s_lshl_b32 s17, s16, 10\n"
"ds_read_b64_tr_b16 v[134:135], v20 offset:34560\n"
"v_mfma_f32_16x16x32_bf16 v[208:211], a[232:235], a[168:171], v[208:211]\n"
"ds_read_b64_tr_b16 a[216:217], v21 offset:30720\n"
"buffer_load_dwordx4 a[76:79], v1, s[44:47], s21 offen offset:3072 sc1\n"
"v_mfma_f32_16x16x32_bf16 v[208:211], v[72:75], a[172:175], v[208:211]\n"
"s_add_u32 s31, s17, s19\n"
"ds_read_b64_tr_b16 a[218:219], v21 offset:32000\n"
"v_mfma_f32_16x16x32_bf16 v[212:215], v[76:79], a[160:163], v[212:215]\n"
"s_waitcnt lgkmcnt(6)\n"
"ds_read_b64_tr_b16 v[136:137], v21 offset:33280\n"
"s_add_u32 s16, s18, 2\n"
"v_mfma_f32_16x16x32_bf16 v[212:215], a[236:239], a[164:167], v[212:215]\n"
"ds_read_b64_tr_b16 v[138:139], v21 offset:34560\n"
"buffer_load_dwordx4 v2, s[40:43], s62 offen lds\n"
"v_mfma_f32_16x16x32_bf16 v[216:219], v[76:79], a[168:171], v[216:219]\n"
"s_waitcnt vmcnt(13)\n"
"s_barrier\n"
"ds_read_b64_tr_b16 a[224:225], v12 offset:35840\n"
"v_mfma_f32_16x16x32_bf16 v[216:219], a[236:239], a[172:175], v[216:219]\n"
"s_sub_u32 s17, s16, 10\n"
"s_cselect_b32 s16, s16, s17\n"
"ds_read_b64_tr_b16 a[226:227], v12 offset:37120\n"
"v_mfma_f32_16x16x32_bf16 v[248:251], v[76:79], v[152:155], v[248:251]\n"
"s_lshl_b32 s17, s16, 10\n"
"buffer_load_dwordx4 v3, s[40:43], s62 offen offset:1024 lds\n"
"v_mfma_f32_16x16x32_bf16 v[248:251], a[236:239], v[156:159], v[248:251]\n"
"s_add_u32 s33, s17, s19\n"
"ds_read_b64_tr_b16 v[140:141], v12 offset:38400\n"
"v_mfma_f32_16x16x32_bf16 v[220:223], a[228:231], a[160:163], v[220:223]\n"
"s_lshl_b32 s16, s16, 9\n"
"ds_read_b64_tr_b16 v[142:143], v12 offset:39680\n"
"v_mfma_f32_16x16x32_bf16 v[220:223], a[244:247], a[164:167], v[220:223]\n"
"s_add_u32 s16, s16, s71\n"
"buffer_load_dwordx4 v4, s[40:43], s62 offen offset:2048 lds\n"
"v_mfma_f32_16x16x32_bf16 v[224:227], a[228:231], a[168:171], v[224:227]\n"
"s_add_u32 s21, s69, 36864\n"
"ds_read_b64_tr_b16 a[44:45], v13 offset:35840\n"
"s_add_u32 s75, s16, 40960\n"
"v_mfma_f32_16x16x32_bf16 v[224:227], a[244:247], a[172:175], v[224:227]\n"
"s_waitcnt lgkmcnt(4)\n"
"ds_read_b64_tr_b16 a[46:47], v13 offset:37120\n"
"s_add_u32 s16, s18, 9\n"
"v_mfma_f32_16x16x32_bf16 v[228:231], v[128:131], a[160:163], v[228:231]\n"
"buffer_load_dwordx4 v5, s[40:43], s62 offen offset:3072 lds\n"
"v_mfma_f32_16x16x32_bf16 v[228:231], v[132:135], a[164:167], v[228:231]\n"
"s_add_u32 m0, s53, 29696\n"
"ds_read_b64_tr_b16 a[132:133], v13 offset:38400\n"
"v_mfma_f32_16x16x32_bf16 v[232:235], v[128:131], a[168:171], v[232:235]\n"
"s_sub_u32 s17, s16, 10\n"
"s_cselect_b32 s16, s16, s17\n"
"ds_read_b64_tr_b16 a[134:135], v13 offset:39680\n"
"v_mfma_f32_16x16x32_bf16 v[232:235], v[132:135], a[172:175], v[232:235]\n"
"s_lshl_b32 s17, s16, 10\n"
"buffer_load_dwordx4 v6, s[40:43], s62 offen lds\n"
"v_mfma_f32_16x16x32_bf16 v[252:255], v[128:131], v[152:155], v[252:255]\n"
"s_add_u32 s34, s17, s19\n"
"ds_read_b64_tr_b16 v[160:161], v14 offset:35840\n"
"v_mfma_f32_16x16x32_bf16 v[252:255], v[132:135], v[156:159], v[252:255]\n"
"ds_read_b64_tr_b16 v[162:163], v14 offset:37120\n"
"v_mfma_f32_16x16x32_bf16 v[236:239], a[216:219], a[160:163], v[236:239]\n"
"buffer_load_dwordx4 a[80:83], v1, s[44:47], s21 offen offset:0 sc1\n"
"v_mfma_f32_16x16x32_bf16 v[236:239], v[136:139], a[164:167], v[236:239]\n"
"ds_read_b64_tr_b16 a[212:213], v14 offset:38400\n"
"v_mfma_f32_16x16x32_bf16 v[240:243], a[216:219], a[168:171], v[240:243]\n"
"ds_read_b64_tr_b16 a[214:215], v14 offset:39680\n"
"v_mfma_f32_16x16x32_bf16 v[240:243], v[136:139], a[172:175], v[240:243]\n"
"s_waitcnt lgkmcnt(4)\n"
"buffer_load_dwordx4 a[84:87], v1, s[44:47], s21 offen offset:1024 sc1\n"
"s_waitcnt vmcnt(15)\n"
"v_mfma_f32_16x16x32_bf16 v[164:167], a[224:227], a[96:99], v[164:167]\n"
"ds_read_b64_tr_b16 v[96:97], v15 offset:35840\n"
"v_mfma_f32_16x16x32_bf16 v[164:167], v[140:143], a[100:103], v[164:167]\n"
"ds_read_b64_tr_b16 v[98:99], v15 offset:37120\n"
"v_mfma_f32_16x16x32_bf16 v[168:171], a[224:227], a[104:107], v[168:171]\n"
"ds_read_b64_tr_b16 v[100:101], v15 offset:38400\n"
"buffer_load_dwordx4 a[88:91], v1, s[44:47], s21 offen offset:2048 sc1\n"
"v_mfma_f32_16x16x32_bf16 v[168:171], v[140:143], a[108:111], v[168:171]\n"
"ds_read_b64_tr_b16 v[102:103], v15 offset:39680\n"
"v_mfma_f32_16x16x32_bf16 v[172:175], a[44:47], a[96:99], v[172:175]\n"
"ds_read_b64_tr_b16 v[104:105], v16 offset:35840\n"
"v_mfma_f32_16x16x32_bf16 v[172:175], a[132:135], a[100:103], v[172:175]\n"
"ds_read_b64_tr_b16 v[106:107], v16 offset:37120\n"
"v_mfma_f32_16x16x32_bf16 v[176:179], a[44:47], a[104:107], v[176:179]\n"
"ds_read_b64_tr_b16 a[140:141], v16 offset:38400\n"
"buffer_load_dwordx4 a[92:95], v1, s[44:47], s21 offen offset:3072 sc1\n"
"v_mfma_f32_16x16x32_bf16 v[176:179], a[132:135], a[108:111], v[176:179]\n"
"s_waitcnt lgkmcnt(6)\n"
"ds_read_b64_tr_b16 a[142:143], v16 offset:39680\n"
"s_add_u32 s21, s73, 36864\n"
"v_mfma_f32_16x16x32_bf16 v[180:183], v[160:163], a[96:99], v[180:183]\n"
"ds_read_b128 v[144:147], v25 offset:22528\n"
"ds_read_b64_tr_b16 v[80:81], v17 offset:35840\n"
"ds_read_b128 v[148:151], v25 offset:23552\n"
"v_mfma_f32_16x16x32_bf16 v[180:183], a[212:215], a[100:103], v[180:183]\n"
"ds_read_b64_tr_b16 v[82:83], v17 offset:37120\n"
"v_mfma_f32_16x16x32_bf16 v[184:187], v[160:163], a[104:107], v[184:187]\n"
"ds_read_b64_tr_b16 v[84:85], v17 offset:38400\n"
"v_mfma_f32_16x16x32_bf16 v[184:187], a[212:215], a[108:111], v[184:187]\n"
"s_waitcnt lgkmcnt(8)\n"
"ds_read_b64_tr_b16 v[86:87], v17 offset:39680\n"
"buffer_load_dwordx4 a[248:251], v1, s[44:47], s21 offen offset:0 sc1\n"
"v_mfma_f32_16x16x32_bf16 v[188:191], v[96:99], a[96:99], v[188:191]\n"
"ds_read_b64_tr_b16 a[220:221], v18 offset:35840\n"
"v_mfma_f32_16x16x32_bf16 v[188:191], v[100:103], a[100:103], v[188:191]\n"
"ds_read_b64_tr_b16 a[222:223], v18 offset:37120\n"
"v_mfma_f32_16x16x32_bf16 v[192:195], v[96:99], a[104:107], v[192:195]\n"
"ds_read_b64_tr_b16 v[88:89], v18 offset:38400\n"
"v_mfma_f32_16x16x32_bf16 v[192:195], v[100:103], a[108:111], v[192:195]\n"
"s_waitcnt lgkmcnt(6)\n"
"ds_read_b64_tr_b16 v[90:91], v18 offset:39680\n"
"v_mfma_f32_16x16x32_bf16 v[244:247], v[96:99], v[144:147], v[244:247]\n"
"ds_read_b64_tr_b16 v[92:93], v19 offset:35840\n"
"buffer_load_dwordx4 a[252:255], v1, s[44:47], s21 offen offset:1024 sc1\n"
"v_mfma_f32_16x16x32_bf16 v[244:247], v[100:103], v[148:151], v[244:247]\n"
"ds_read_b64_tr_b16 v[94:95], v19 offset:37120\n"
"v_mfma_f32_16x16x32_bf16 v[196:199], v[104:107], a[96:99], v[196:199]\n"
"ds_read_b64_tr_b16 a[196:197], v19 offset:38400\n"
"v_mfma_f32_16x16x32_bf16 v[196:199], a[140:143], a[100:103], v[196:199]\n"
"ds_read_b64_tr_b16 a[198:199], v19 offset:39680\n"
"v_mfma_f32_16x16x32_bf16 v[200:203], v[104:107], a[104:107], v[200:203]\n"
"ds_read_b64_tr_b16 a[128:129], v20 offset:35840\n"
"v_mfma_f32_16x16x32_bf16 v[200:203], a[140:143], a[108:111], v[200:203]\n"
"s_waitcnt lgkmcnt(6)\n"
"ds_read_b64_tr_b16 a[130:131], v20 offset:37120\n"
"v_mfma_f32_16x16x32_bf16 v[204:207], v[80:83], a[96:99], v[204:207]\n"
"ds_read_b64_tr_b16 a[40:41], v20 offset:38400\n"
"v_mfma_f32_16x16x32_bf16 v[204:207], v[84:87], a[100:103], v[204:207]\n"
"ds_read_b64_tr_b16 a[42:43], v20 offset:39680\n"
"v_mfma_f32_16x16x32_bf16 v[208:211], v[80:83], a[104:107], v[208:211]\n"
"ds_read_b64_tr_b16 a[192:193], v21 offset:35840\n"
"v_mfma_f32_16x16x32_bf16 v[208:211], v[84:87], a[108:111], v[208:211]\n"
"ds_read_b64_tr_b16 a[194:195], v21 offset:37120\n"
"v_mfma_f32_16x16x32_bf16 v[212:215], a[220:223], a[96:99], v[212:215]\n"
"s_waitcnt lgkmcnt(6)\n"
"ds_read_b64_tr_b16 a[136:137], v21 offset:38400\n"
"v_mfma_f32_16x16x32_bf16 v[212:215], v[88:91], a[100:103], v[212:215]\n"
"ds_read_b64_tr_b16 a[138:139], v21 offset:39680\n"
"v_mfma_f32_16x16x32_bf16 v[216:219], a[220:223], a[104:107], v[216:219]\n"
"s_waitcnt vmcnt(6)\n"
"s_barrier\n"
"ds_read_b64_tr_b16 a[232:233], v12 offset:25600\n"
"v_mfma_f32_16x16x32_bf16 v[216:219], v[88:91], a[108:111], v[216:219]\n"
"ds_read_b64_tr_b16 a[234:235], v12 offset:26880\n"
"v_mfma_f32_16x16x32_bf16 v[248:251], a[220:223], v[144:147], v[248:251]\n"
"ds_read_b64_tr_b16 v[72:73], v12 offset:28160\n"
"v_mfma_f32_16x16x32_bf16 v[248:251], v[88:91], v[148:151], v[248:251]\n"
"ds_read_b64_tr_b16 v[74:75], v12 offset:29440\n"
"v_mfma_f32_16x16x32_bf16 v[220:223], v[92:95], a[96:99], v[220:223]\n"
"ds_read_b64_tr_b16 v[76:77], v12 offset:15360\n"
"v_mfma_f32_16x16x32_bf16 v[220:223], a[196:199], a[100:103], v[220:223]\n"
"ds_read_b64_tr_b16 v[78:79], v12 offset:16640\n"
"v_mfma_f32_16x16x32_bf16 v[224:227], v[92:95], a[104:107], v[224:227]\n"
"ds_read_b64_tr_b16 a[236:237], v12 offset:17920\n"
"v_mfma_f32_16x16x32_bf16 v[224:227], a[196:199], a[108:111], v[224:227]\n"
"s_waitcnt lgkmcnt(6)\n"
"ds_read_b64_tr_b16 a[238:239], v12 offset:19200\n"
"v_mfma_f32_16x16x32_bf16 v[228:231], a[128:131], a[96:99], v[228:231]\n"
"ds_read_b64_tr_b16 a[228:229], v12 offset:10240\n"
"v_mfma_f32_16x16x32_bf16 v[228:231], a[40:43], a[100:103], v[228:231]\n"
"ds_read_b64_tr_b16 a[230:231], v12 offset:11520\n"
"v_mfma_f32_16x16x32_bf16 v[232:235], a[128:131], a[104:107], v[232:235]\n"
"ds_read_b64_tr_b16 a[244:245], v12 offset:12800\n"
"v_mfma_f32_16x16x32_bf16 v[232:235], a[40:43], a[108:111], v[232:235]\n"
"ds_read_b64_tr_b16 a[246:247], v12 offset:14080\n"
"v_mfma_f32_16x16x32_bf16 v[252:255], a[128:131], v[144:147], v[252:255]\n"
"ds_read_b64_tr_b16 v[128:129], v13 offset:25600\n"
"v_mfma_f32_16x16x32_bf16 v[252:255], a[40:43], v[148:151], v[252:255]\n"
"ds_read_b64_tr_b16 v[130:131], v13 offset:26880\n"
"v_mfma_f32_16x16x32_bf16 v[236:239], a[192:195], a[96:99], v[236:239]\n"
"ds_read_b64_tr_b16 v[132:133], v13 offset:28160\n"
"v_mfma_f32_16x16x32_bf16 v[236:239], a[136:139], a[100:103], v[236:239]\n"
"ds_read_b64_tr_b16 v[134:135], v13 offset:29440\n"
"v_mfma_f32_16x16x32_bf16 v[240:243], a[192:195], a[104:107], v[240:243]\n"
"ds_read_b64_tr_b16 a[216:217], v13 offset:15360\n"
"v_mfma_f32_16x16x32_bf16 v[240:243], a[136:139], a[108:111], v[240:243]\n"
"s_waitcnt lgkmcnt(6)\n"
"s_waitcnt vmcnt(2)\n"
"v_mfma_f32_16x16x32_bf16 v[164:167], a[232:235], a[48:51], v[164:167]\n"
"ds_read_b64_tr_b16 a[218:219], v13 offset:16640\n"
"v_mfma_f32_16x16x32_bf16 v[164:167], v[72:75], a[52:55], v[164:167]\n"
"ds_read_b64_tr_b16 v[136:137], v13 offset:17920\n"
"v_mfma_f32_16x16x32_bf16 v[164:167], v[76:79], a[64:67], v[164:167]\n"
"ds_read_b64_tr_b16 v[138:139], v13 offset:19200\n"
"v_mfma_f32_16x16x32_bf16 v[164:167], a[236:239], a[68:71], v[164:167]\n"
"ds_read_b64_tr_b16 a[224:225], v13 offset:10240\n"
"v_mfma_f32_16x16x32_bf16 v[164:167], a[228:231], a[80:83], v[164:167]\n"
"s_waitcnt lgkmcnt(5)\n"
"ds_read_b64_tr_b16 a[226:227], v13 offset:11520\n"
".Lpad16_%=:\n"
"s_cbranch_execz .Lwb_%=\n"
"v_mfma_f32_16x16x32_bf16 v[164:167], a[244:247], a[84:87], v[164:167]\n"
"ds_read_b64_tr_b16 v[140:141], v13 offset:12800\n"
"v_mfma_f32_16x16x32_bf16 v[168:171], a[232:235], a[56:59], v[168:171]\n"
"ds_read_b64_tr_b16 v[142:143], v13 offset:14080\n"
"v_mfma_f32_16x16x32_bf16 v[168:171], v[72:75], a[60:63], v[168:171]\n"
"ds_read_b64_tr_b16 a[44:45], v14 offset:25600\n"
"v_mfma_f32_16x16x32_bf16 v[168:171], v[76:79], a[72:75], v[168:171]\n"
"ds_read_b64_tr_b16 a[46:47], v14 offset:26880\n"
"v_mfma_f32_16x16x32_bf16 v[168:171], a[236:239], a[76:79], v[168:171]\n"
"ds_read_b64_tr_b16 a[132:133], v14 offset:28160\n"
"v_cvt_pk_bf16_f32 v48, v164, v165\n"
"v_mfma_f32_16x16x32_bf16 v[168:171], a[228:231], a[88:91], v[168:171]\n"
"ds_read_b64_tr_b16 a[134:135], v14 offset:29440\n"
"v_cvt_pk_bf16_f32 v49, v166, v167\n"
"v_mfma_f32_16x16x32_bf16 v[168:171], a[244:247], a[92:95], v[168:171]\n"
"ds_read_b64_tr_b16 v[160:161], v14 offset:15360\n"
"v_mfma_f32_16x16x32_bf16 v[172:175], v[128:131], a[48:51], v[172:175]\n"
"ds_read_b64_tr_b16 v[162:163], v14 offset:16640\n"
"v_mfma_f32_16x16x32_bf16 v[172:175], v[132:135], a[52:55], v[172:175]\n"
"s_waitcnt lgkmcnt(6)\n"
"ds_read_b64_tr_b16 a[212:213], v14 offset:17920\n"
"v_mfma_f32_16x16x32_bf16 v[172:175], a[216:219], a[64:67], v[172:175]\n"
"s_waitcnt vmcnt(0)\n"
"ds_write_b64 v11, a[248:249] offset:20480\n"
"v_mfma_f32_16x16x32_bf16 v[172:175], v[136:139], a[68:71], v[172:175]\n"
"ds_read_b64_tr_b16 a[214:215], v14 offset:19200\n"
"v_cvt_pk_bf16_f32 v50, v168, v169\n"
"v_mfma_f32_16x16x32_bf16 v[172:175], a[224:227], a[80:83], v[172:175]\n"
"ds_read_b64_tr_b16 v[96:97], v14 offset:10240\n"
"v_cvt_pk_bf16_f32 v51, v170, v171\n"
"v_mfma_f32_16x16x32_bf16 v[172:175], v[140:143], a[84:87], v[172:175]\n"
"ds_read_b64_tr_b16 v[98:99], v14 offset:11520\n"
"v_mfma_f32_16x16x32_bf16 v[176:179], v[128:131], a[56:59], v[176:179]\n"
"ds_write_b64 v11, a[250:251] offset:20496\n"
"v_mfma_f32_16x16x32_bf16 v[176:179], v[132:135], a[60:63], v[176:179]\n"
"ds_read_b64_tr_b16 v[100:101], v14 offset:12800\n"
"v_mfma_f32_16x16x32_bf16 v[176:179], a[216:219], a[72:75], v[176:179]\n"
"buffer_store_dwordx4 v[48:51], v1, s[44:47], s24 offen sc1\n"
"v_mfma_f32_16x16x32_bf16 v[176:179], v[136:139], a[76:79], v[176:179]\n"
"ds_read_b64_tr_b16 v[102:103], v14 offset:14080\n"
"ds_write_b64 v11, a[252:253] offset:21504\n"
"v_mfma_f32_16x16x32_bf16 v[176:179], a[224:227], a[88:91], v[176:179]\n"
"ds_read_b64_tr_b16 v[104:105], v15 offset:25600\n"
"ds_write_b64 v11, a[254:255] offset:21520\n"
"v_mfma_f32_16x16x32_bf16 v[176:179], v[140:143], a[92:95], v[176:179]\n"
"s_waitcnt lgkmcnt(7)\n"
"ds_read_b64_tr_b16 v[106:107], v15 offset:26880\n"
"v_cvt_pk_bf16_f32 v56, v172, v173\n"
"v_mfma_f32_16x16x32_bf16 v[180:183], a[44:47], a[48:51], v[180:183]\n"
"s_waitcnt lgkmcnt(1)\n"
"s_barrier\n"
"ds_read_b64_tr_b16 a[140:141], v15 offset:28160\n"
"v_mfma_f32_16x16x32_bf16 v[180:183], a[132:135], a[52:55], v[180:183]\n"
"ds_read_b64_tr_b16 a[142:143], v15 offset:29440\n"
"v_cvt_pk_bf16_f32 v57, v174, v175\n"
"v_mfma_f32_16x16x32_bf16 v[180:183], v[160:163], a[64:67], v[180:183]\n"
"ds_read_b64_tr_b16 v[80:81], v15 offset:15360\n"
"v_mfma_f32_16x16x32_bf16 v[180:183], a[212:215], a[68:71], v[180:183]\n"
"ds_read_b64_tr_b16 v[82:83], v15 offset:16640\n"
"v_cvt_pk_bf16_f32 v58, v176, v177\n"
"v_mfma_f32_16x16x32_bf16 v[180:183], v[96:99], a[80:83], v[180:183]\n"
"ds_read_b64_tr_b16 v[84:85], v15 offset:17920\n"
"v_cvt_pk_bf16_f32 v59, v178, v179\n"
"v_mfma_f32_16x16x32_bf16 v[180:183], v[100:103], a[84:87], v[180:183]\n"
"ds_read_b64_tr_b16 v[86:87], v15 offset:19200\n"
"v_mfma_f32_16x16x32_bf16 v[184:187], a[44:47], a[56:59], v[184:187]\n"
"ds_read_b64_tr_b16 a[220:221], v15 offset:10240\n"
"v_mfma_f32_16x16x32_bf16 v[184:187], a[132:135], a[60:63], v[184:187]\n"
"ds_read_b64_tr_b16 a[222:223], v15 offset:11520\n"
"v_mfma_f32_16x16x32_bf16 v[184:187], v[160:163], a[72:75], v[184:187]\n"
"ds_read_b64_tr_b16 v[88:89], v15 offset:12800\n"
"buffer_store_dwordx4 v[56:59], v1, s[44:47], s25 offen sc1\n"
"v_mfma_f32_16x16x32_bf16 v[184:187], a[212:215], a[76:79], v[184:187]\n"
"ds_read_b64_tr_b16 v[90:91], v15 offset:14080\n"
"v_cvt_pk_bf16_f32 v64, v180, v181\n"
"v_mfma_f32_16x16x32_bf16 v[184:187], v[96:99], a[88:91], v[184:187]\n"
"ds_read_b128 a[112:115], v1 offset:20480\n"
"v_cvt_pk_bf16_f32 v65, v182, v183\n"
"v_mfma_f32_16x16x32_bf16 v[184:187], v[100:103], a[92:95], v[184:187]\n"
"s_waitcnt lgkmcnt(6)\n"
"ds_read_b128 a[116:119], v1 offset:21504\n"
"v_mfma_f32_16x16x32_bf16 v[188:191], v[104:107], a[48:51], v[188:191]\n"
"ds_read_b128 a[120:123], v1 offset:61440\n"
"v_mfma_f32_16x16x32_bf16 v[188:191], a[140:143], a[52:55], v[188:191]\n"
"ds_read_b128 a[124:127], v1 offset:62464\n"
"v_mfma_f32_16x16x32_bf16 v[188:191], v[80:83], a[64:67], v[188:191]\n"
"s_waitcnt lgkmcnt(6)\n"
"ds_read_b64_tr_b16 v[92:93], v16 offset:25600\n"
"v_mfma_f32_16x16x32_bf16 v[188:191], v[84:87], a[68:71], v[188:191]\n"
"ds_read_b128 a[144:147], v25 offset:20480\n"
"v_cvt_pk_bf16_f32 v66, v184, v185\n"
"v_mfma_f32_16x16x32_bf16 v[188:191], a[220:223], a[80:83], v[188:191]\n"
"s_waitcnt lgkmcnt(6)\n"
"ds_read_b128 a[148:151], v25 offset:21504\n"
"v_cvt_pk_bf16_f32 v67, v186, v187\n"
"v_mfma_f32_16x16x32_bf16 v[188:191], v[88:91], a[84:87], v[188:191]\n"
"ds_read_b64_tr_b16 v[94:95], v16 offset:26880\n"
"v_mfma_f32_16x16x32_bf16 v[192:195], v[104:107], a[56:59], v[192:195]\n"
"ds_read_b64_tr_b16 a[196:197], v16 offset:28160\n"
"v_mfma_f32_16x16x32_bf16 v[192:195], a[140:143], a[60:63], v[192:195]\n"
"ds_read_b64_tr_b16 a[198:199], v16 offset:29440\n"
"v_mfma_f32_16x16x32_bf16 v[192:195], v[80:83], a[72:75], v[192:195]\n"
"ds_read_b64_tr_b16 a[128:129], v16 offset:15360\n"
"buffer_store_dwordx4 v[64:67], v1, s[44:47], s26 offen sc1\n"
"v_mfma_f32_16x16x32_bf16 v[192:195], v[84:87], a[76:79], v[192:195]\n"
"ds_read_b64_tr_b16 a[130:131], v16 offset:16640\n"
"v_cvt_pk_bf16_f32 v48, v188, v189\n"
"v_mfma_f32_16x16x32_bf16 v[192:195], a[220:223], a[88:91], v[192:195]\n"
"ds_read_b64_tr_b16 a[40:41], v16 offset:17920\n"
"v_cvt_pk_bf16_f32 v49, v190, v191\n"
"v_mfma_f32_16x16x32_bf16 v[192:195], v[88:91], a[92:95], v[192:195]\n"
"s_waitcnt lgkmcnt(6)\n"
"ds_read_b64_tr_b16 a[42:43], v16 offset:19200\n"
"v_mfma_f32_16x16x32_bf16 v[244:247], v[104:107], a[112:115], v[244:247]\n"
"ds_read_b64_tr_b16 a[192:193], v16 offset:10240\n"
"v_mfma_f32_16x16x32_bf16 v[244:247], a[140:143], a[116:119], v[244:247]\n"
"ds_read_b64_tr_b16 a[194:195], v16 offset:11520\n"
"v_mfma_f32_16x16x32_bf16 v[244:247], v[80:83], a[120:123], v[244:247]\n"
"ds_read_b64_tr_b16 a[136:137], v16 offset:12800\n"
"v_mfma_f32_16x16x32_bf16 v[244:247], v[84:87], a[124:127], v[244:247]\n"
"ds_read_b64_tr_b16 a[138:139], v16 offset:14080\n"
"v_cvt_pk_bf16_f32 v50, v192, v193\n"
"v_mfma_f32_16x16x32_bf16 v[244:247], a[220:223], a[144:147], v[244:247]\n"
"ds_read_b64_tr_b16 a[232:233], v17 offset:25600\n"
"v_cvt_pk_bf16_f32 v51, v194, v195\n"
"v_mfma_f32_16x16x32_bf16 v[244:247], v[88:91], a[148:151], v[244:247]\n"
"s_waitcnt lgkmcnt(6)\n"
"ds_read_b64_tr_b16 a[234:235], v17 offset:26880\n"
"v_mfma_f32_16x16x32_bf16 v[196:199], v[92:95], a[48:51], v[196:199]\n"
"ds_read_b64_tr_b16 v[72:73], v17 offset:28160\n"
"v_mfma_f32_16x16x32_bf16 v[196:199], a[196:199], a[52:55], v[196:199]\n"
"ds_read_b64_tr_b16 v[74:75], v17 offset:29440\n"
"v_mfma_f32_16x16x32_bf16 v[196:199], a[128:131], a[64:67], v[196:199]\n"
"s_waitcnt lgkmcnt(6)\n"
"ds_read_b64_tr_b16 v[76:77], v17 offset:15360\n"
"buffer_store_dwordx4 v[48:51], v1, s[44:47], s27 offen sc1\n"
"v_mfma_f32_16x16x32_bf16 v[196:199], a[40:43], a[68:71], v[196:199]\n"
"ds_read_b64_tr_b16 v[78:79], v17 offset:16640\n"
"v_cvt_pk_bf16_f32 v52, v244, v245\n"
"v_mfma_f32_16x16x32_bf16 v[196:199], a[192:195], a[80:83], v[196:199]\n"
"s_waitcnt lgkmcnt(6)\n"
"ds_read_b64_tr_b16 a[236:237], v17 offset:17920\n"
"v_cvt_pk_bf16_f32 v53, v246, v247\n"
"v_mfma_f32_16x16x32_bf16 v[196:199], a[136:139], a[84:87], v[196:199]\n"
"ds_read_b64_tr_b16 a[238:239], v17 offset:19200\n"
"v_mfma_f32_16x16x32_bf16 v[200:203], v[92:95], a[56:59], v[200:203]\n"
"ds_read_b64_tr_b16 a[228:229], v17 offset:10240\n"
"v_mfma_f32_16x16x32_bf16 v[200:203], a[196:199], a[60:63], v[200:203]\n"
"ds_read_b64_tr_b16 a[230:231], v17 offset:11520\n"
"v_mfma_f32_16x16x32_bf16 v[200:203], a[128:131], a[72:75], v[200:203]\n"
"ds_read_b64_tr_b16 a[244:245], v17 offset:12800\n"
"buffer_store_dwordx2 v[52:53], v122, s[44:47], s35 offen sc1\n"
"v_mfma_f32_16x16x32_bf16 v[200:203], a[40:43], a[76:79], v[200:203]\n"
"ds_read_b64_tr_b16 a[246:247], v17 offset:14080\n"
"v_cvt_pk_bf16_f32 v56, v196, v197\n"
"v_mfma_f32_16x16x32_bf16 v[200:203], a[192:195], a[88:91], v[200:203]\n"
"ds_read_b64_tr_b16 v[128:129], v18 offset:25600\n"
"v_cvt_pk_bf16_f32 v57, v198, v199\n"
"v_mfma_f32_16x16x32_bf16 v[200:203], a[136:139], a[92:95], v[200:203]\n"
"s_waitcnt lgkmcnt(6)\n"
"ds_read_b64_tr_b16 v[130:131], v18 offset:26880\n"
"v_mfma_f32_16x16x32_bf16 v[204:207], a[232:235], a[48:51], v[204:207]\n"
"ds_read_b64_tr_b16 v[132:133], v18 offset:28160\n"
"v_mfma_f32_16x16x32_bf16 v[204:207], v[72:75], a[52:55], v[204:207]\n"
"ds_read_b64_tr_b16 v[134:135], v18 offset:29440\n"
"v_mfma_f32_16x16x32_bf16 v[204:207], v[76:79], a[64:67], v[204:207]\n"
"s_waitcnt lgkmcnt(6)\n"
"ds_read_b64_tr_b16 a[216:217], v18 offset:15360\n"
"v_mfma_f32_16x16x32_bf16 v[204:207], a[236:239], a[68:71], v[204:207]\n"
"ds_read_b64_tr_b16 a[218:219], v18 offset:16640\n"
"v_cvt_pk_bf16_f32 v58, v200, v201\n"
"v_mfma_f32_16x16x32_bf16 v[204:207], a[228:231], a[80:83], v[204:207]\n"
"s_waitcnt lgkmcnt(6)\n"
"ds_read_b64_tr_b16 v[136:137], v18 offset:17920\n"
"v_cvt_pk_bf16_f32 v59, v202, v203\n"
"v_mfma_f32_16x16x32_bf16 v[204:207], a[244:247], a[84:87], v[204:207]\n"
"ds_read_b64_tr_b16 v[138:139], v18 offset:19200\n"
"v_mfma_f32_16x16x32_bf16 v[208:211], a[232:235], a[56:59], v[208:211]\n"
"ds_read_b64_tr_b16 a[224:225], v18 offset:10240\n"
"v_mfma_f32_16x16x32_bf16 v[208:211], v[72:75], a[60:63], v[208:211]\n"
"ds_read_b64_tr_b16 a[226:227], v18 offset:11520\n"
"v_mfma_f32_16x16x32_bf16 v[208:211], v[76:79], a[72:75], v[208:211]\n"
"ds_read_b64_tr_b16 v[140:141], v18 offset:12800\n"
"buffer_store_dwordx4 v[56:59], v1, s[44:47], s28 offen sc1\n"
"v_mfma_f32_16x16x32_bf16 v[208:211], a[236:239], a[76:79], v[208:211]\n"
"ds_read_b64_tr_b16 v[142:143], v18 offset:14080\n"
"v_cvt_pk_bf16_f32 v64, v204, v205\n"
"v_mfma_f32_16x16x32_bf16 v[208:211], a[228:231], a[88:91], v[208:211]\n"
"v_cvt_pk_bf16_f32 v65, v206, v207\n"
"v_mfma_f32_16x16x32_bf16 v[208:211], a[244:247], a[92:95], v[208:211]\n"
"s_waitcnt lgkmcnt(5)\n"
"v_mfma_f32_16x16x32_bf16 v[212:215], v[128:131], a[48:51], v[212:215]\n"
"ds_read_b64_tr_b16 a[44:45], v19 offset:25600\n"
"v_mfma_f32_16x16x32_bf16 v[212:215], v[132:135], a[52:55], v[212:215]\n"
"ds_read_b64_tr_b16 a[46:47], v19 offset:26880\n"
"v_mfma_f32_16x16x32_bf16 v[212:215], a[216:219], a[64:67], v[212:215]\n"
"s_waitcnt lgkmcnt(4)\n"
"ds_read_b64_tr_b16 a[132:133], v19 offset:28160\n"
"v_mfma_f32_16x16x32_bf16 v[212:215], v[136:139], a[68:71], v[212:215]\n"
"ds_read_b64_tr_b16 a[134:135], v19 offset:29440\n"
"v_cvt_pk_bf16_f32 v66, v208, v209\n"
"v_mfma_f32_16x16x32_bf16 v[212:215], a[224:227], a[80:83], v[212:215]\n"
"s_waitcnt lgkmcnt(4)\n"
"ds_read_b64_tr_b16 v[160:161], v19 offset:15360\n"
"v_cvt_pk_bf16_f32 v67, v210, v211\n"
"v_mfma_f32_16x16x32_bf16 v[212:215], v[140:143], a[84:87], v[212:215]\n"
"buffer_store_dwordx4 v[64:67], v1, s[44:47], s29 offen sc1\n"
"v_mfma_f32_16x16x32_bf16 v[216:219], v[128:131], a[56:59], v[216:219]\n"
"ds_read_b64_tr_b16 v[162:163], v19 offset:16640\n"
"v_mfma_f32_16x16x32_bf16 v[216:219], v[132:135], a[60:63], v[216:219]\n"
"ds_read_b64_tr_b16 a[212:213], v19 offset:17920\n"
"v_mfma_f32_16x16x32_bf16 v[216:219], a[216:219], a[72:75], v[216:219]\n"
"ds_read_b64_tr_b16 a[214:215], v19 offset:19200\n"
"v_mfma_f32_16x16x32_bf16 v[216:219], v[136:139], a[76:79], v[216:219]\n"
"ds_read_b64_tr_b16 v[96:97], v19 offset:10240\n"
"v_cvt_pk_bf16_f32 v48, v212, v213\n"
"v_mfma_f32_16x16x32_bf16 v[216:219], a[224:227], a[88:91], v[216:219]\n"
"ds_read_b64_tr_b16 v[98:99], v19 offset:11520\n"
"v_cvt_pk_bf16_f32 v49, v214, v215\n"
"v_mfma_f32_16x16x32_bf16 v[216:219], v[140:143], a[92:95], v[216:219]\n"
"buffer_load_dwordx2 v[124:125], v114, s[44:47], 0 offen offset:1544 sc1\n"
"v_mfma_f32_16x16x32_bf16 v[248:251], v[128:131], a[112:115], v[248:251]\n"
"ds_read_b64_tr_b16 v[100:101], v19 offset:12800\n"
"v_mfma_f32_16x16x32_bf16 v[248:251], v[132:135], a[116:119], v[248:251]\n"
"ds_read_b64_tr_b16 v[102:103], v19 offset:14080\n"
"v_mfma_f32_16x16x32_bf16 v[248:251], a[216:219], a[120:123], v[248:251]\n"
"ds_read_b64_tr_b16 v[104:105], v20 offset:25600\n"
"v_mfma_f32_16x16x32_bf16 v[248:251], v[136:139], a[124:127], v[248:251]\n"
"ds_read_b64_tr_b16 v[106:107], v20 offset:26880\n"
"v_cvt_pk_bf16_f32 v50, v216, v217\n"
"v_mfma_f32_16x16x32_bf16 v[248:251], a[224:227], a[144:147], v[248:251]\n"
"ds_read_b64_tr_b16 a[140:141], v20 offset:28160\n"
"v_cvt_pk_bf16_f32 v51, v218, v219\n"
"v_mfma_f32_16x16x32_bf16 v[248:251], v[140:143], a[148:151], v[248:251]\n"
"s_waitcnt lgkmcnt(5)\n"
"ds_read_b64_tr_b16 a[142:143], v20 offset:29440\n"
"v_mfma_f32_16x16x32_bf16 v[220:223], a[44:47], a[48:51], v[220:223]\n"
"ds_read_b64_tr_b16 v[80:81], v20 offset:15360\n"
"v_mfma_f32_16x16x32_bf16 v[220:223], a[132:135], a[52:55], v[220:223]\n"
"buffer_store_dwordx4 v[48:51], v1, s[44:47], s30 offen sc1\n"
"v_mfma_f32_16x16x32_bf16 v[220:223], v[160:163], a[64:67], v[220:223]\n"
"ds_read_b64_tr_b16 v[82:83], v20 offset:16640\n"
"v_mfma_f32_16x16x32_bf16 v[220:223], a[212:215], a[68:71], v[220:223]\n"
"ds_read_b64_tr_b16 v[84:85], v20 offset:17920\n"
"v_cvt_pk_bf16_f32 v52, v248, v249\n"
"v_mfma_f32_16x16x32_bf16 v[220:223], v[96:99], a[80:83], v[220:223]\n"
"s_waitcnt lgkmcnt(5)\n"
"ds_read_b64_tr_b16 v[86:87], v20 offset:19200\n"
"v_cvt_pk_bf16_f32 v53, v250, v251\n"
"v_mfma_f32_16x16x32_bf16 v[220:223], v[100:103], a[84:87], v[220:223]\n"
"ds_read_b64_tr_b16 a[220:221], v20 offset:10240\n"
"v_mfma_f32_16x16x32_bf16 v[224:227], a[44:47], a[56:59], v[224:227]\n"
"ds_read_b64_tr_b16 a[222:223], v20 offset:11520\n"
"v_mfma_f32_16x16x32_bf16 v[224:227], a[132:135], a[60:63], v[224:227]\n"
"ds_read_b64_tr_b16 v[88:89], v20 offset:12800\n"
"v_mfma_f32_16x16x32_bf16 v[224:227], v[160:163], a[72:75], v[224:227]\n"
"buffer_store_dwordx2 v[52:53], v122, s[44:47], s74 offen sc1\n"
"v_mfma_f32_16x16x32_bf16 v[224:227], a[212:215], a[76:79], v[224:227]\n"
"ds_read_b64_tr_b16 v[90:91], v20 offset:14080\n"
"v_cvt_pk_bf16_f32 v56, v220, v221\n"
"v_mfma_f32_16x16x32_bf16 v[224:227], v[96:99], a[88:91], v[224:227]\n"
"v_cvt_pk_bf16_f32 v57, v222, v223\n"
"v_mfma_f32_16x16x32_bf16 v[224:227], v[100:103], a[92:95], v[224:227]\n"
"v_mfma_f32_16x16x32_bf16 v[228:231], v[104:107], a[48:51], v[228:231]\n"
"s_waitcnt lgkmcnt(3)\n"
"ds_read_b64_tr_b16 v[92:93], v21 offset:25600\n"
"v_mfma_f32_16x16x32_bf16 v[228:231], a[140:143], a[52:55], v[228:231]\n"
"ds_read_b64_tr_b16 v[94:95], v21 offset:26880\n"
"v_mfma_f32_16x16x32_bf16 v[228:231], v[80:83], a[64:67], v[228:231]\n"
"ds_read_b64_tr_b16 a[196:197], v21 offset:28160\n"
"v_mfma_f32_16x16x32_bf16 v[228:231], v[84:87], a[68:71], v[228:231]\n"
"s_waitcnt lgkmcnt(4)\n"
"ds_read_b64_tr_b16 a[198:199], v21 offset:29440\n"
"v_cvt_pk_bf16_f32 v58, v224, v225\n"
"v_mfma_f32_16x16x32_bf16 v[228:231], a[220:223], a[80:83], v[228:231]\n"
"s_waitcnt lgkmcnt(4)\n"
"ds_read_b64_tr_b16 a[128:129], v21 offset:15360\n"
"v_cvt_pk_bf16_f32 v59, v226, v227\n"
"v_mfma_f32_16x16x32_bf16 v[228:231], v[88:91], a[84:87], v[228:231]\n"
"buffer_store_dwordx4 v[56:59], v1, s[44:47], s31 offen sc1\n"
"v_mfma_f32_16x16x32_bf16 v[232:235], v[104:107], a[56:59], v[232:235]\n"
"ds_read_b64_tr_b16 a[130:131], v21 offset:16640\n"
"v_mfma_f32_16x16x32_bf16 v[232:235], a[140:143], a[60:63], v[232:235]\n"
"ds_read_b64_tr_b16 a[40:41], v21 offset:17920\n"
"v_mfma_f32_16x16x32_bf16 v[232:235], v[80:83], a[72:75], v[232:235]\n"
"ds_read_b64_tr_b16 a[42:43], v21 offset:19200\n"
"v_mfma_f32_16x16x32_bf16 v[232:235], v[84:87], a[76:79], v[232:235]\n"
"ds_read_b64_tr_b16 a[192:193], v21 offset:10240\n"
"v_cvt_pk_bf16_f32 v64, v228, v229\n"
"v_mfma_f32_16x16x32_bf16 v[232:235], a[220:223], a[88:91], v[232:235]\n"
"ds_read_b64_tr_b16 a[194:195], v21 offset:11520\n"
"v_cvt_pk_bf16_f32 v65, v230, v231\n"
"v_mfma_f32_16x16x32_bf16 v[232:235], v[88:91], a[92:95], v[232:235]\n"
"ds_read_b64_tr_b16 a[136:137], v21 offset:12800\n"
"v_mfma_f32_16x16x32_bf16 v[252:255], v[104:107], a[112:115], v[252:255]\n"
"ds_read_b64_tr_b16 a[138:139], v21 offset:14080\n"
"v_mfma_f32_16x16x32_bf16 v[252:255], a[140:143], a[116:119], v[252:255]\n"
"v_mfma_f32_16x16x32_bf16 v[252:255], v[80:83], a[120:123], v[252:255]\n"
"v_mfma_f32_16x16x32_bf16 v[252:255], v[84:87], a[124:127], v[252:255]\n"
"s_nop 1\n"
"v_cvt_pk_bf16_f32 v66, v232, v233\n"
"v_cvt_pk_bf16_f32 v67, v234, v235\n"
"v_mfma_f32_16x16x32_bf16 v[252:255], a[220:223], a[144:147], v[252:255]\n"
"buffer_store_dwordx4 v[64:67], v1, s[44:47], s33 offen sc1\n"
"v_mfma_f32_16x16x32_bf16 v[252:255], v[88:91], a[148:151], v[252:255]\n"
"s_waitcnt lgkmcnt(2)\n"
"v_mfma_f32_16x16x32_bf16 v[236:239], v[92:95], a[48:51], v[236:239]\n"
"v_mfma_f32_16x16x32_bf16 v[236:239], a[196:199], a[52:55], v[236:239]\n"
"v_mfma_f32_16x16x32_bf16 v[236:239], a[128:131], a[64:67], v[236:239]\n"
"v_mfma_f32_16x16x32_bf16 v[236:239], a[40:43], a[68:71], v[236:239]\n"
"s_nop 2\n"
"v_cvt_pk_bf16_f32 v68, v252, v253\n"
"v_cvt_pk_bf16_f32 v69, v254, v255\n"
"v_mfma_f32_16x16x32_bf16 v[236:239], a[192:195], a[80:83], v[236:239]\n"
"s_waitcnt lgkmcnt(0)\n"
"buffer_store_dwordx2 v[68:69], v122, s[44:47], s75 offen sc1\n"
"v_mfma_f32_16x16x32_bf16 v[236:239], a[136:139], a[84:87], v[236:239]\n"
"v_mfma_f32_16x16x32_bf16 v[240:243], v[92:95], a[56:59], v[240:243]\n"
"v_mfma_f32_16x16x32_bf16 v[240:243], a[196:199], a[60:63], v[240:243]\n"
"v_mfma_f32_16x16x32_bf16 v[240:243], a[128:131], a[72:75], v[240:243]\n"
"v_mfma_f32_16x16x32_bf16 v[240:243], a[40:43], a[76:79], v[240:243]\n"
"s_nop 3\n"
"v_cvt_pk_bf16_f32 v48, v236, v237\n"
"v_cvt_pk_bf16_f32 v49, v238, v239\n"
"v_mfma_f32_16x16x32_bf16 v[240:243], a[192:195], a[88:91], v[240:243]\n"
"v_mfma_f32_16x16x32_bf16 v[240:243], a[136:139], a[92:95], v[240:243]\n"
"s_nop 7\n"
"v_cvt_pk_bf16_f32 v50, v240, v241\n"
"v_cvt_pk_bf16_f32 v51, v242, v243\n"
"buffer_store_dwordx4 v[48:51], v1, s[44:47], s34 offen sc1\n"
"s_mov_b64 s[14:15], -1\n"
"s_lshl_b32 s16, s54, 3\n"
"s_lshl_b64 s[18:19], 0xff, s16\n"
"s_mov_b32 s72, 0\n"
"s_waitcnt vmcnt(0)\n"
"s_barrier\n"
"s_mov_b64 exec, 0xffff\n"
"buffer_store_dwordx4 v[24:27], v1, s[44:47], s12 offen offset:1536 sc1\n"
"s_mov_b64 exec, -1\n"
".Lrp_%=:\n"
"v_cmp_eq_u64 vcc, v[124:125], s[58:59]\n"
"s_add_u32 s72, s72, 1\n"
"s_cmp_lt_u32 s72, 0x80000\n"
"s_cselect_b64 s[18:19], s[18:19], -1\n"
"s_nop 0\n"
"s_or_b64 s[16:17], vcc, s[18:19]\n"
"s_and_b64 s[16:17], s[16:17], s[14:15]\n"
"s_cbranch_scc0 .Lrn_%=\n"
"s_andn2_b64 s[14:15], s[14:15], s[16:17]\n"
"s_bitcmp1_b64 s[16:17], 0\n"
"s_cbranch_scc0 .Lrs0_%=\n"
"s_add_u32 s6, s7, 0\n"
"buffer_load_dwordx4 a[0:3], v108, s[44:47], s6 offen sc1\n"
"buffer_load_dwordx4 a[4:7], v109, s[44:47], s6 offen sc1\n"
".Lrs0_%=:\n"
"s_bitcmp1_b64 s[16:17], 8\n"
"s_cbranch_scc0 .Lrs1_%=\n"
"s_add_u32 s6, s7, 46080\n"
"buffer_load_dwordx4 a[8:11], v108, s[44:47], s6 offen sc1\n"
"buffer_load_dwordx4 a[12:15], v109, s[44:47], s6 offen sc1\n"
".Lrs1_%=:\n"
"s_bitcmp1_b64 s[16:17], 16\n"
"s_cbranch_scc0 .Lrs2_%=\n"
"s_add_u32 s6, s7, 92160\n"
"buffer_load_dwordx4 a[16:19], v108, s[44:47], s6 offen sc1\n"
"buffer_load_dwordx4 a[20:23], v109, s[44:47], s6 offen sc1\n"
".Lrs2_%=:\n"
"s_bitcmp1_b64 s[16:17], 24\n"
"s_cbranch_scc0 .Lrs3_%=\n"
"s_add_u32 s6, s7, 138240\n"
"buffer_load_dwordx4 a[24:27], v108, s[44:47], s6 offen sc1\n"
"buffer_load_dwordx4 a[28:31], v109, s[44:47], s6 offen sc1\n"
".Lrs3_%=:\n"
"s_bitcmp1_b64 s[16:17], 32\n"
"s_cbranch_scc0 .Lrs4_%=\n"
"s_add_u32 s6, s7, 184320\n"
"buffer_load_dwordx4 a[32:35], v108, s[44:47], s6 offen sc1\n"
"buffer_load_dwordx4 a[36:39], v109, s[44:47], s6 offen sc1\n"
".Lrs4_%=:\n"
"s_bitcmp1_b64 s[16:17], 40\n"
"s_cbranch_scc0 .Lrs5_%=\n"
"s_add_u32 s6, s7, 230400\n"
"buffer_load_dwordx4 a[40:43], v108, s[44:47], s6 offen sc1\n"
"buffer_load_dwordx4 a[44:47], v109, s[44:47], s6 offen sc1\n"
".Lrs5_%=:\n"
"s_bitcmp1_b64 s[16:17], 48\n"
"s_cbranch_scc0 .Lrs6_%=\n"
"s_add_u32 s6, s7, 276480\n"
"buffer_load_dwordx4 a[48:51], v108, s[44:47], s6 offen sc1\n"
"buffer_load_dwordx4 a[52:55], v109, s[44:47], s6 offen sc1\n"
".Lrs6_%=:\n"
"s_bitcmp1_b64 s[16:17], 56\n"
"s_cbranch_scc0 .Lrs7_%=\n"
"s_add_u32 s6, s7, 322560\n"
"buffer_load_dwordx4 a[56:59], v108, s[44:47], s6 offen sc1\n"
"buffer_load_dwordx4 a[60:63], v109, s[44:47], s6 offen sc1\n"
".Lrs7_%=:\n"
"s_cmp_eq_u64 s[14:15], 0\n"
"s_cbranch_scc1 .Lred_%=\n"
".Lrn_%=:\n"
"s_sleep 2\n"
"buffer_load_dwordx2 v[124:125], v114, s[44:47], 0 offen offset:1544 sc1\n"
"s_waitcnt vmcnt(0)\n"
"s_branch .Lrp_%=\n"
".Lred_%=:\n"
"s_and_b32 s16, s57, 31\n"
"s_or_b32 s16, s16, s52\n"
"s_cmp_lg_u32 s16, 0\n"
"s_waitcnt vmcnt(0)\n"
"v_mfma_f32_16x16x32_bf16 v[80:83], v[40:43], a[0:3], 0\n"
"v_mfma_f32_16x16x32_bf16 v[84:87], v[42:45], a[0:3], 0\n"
"v_mfma_f32_16x16x32_bf16 v[88:91], v[40:43], a[4:7], 0\n"
"v_mfma_f32_16x16x32_bf16 v[92:95], v[42:45], a[4:7], 0\n"
"v_mfma_f32_16x16x32_bf16 v[80:83], v[40:43], a[8:11], v[80:83]\n"
"v_mfma_f32_16x16x32_bf16 v[84:87], v[42:45], a[8:11], v[84:87]\n"
"v_mfma_f32_16x16x32_bf16 v[88:91], v[40:43], a[12:15], v[88:91]\n"
"v_mfma_f32_16x16x32_bf16 v[92:95], v[42:45], a[12:15], v[92:95]\n"
"v_mfma_f32_16x16x32_bf16 v[80:83], v[40:43], a[16:19], v[80:83]\n"
"v_mfma_f32_16x16x32_bf16 v[84:87], v[42:45], a[16:19], v[84:87]\n"
"v_mfma_f32_16x16x32_bf16 v[88:91], v[40:43], a[20:23], v[88:91]\n"
"v_mfma_f32_16x16x32_bf16 v[92:95], v[42:45], a[20:23], v[92:95]\n"
"v_mfma_f32_16x16x32_bf16 v[80:83], v[40:43], a[24:27], v[80:83]\n"
"v_mfma_f32_16x16x32_bf16 v[84:87], v[42:45], a[24:27], v[84:87]\n"
"v_mfma_f32_16x16x32_bf16 v[88:91], v[40:43], a[28:31], v[88:91]\n"
"v_mfma_f32_16x16x32_bf16 v[92:95], v[42:45], a[28:31], v[92:95]\n"
"v_mfma_f32_16x16x32_bf16 v[80:83], v[40:43], a[32:35], v[80:83]\n"
"v_mfma_f32_16x16x32_bf16 v[84:87], v[42:45], a[32:35], v[84:87]\n"
"v_mfma_f32_16x16x32_bf16 v[88:91], v[40:43], a[36:39], v[88:91]\n"
"v_mfma_f32_16x16x32_bf16 v[92:95], v[42:45], a[36:39], v[92:95]\n"
"v_mfma_f32_16x16x32_bf16 v[80:83], v[40:43], a[40:43], v[80:83]\n"
"v_mfma_f32_16x16x32_bf16 v[84:87], v[42:45], a[40:43], v[84:87]\n"
"v_mfma_f32_16x16x32_bf16 v[88:91], v[40:43], a[44:47], v[88:91]\n"
"v_mfma_f32_16x16x32_bf16 v[92:95], v[42:45], a[44:47], v[92:95]\n"
"v_mfma_f32_16x16x32_bf16 v[80:83], v[40:43], a[48:51], v[80:83]\n"
"v_mfma_f32_16x16x32_bf16 v[84:87], v[42:45], a[48:51], v[84:87]\n"
"v_mfma_f32_16x16x32_bf16 v[88:91], v[40:43], a[52:55], v[88:91]\n"
"v_mfma_f32_16x16x32_bf16 v[92:95], v[42:45], a[52:55], v[92:95]\n"
"v_mfma_f32_16x16x32_bf16 v[80:83], v[40:43], a[56:59], v[80:83]\n"
"v_mfma_f32_16x16x32_bf16 v[84:87], v[42:45], a[56:59], v[84:87]\n"
"v_mfma_f32_16x16x32_bf16 v[88:91], v[40:43], a[60:63], v[88:91]\n"
"v_mfma_f32_16x16x32_bf16 v[92:95], v[42:45], a[60:63], v[92:95]\n"
"s_nop 4\n"
"v_cvt_pk_bf16_f32 v48, v80, v81\n"
"v_cvt_pk_bf16_f32 v49, v82, v83\n"
"v_cvt_pk_bf16_f32 v50, v84, v85\n"
"v_cvt_pk_bf16_f32 v51, v86, v87\n"
"buffer_store_dwordx2 v[48:49], v110, s[48:51], 0 offen\n"
"buffer_store_dwordx2 v[50:51], v112, s[48:51], 0 offen\n"
"v_cvt_pk_bf16_f32 v52, v88, v89\n"
"v_cvt_pk_bf16_f32 v53, v90, v91\n"
"v_cvt_pk_bf16_f32 v54, v92, v93\n"
"v_cvt_pk_bf16_f32 v55, v94, v95\n"
"s_mov_b64 exec, 0x3ffffff\n"
"buffer_store_dwordx2 v[52:53], v111, s[48:51], 0 offen\n"
"buffer_store_dwordx2 v[54:55], v113, s[48:51], 0 offen\n"
"s_mov_b64 exec, -1\n"
"s_cbranch_scc1 .Lnoe_%=\n"
"v_mov_b32 v124, s58\n"
"v_mov_b32 v125, s59\n"
"s_lshl_b32 s16, s55, 7\n"
"s_mov_b64 exec, 1\n"
"buffer_store_dwordx2 v[124:125], v1, s[44:47], s16 offen sc1\n"
"s_mov_b64 exec, -1\n"
".Lnoe_%=:\n"
"s_branch .Lend_%=\n"
".Lsp0_%=:\n"
"s_mov_b32 s72, 0\n"
".Lsq0_%=:\n"
"s_sleep 1\n"
"buffer_load_dwordx2 v[124:125], v7, s[44:47], 0 offen offset:520 sc1\n"
"s_waitcnt vmcnt(0)\n"
"v_cmp_ne_u64 vcc, v[124:125], s[58:59]\n"
"s_add_u32 s72, s72, 1\n"
"s_nop 3\n"
"s_cbranch_vccz .Lpr1_%=\n"
"s_cmp_lt_u32 s72, 0x80000\n"
"s_cbranch_scc1 .Lsq0_%=\n"
"s_branch .Lpr1_%=\n"
".Lsp1_%=:\n"
"s_mov_b32 s72, 0\n"
".Lsq1_%=:\n"
"s_sleep 1\n"
"buffer_load_dwordx2 v[124:125], v7, s[44:47], 0 offen offset:1032 sc1\n"
"s_waitcnt vmcnt(0)\n"
"v_cmp_ne_u64 vcc, v[124:125], s[58:59]\n"
"s_add_u32 s72, s72, 1\n"
"s_nop 3\n"
"s_cbranch_vccz .Lpr2_%=\n"
"s_cmp_lt_u32 s72, 0x80000\n"
"s_cbranch_scc1 .Lsq1_%=\n"
"s_branch .Lpr2_%=\n"
".Lsh_%=:\n"
"s_mov_b32 s72, 0\n"
".Lsi_%=:\n"
"s_sleep 4\n"
"buffer_load_dword v8, v125, s[44:47], s5 offen sc1\n"
"s_waitcnt vmcnt(0)\n"
"v_cmp_ne_u32 vcc, v8, v124\n"
"s_add_u32 s72, s72, 1\n"
"s_nop 3\n"
"s_cbranch_vccz .Lshr_%=\n"
"s_cmp_lt_u32 s72, 10\n"
"s_cbranch_scc1 .Lsi_%=\n"
"s_branch .Lshr_%=\n"
".Lfb_%=:\n"
"s_cmp_lg_u32 s65, 0\n"
"s_cbranch_scc1 .Lfbret_%=\n"
"s_mov_b32 s65, 1\n"
"s_waitcnt vmcnt(0) lgkmcnt(0)\n"
"s_nop 15\n"
"s_barrier\n"
"s_load_dwordx8 s[8:15], %1, 0x0\n"
"s_load_dwordx4 s[16:19], %1, 0x20\n"
"s_load_dwordx2 s[20:21], %1, 0x30\n"
"s_branch .Ltop_%=\n"
".Lmx_%=:\n"
"s_waitcnt lgkmcnt(0)\n"
"v_and_b32 v126, 15, v0\n"
"v_lshrrev_b32 v127, 4, v0\n"
"v_mul_u32_u24 v126, 384, v126\n"
"v_lshl_add_u32 v126, v127, 4, v126\n"
"s_mov_b32 s16, 0\n"
".Lfl_%=:\n"
"buffer_load_dwordx4 v[128:131], v126, s[32:35], s16 offen offset:0\n"
"buffer_load_dwordx4 v[132:135], v126, s[32:35], s16 offen offset:64\n"
"buffer_load_dwordx4 v[136:139], v126, s[32:35], s16 offen offset:128\n"
"buffer_load_dwordx4 v[140:143], v126, s[32:35], s16 offen offset:192\n"
"buffer_load_dwordx4 v[144:147], v126, s[32:35], s16 offen offset:256\n"
"buffer_load_dwordx4 v[148:151], v126, s[32:35], s16 offen offset:320\n"
"s_waitcnt vmcnt(0)\n"
"v_mfma_f32_16x16x32_bf16 v[160:163], v[128:131], a[144:147], 0\n"
"v_mfma_f32_16x16x32_bf16 v[160:163], v[132:135], a[148:151], v[160:163]\n"
"v_mfma_f32_16x16x32_bf16 v[160:163], v[136:139], a[152:155], v[160:163]\n"
"v_mfma_f32_16x16x32_bf16 v[160:163], v[140:143], a[156:159], v[160:163]\n"
"v_mfma_f32_16x16x32_bf16 v[160:163], v[144:147], a[160:163], v[160:163]\n"
"v_mfma_f32_16x16x32_bf16 v[160:163], v[148:151], a[164:167], v[160:163]\n"
"v_mfma_f32_16x16x32_bf16 v[164:167], v[128:131], a[168:171], 0\n"
"v_mfma_f32_16x16x32_bf16 v[164:167], v[132:135], a[172:175], v[164:167]\n"
"v_mfma_f32_16x16x32_bf16 v[164:167], v[136:139], a[176:179], v[164:167]\n"
"v_mfma_f32_16x16x32_bf16 v[164:167], v[140:143], a[180:183], v[164:167]\n"
"v_mfma_f32_16x16x32_bf16 v[164:167], v[144:147], a[184:187], v[164:167]\n"
"v_mfma_f32_16x16x32_bf16 v[164:167], v[148:151], a[188:191], v[164:167]\n"
"v_mfma_f32_16x16x32_bf16 v[168:171], v[128:131], a[192:195], 0\n"
"v_mfma_f32_16x16x32_bf16 v[168:171], v[132:135], a[196:199], v[168:171]\n"
"v_mfma_f32_16x16x32_bf16 v[168:171], v[136:139], a[200:203], v[168:171]\n"
"v_mfma_f32_16x16x32_bf16 v[168:171], v[140:143], a[204:207], v[168:171]\n"
"v_mfma_f32_16x16x32_bf16 v[168:171], v[144:147], a[208:211], v[168:171]\n"
"v_mfma_f32_16x16x32_bf16 v[168:171], v[148:151], a[212:215], v[168:171]\n"
"s_nop 9\n"
"v_max3_f32 v22, v22, v160, v161\n"
"v_max3_f32 v22, v22, v162, v163\n"
"v_max3_f32 v23, v23, v164, v165\n"
"v_max3_f32 v23, v23, v166, v167\n"
"v_max3_f32 v24, v24, v168, v169\n"
"v_max3_f32 v24, v24, v170, v171\n"
"s_add_u32 s16, s16, 6144\n"
"s_cmp_lt_u32 s16, 196608\n"
"s_cbranch_scc1 .Lfl_%=\n"
"s_nop 9\n"
"s_branch .Lg2_%=\n"
".Lend_%=:\n"
    : "+v"(tid)
    : "s"(__builtin_amdgcn_kernarg_segment_ptr()), "s"(blockIdx.x)
    : "v1", "v2", "v3", "v4", "v5", "v6", "v7", "v8", "v9", "v10", "v11", "v12", "v13", "v14", "v15", "v16", "v17", "v18", "v19", "v20", "v21", "v22", "v23", "v24", "v25", "v26", "v27", "v28", "v29", "v30", "v31", "v32", "v33", "v34", "v35", "v36", "v37", "v38", "v39", "v40", "v41", "v42", "v43", "v44", "v45", "v46", "v47", "v48", "v49", "v50", "v51", "v52", "v53", "v54", "v55", "v56", "v57", "v58", "v59", "v60", "v61", "v62", "v63", "v64", "v65", "v66", "v67", "v68", "v69", "v70", "v71", "v72", "v73", "v74", "v75", "v76", "v77", "v78", "v79", "v80", "v81", "v82", "v83", "v84", "v85", "v86", "v87", "v88", "v89", "v90", "v91", "v92", "v93", "v94", "v95", "v96", "v97", "v98", "v99", "v100", "v101", "v102", "v103", "v104", "v105", "v106", "v107", "v108", "v109", "v110", "v111", "v112", "v113", "v114", "v115", "v116", "v117", "v118", "v119", "v120", "v121", "v122", "v123", "v124", "v125", "v126", "v127", "v128", "v129", "v130", "v131", "v132", "v133", "v134", "v135", "v136", "v137", "v138", "v139", "v140", "v141", "v142", "v143", "v144", "v145", "v146", "v147", "v148", "v149", "v150", "v151", "v152", "v153", "v154", "v155", "v156", "v157", "v158", "v159", "v160", "v161", "v162", "v163", "v164", "v165", "v166", "v167", "v168", "v169", "v170", "v171", "v172", "v173", "v174", "v175", "v176", "v177", "v178", "v179", "v180", "v181", "v182", "v183", "v184", "v185", "v186", "v187", "v188", "v189", "v190", "v191", "v192", "v193", "v194", "v195", "v196", "v197", "v198", "v199", "v200", "v201", "v202", "v203", "v204", "v205", "v206", "v207", "v208", "v209", "v210", "v211", "v212", "v213", "v214", "v215", "v216", "v217", "v218", "v219", "v220", "v221", "v222", "v223", "v224", "v225", "v226", "v227", "v228", "v229", "v230", "v231", "v232", "v233", "v234", "v235", "v236", "v237", "v238", "v239", "v240", "v241", "v242", "v243", "v244", "v245", "v246", "v247", "v248", "v249", "v250", "v251", "v252", "v253", "v254", "v255", "a0", "a1", "a2", "a3", "a4", "a5", "a6", "a7", "a8", "a9", "a10", "a11", "a12", "a13", "a14", "a15", "a16", "a17", "a18", "a19", "a20", "a21", "a22", "a23", "a24", "a25", "a26", "a27", "a28", "a29", "a30", "a31", "a32", "a33", "a34", "a35", "a36", "a37", "a38", "a39", "a40", "a41", "a42", "a43", "a44", "a45", "a46", "a47", "a48", "a49", "a50", "a51", "a52", "a53", "a54", "a55", "a56", "a57", "a58", "a59", "a60", "a61", "a62", "a63", "a64", "a65", "a66", "a67", "a68", "a69", "a70", "a71", "a72", "a73", "a74", "a75", "a76", "a77", "a78", "a79", "a80", "a81", "a82", "a83", "a84", "a85", "a86", "a87", "a88", "a89", "a90", "a91", "a92", "a93", "a94", "a95", "a96", "a97", "a98", "a99", "a100", "a101", "a102", "a103", "a104", "a105", "a106", "a107", "a108", "a109", "a110", "a111", "a112", "a113", "a114", "a115", "a116", "a117", "a118", "a119", "a120", "a121", "a122", "a123", "a124", "a125", "a126", "a127", "a128", "a129", "a130", "a131", "a132", "a133", "a134", "a135", "a136", "a137", "a138", "a139", "a140", "a141", "a142", "a143", "a144", "a145", "a146", "a147", "a148", "a149", "a150", "a151", "a152", "a153", "a154", "a155", "a156", "a157", "a158", "a159", "a160", "a161", "a162", "a163", "a164", "a165", "a166", "a167", "a168", "a169", "a170", "a171", "a172", "a173", "a174", "a175", "a176", "a177", "a178", "a179", "a180", "a181", "a182", "a183", "a184", "a185", "a186", "a187", "a188", "a189", "a190", "a191", "a192", "a193", "a194", "a195", "a196", "a197", "a198", "a199", "a200", "a201", "a202", "a203", "a204", "a205", "a206", "a207", "a208", "a209", "a210", "a211", "a212", "a213", "a214", "a215", "a216", "a217", "a218", "a219", "a220", "a221", "a222", "a223", "a224", "a225", "a226", "a227", "a228", "a229", "a230", "a231", "a232", "a233", "a234", "a235", "a236", "a237", "a238", "a239", "a240", "a241", "a242", "a243", "a244", "a245", "a246", "a247", "a248", "a249", "a250", "a251", "a252", "a253", "a254", "a255", "s3", "s4", "s5", "s6", "s7", "s8", "s9", "s10", "s11", "s12", "s13", "s14", "s15", "s16", "s17", "s18", "s19", "s20", "s21", "s22", "s23", "s24", "s25", "s26", "s27", "s28", "s29", "s30", "s31", "s33", "s34", "s35", "s36", "s37", "s38", "s39", "s40", "s41", "s42", "s43", "s44", "s45", "s46", "s47", "s48", "s49", "s50", "s51", "s52", "s53", "s54", "s55", "s56", "s57", "s58", "s59", "s60", "s61", "s62", "s63", "s64", "s65", "s66", "s67", "s68", "s69", "s70", "s71", "s72", "s73", "s74", "s75", "s76", "s77", "s78", "s79", "s80", "s81", "s82", "s83", "s84", "s85", "s86", "s87", "s88", "s89", "s90", "s91", "s92", "s93", "s94", "s95", "s96", "s97", "s98", "s99", "vcc", "scc", "memory");
}

extern "C" void kernel_launch(void* const* d_in, const int* in_sizes, int n_in, void* d_out, int out_size, void* d_ws, size_t ws_size,
                              hipStream_t stream) {
  if (n_in != 5 || ws_size < (size_t)28573696) return;
  (void)hipFuncSetAttribute((const void*)gqa_fused, hipFuncAttributeMaxDynamicSharedMemorySize, 163840);
  hipLaunchKernelGGL(gqa_fused, dim3(256), dim3(256), 163840, stream, (const void*)d_in[0], (const void*)d_in[1], (const void*)d_in[2],
                     (const void*)d_in[3], (const void*)d_in[4], d_out, d_ws);
}
```
